```python
import torch

def _softmax(x, axis=-1):
    return torch.softmax(x, dim=axis)

def _gelu(x):
    return torch.nn.functional.gelu(x, approximate='tanh')

def _silu(x):
    return torch.nn.functional.silu(x)

def _sigmoid(x):
    return torch.sigmoid(x)

def _layernorm(x, axis=-1, eps=1e-05):
    m = x.mean(dim=axis, keepdim=True)
    v = x.var(dim=axis, keepdim=True, unbiased=False)
    return (x - m) / torch.sqrt(v + eps)

def _scatter_add(tgt, idx, src, axis=0):
    return tgt.clone().index_add_(axis, idx, src)

def _segment_sum(src, seg, n):
    out = torch.zeros((n,) + src.shape[1:], dtype=src.dtype)
    return out.index_add_(0, seg, src)

def _pad(x, pw):
    flat = tuple((v for pair in reversed(pw) for v in pair))
    return torch.nn.functional.pad(x, flat)
kernel_name = 'interleave_layers__grouped_kv_mqa__memory_aug_recurrent_mem_tr__prompt'

def setup_inputs():

    _w = lambda a: (lambda t: t.to(torch.bfloat16) if t.is_floating_point() else t)(torch.as_tensor(a))

    def _A_setup():
        B, S, D, H = (1, 2048, 640, 32)
        Dh = D // H
        return {'x': _w((lambda *s: torch.randn(*s, dtype=torch.bfloat16) * 0.05)(B, S, D)), 'wq': _w((lambda *s: torch.randn(*s, dtype=torch.bfloat16) * 0.05)(D, H * Dh)), 'wk': _w((lambda *s: torch.randn(*s, dtype=torch.bfloat16) * 0.05)(D, Dh)), 'wv': _w((lambda *s: torch.randn(*s, dtype=torch.bfloat16) * 0.05)(D, Dh)), 'wo': _w((lambda *s: torch.randn(*s, dtype=torch.bfloat16) * 0.05)(H * Dh, D))}
    _A_in = _A_setup()

    def _B_setup():
        B, S, Dm = (1, 2048, 640)
        H, Dh = (32, 128)
        M, seg = (8, 32)
        seg = min(seg, S)
        S = S // seg * seg
        return {'x': _w((lambda *s: torch.randn(*s, dtype=torch.bfloat16) * 0.05)(B, S, Dm)), 'mem0': _w((lambda *s: torch.randn(*s, dtype=torch.bfloat16) * 0.8)(B, M, Dm)), 'Wq': _w((lambda *s: torch.randn(*s, dtype=torch.bfloat16) * 1.6)(Dm, H, Dh) * Dm ** (-0.5)), 'Wk': _w((lambda *s: torch.randn(*s, dtype=torch.bfloat16) * 1.6)(Dm, H, Dh) * Dm ** (-0.5)), 'Wv': _w((lambda *s: torch.randn(*s, dtype=torch.bfloat16) * 0.8)(Dm, H, Dh) * Dm ** (-0.5)), 'Wo': _w((lambda *s: torch.randn(*s, dtype=torch.bfloat16) * 0.8)(H, Dh, Dm) * (H * Dh) ** (-0.5)), 'seg_len': seg}
    _B_in = _B_setup()
    return {'A_x': _A_in['x'], 'A_wq': _A_in['wq'], 'A_wk': _A_in['wk'], 'A_wv': _A_in['wv'], 'A_wo': _A_in['wo'], 'B_x': _B_in['x'], 'B_mem0': _B_in['mem0'], 'B_Wq': _B_in['Wq'], 'B_Wk': _B_in['Wk'], 'B_Wv': _B_in['Wv'], 'B_Wo': _B_in['Wo'], 'B_seg_len': _B_in['seg_len']}

def reference(A_x, A_wq, A_wk, A_wv, A_wo, B_x, B_mem0, B_Wq, B_Wk, B_Wv, B_Wo, B_seg_len):

    def _A_ref(x, wq, wk, wv, wo):
        B, S, D = x.shape
        H = 32
        Dh = D // H
        q = (lambda x, *d: x.permute(*d))((x @ wq).reshape(B, S, H, Dh), 0, 2, 1, 3)
        k = x @ wk
        v = x @ wv
        scores = torch.einsum('bhqd,bkd->bhqk', q, k) * Dh ** (-0.5)
        p = _softmax(scores, axis=-1)
        o = torch.einsum('bhqk,bkd->bhqd', p, v)
        return (lambda x, *d: x.permute(*d))(o, 0, 2, 1, 3).reshape(B, S, H * Dh) @ wo

    def _B_ref(x, mem0, Wq, Wk, Wv, Wo, seg_len):
        B, S, Dm = x.shape
        H, Dh = (Wq.shape[1], Wq.shape[2])
        M = mem0.shape[1]
        scale = Dh ** (-0.5)
        n_seg = S // seg_len
        L = M + seg_len
        idx = torch.arange(L)
        mask = (idx[:, None] < M) | (idx[:, None] >= idx[None, :])
        mem = mem0
        outs = []
        for s in range(n_seg):
            xs = x[:, s * seg_len:(s + 1) * seg_len, :].clone()
            h = torch.concatenate([mem, xs], axis=1)
            q = torch.einsum('btd,dhe->bhte', h, Wq)
            k = torch.einsum('btd,dhe->bhte', h, Wk)
            v = torch.einsum('btd,dhe->bhte', h, Wv)
            scores = torch.einsum('bhqe,bhke->bhqk', q, k) * scale
            scores = torch.where(mask[None, None], scores, -1000000000.0)
            p = _softmax(scores, axis=-1)
            o = torch.einsum('bhqk,bhke->bhqe', p, v)
            o = torch.einsum('bhte,hed->btd', o, Wo)
            mem = o[:, :M, :].clone()
            outs.append(o[:, M:, :])
        return torch.concatenate(outs, axis=1)
    x = A_x
    x = x + _A_ref(_layernorm(x, axis=-1), A_wq, A_wk, A_wv, A_wo)
    x = x + _B_ref(_layernorm(x, axis=-1), B_mem0, B_Wq, B_Wk, B_Wv, B_Wo, B_seg_len)
    x = x + _A_ref(_layernorm(x, axis=-1), A_wq, A_wk, A_wv, A_wo)
    x = x + _B_ref(_layernorm(x, axis=-1), B_mem0, B_Wq, B_Wk, B_Wv, B_Wo, B_seg_len)
    return x
```

```cpp
#include <hip/hip_runtime.h>
#include <hip/hip_bf16.h>
typedef unsigned short u16;
__device__ __forceinline__ float b2f(u16 v) { return __uint_as_float(((unsigned)v) << 16); }
__device__ __forceinline__ u16 f2b(float f) { __bf16 h = (__bf16)f; return __builtin_bit_cast(u16, h); }

__global__ void k_cvt(const u16* in, float* x, int n) { int i = blockIdx.x * 256 + threadIdx.x; if (i < n) x[i] = b2f(in[i]); }
__global__ void k_out(const float* x, u16* o, int n) { int i = blockIdx.x * 256 + threadIdx.x; if (i < n) o[i] = f2b(x[i]); }
__global__ void k_ln(const float* x, u16* xn) {
  int r = blockIdx.x, l = threadIdx.x;
  float v[10], s = 0;
  for (int i = 0; i < 10; ++i) { v[i] = x[r * 640 + l + 64 * i]; s += v[i]; }
  for (int o = 32; o; o >>= 1) s += __shfl_xor(s, o);
  float m = s / 640, q = 0;
  for (int i = 0; i < 10; ++i) { v[i] -= m; q += v[i] * v[i]; }
  for (int o = 32; o; o >>= 1) q += __shfl_xor(q, o);
  float rs = 1.f / sqrtf(q / 640 + 1e-5f);
  for (int i = 0; i < 10; ++i) xn[r * 640 + l + 64 * i] = f2b(v[i] * rs);
}
__global__ void k_gemm(const u16* A, int lda, const u16* B, int ldb, u16* Cb, float* Cf, int ldc, int M, int N, int K, int mode) {
  __shared__ float As[16][17], Bs[16][17];
  int tx = threadIdx.x & 15, ty = threadIdx.x >> 4;
  int r = blockIdx.y * 16 + ty, c = blockIdx.x * 16 + tx;
  float acc = 0;
  for (int k0 = 0; k0 < K; k0 += 16) {
    As[ty][tx] = (r < M) ? b2f(A[(size_t)r * lda + k0 + tx]) : 0.f;
    Bs[ty][tx] = (c < N) ? b2f(B[(size_t)(k0 + ty) * ldb + c]) : 0.f;
    __syncthreads();
    for (int k = 0; k < 16; ++k) acc += As[ty][k] * Bs[k][tx];
    __syncthreads();
  }
  if (r < M && c < N) { if (mode == 0) Cb[(size_t)r * ldc + c] = f2b(acc); else Cf[(size_t)r * ldc + c] += acc; }
}
__global__ void k_attnA(const u16* q, const u16* k, const u16* v, u16* o) {
  int row = blockIdx.x * 256 + threadIdx.x;
  float qv[20], acc[20];
  for (int d = 0; d < 20; ++d) { qv[d] = b2f(q[row * 20 + d]) * 0.22360679775f; acc[d] = 0; }
  float m = -1e30f, l = 0;
  __shared__ float ks[64][20], vs[64][20];
  for (int j0 = 0; j0 < 2048; j0 += 64) {
    __syncthreads();
    for (int i = threadIdx.x; i < 1280; i += 256) { ks[i / 20][i % 20] = b2f(k[j0 * 20 + i]); vs[i / 20][i % 20] = b2f(v[j0 * 20 + i]); }
    __syncthreads();
    for (int j = 0; j < 64; ++j) {
      float s = 0;
      for (int d = 0; d < 20; ++d) s += qv[d] * ks[j][d];
      float mn = fmaxf(m, s), a = __expf(m - mn), p = __expf(s - mn);
      l = l * a + p;
      for (int d = 0; d < 20; ++d) acc[d] = acc[d] * a + p * vs[j][d];
      m = mn;
    }
  }
  for (int d = 0; d < 20; ++d) o[row * 20 + d] = f2b(acc[d] / l);
}
__global__ void k_attnB(const u16* qt, const u16* kt, const u16* vt, const u16* qm, const u16* km, const u16* vm, u16* om, u16* ot, int s) {
  int h = blockIdx.x, i = blockIdx.y, t = threadIdx.x;
  __shared__ float sc[40];
  const u16* qr = (i < 8) ? qm + i * 4096 + h * 128 : qt + (size_t)(s * 32 + i - 8) * 4096 + h * 128;
  if (t < 40) {
    const u16* kr = (t < 8) ? km + t * 4096 + h * 128 : kt + (size_t)(s * 32 + t - 8) * 4096 + h * 128;
    float a = 0;
    for (int e = 0; e < 128; ++e) a += b2f(qr[e]) * b2f(kr[e]);
    a *= 0.08838834764f;
    if (!(i < 8 || i >= t)) a = -1e9f;
    sc[t] = a;
  }
  __syncthreads();
  float m = -1e30f;
  for (int j = 0; j < 40; ++j) m = fmaxf(m, sc[j]);
  float l = 0, acc = 0;
  for (int j = 0; j < 40; ++j) {
    float p = __expf(sc[j] - m); l += p;
    const u16* vr = (j < 8) ? vm + j * 4096 + h * 128 : vt + (size_t)(s * 32 + j - 8) * 4096 + h * 128;
    acc += p * b2f(vr[t]);
  }
  u16* orow = (i < 8) ? om + i * 4096 + h * 128 : ot + (size_t)(s * 32 + i - 8) * 4096 + h * 128;
  orow[t] = f2b(acc / l);
}
static void gemm(hipStream_t st, const u16* A, int lda, const u16* B, int ldb, u16* Cb, float* Cf, int ldc, int M, int N, int K, int mode) {
  k_gemm<<<dim3((N + 15) / 16, (M + 15) / 16), 256, 0, st>>>(A, lda, B, ldb, Cb, Cf, ldc, M, N, K, mode);
}
extern "C" void kernel_launch(void* const* d_in, const int* in_sizes, int n_in, void* d_out, int out_size, void* d_ws, size_t ws_size, hipStream_t st) {
  const u16 *Ax = (const u16*)d_in[0], *wq = (const u16*)d_in[1], *wk = (const u16*)d_in[2], *wv = (const u16*)d_in[3], *wo = (const u16*)d_in[4];
  const u16 *mem0 = (const u16*)d_in[6], *Wq = (const u16*)d_in[7], *Wk = (const u16*)d_in[8], *Wv = (const u16*)d_in[9], *Wo = (const u16*)d_in[10];
  char* w = (char*)d_ws;
  float* x = (float*)w; w += 2048 * 640 * 4;
  u16* xn = (u16*)w; w += 2048 * 640 * 2;
  u16* qa = (u16*)w; w += 2048 * 640 * 2;
  u16* oa = (u16*)w; w += 2048 * 640 * 2;
  u16* ka = (u16*)w; w += 2048 * 32 * 2;
  u16* va = (u16*)w; w += 2048 * 32 * 2;
  u16* qt = (u16*)w; w += 2048 * 4096 * 2;
  u16* kt = (u16*)w; w += 2048 * 4096 * 2;
  u16* vt = (u16*)w; w += 2048 * 4096 * 2;
  u16* ot = (u16*)w; w += 2048 * 4096 * 2;
  u16* qm = (u16*)w; w += 8 * 4096 * 2;
  u16* km = (u16*)w; w += 8 * 4096 * 2;
  u16* vm = (u16*)w; w += 8 * 4096 * 2;
  u16* om = (u16*)w; w += 8 * 4096 * 2;
  u16* mem = (u16*)w; w += 8 * 640 * 2;
  int n = 2048 * 640;
  k_cvt<<<n / 256, 256, 0, st>>>(Ax, x, n);
  for (int L = 0; L < 2; ++L) {
    k_ln<<<2048, 64, 0, st>>>(x, xn);
    gemm(st, xn, 640, wq, 640, qa, nullptr, 640, 2048, 640, 640, 0);
    gemm(st, xn, 640, wk, 20, ka, nullptr, 20, 2048, 20, 640, 0);
    gemm(st, xn, 640, wv, 20, va, nullptr, 20, 2048, 20, 640, 0);
    k_attnA<<<256, 256, 0, st>>>(qa, ka, va, oa);
    gemm(st, oa, 640, wo, 640, nullptr, x, 640, 2048, 640, 640, 1);
    k_ln<<<2048, 64, 0, st>>>(x, xn);
    gemm(st, xn, 640, Wq, 4096, qt, nullptr, 4096, 2048, 4096, 640, 0);
    gemm(st, xn, 640, Wk, 4096, kt, nullptr, 4096, 2048, 4096, 640, 0);
    gemm(st, xn, 640, Wv, 4096, vt, nullptr, 4096, 2048, 4096, 640, 0);
    for (int s = 0; s < 64; ++s) {
      const u16* m = s == 0 ? mem0 : mem;
      gemm(st, m, 640, Wq, 4096, qm, nullptr, 4096, 8, 4096, 640, 0);
      gemm(st, m, 640, Wk, 4096, km, nullptr, 4096, 8, 4096, 640, 0);
      gemm(st, m, 640, Wv, 4096, vm, nullptr, 4096, 8, 4096, 640, 0);
      k_attnB<<<dim3(32, 40), 128, 0, st>>>(qt, kt, vt, qm, km, vm, om, ot, s);
      gemm(st, om, 4096, Wo, 640, mem, nullptr, 640, 8, 640, 4096, 0);
    }
    gemm(st, ot, 4096, Wo, 640, nullptr, x, 640, 2048, 640, 4096, 1);
  }
  k_out<<<n / 256, 256, 0, st>>>(x, (u16*)d_out, n);
}
```

```cpp
#include <hip/hip_runtime.h>
#include <stdint.h>
typedef unsigned short u16;
typedef unsigned u32;
typedef __bf16 bf2 __attribute__((ext_vector_type(2)));
typedef __bf16 bf8 __attribute__((ext_vector_type(8)));
typedef __bf16 bf16v __attribute__((ext_vector_type(16)));
typedef short s4 __attribute__((ext_vector_type(4)));
typedef float f2 __attribute__((ext_vector_type(2)));
typedef float f4 __attribute__((ext_vector_type(4)));
typedef float f8 __attribute__((ext_vector_type(8)));
typedef float f16v __attribute__((ext_vector_type(16)));
typedef u32 u2 __attribute__((ext_vector_type(2)));
typedef u32 u4 __attribute__((ext_vector_type(4)));
typedef u32 u8v __attribute__((ext_vector_type(8)));
typedef __amdgpu_buffer_rsrc_t rsrc_t;
#define DI __device__ __forceinline__
#define LAS __attribute__((address_space(3)))
#define SB() __builtin_amdgcn_sched_barrier(0)
#define PINV(x) asm volatile("" : "+v"(x))
#define PINA(x) asm volatile("" : "+a"(x))
#define BAR() asm volatile("s_barrier" ::: "memory")
#define WAIT_LGKM(n) asm volatile("s_waitcnt lgkmcnt(" #n ")" ::: "memory")
#define WAIT_VM0() asm volatile("s_waitcnt vmcnt(0)" ::: "memory")
constexpr u32 OOB = 0x7FFF0000u;
constexpr u32 SENT = 0xFFBFFFBFu;
DI float b2f(u16 v) { return __uint_as_float(((u32)v) << 16); }
DI u32 pk(float a, float b) { bf2 v = {(__bf16)a, (__bf16)b}; return __builtin_bit_cast(u32, v); }
DI rsrc_t mkrs(const void* p, u32 bytes) { return __builtin_amdgcn_make_buffer_rsrc((void*)p, 0, bytes, 0x00020000); }
DI u4 ld128(rsrc_t r, u32 off) { return __builtin_amdgcn_raw_buffer_load_b128(r, off, 0, 16); }
DI u2 ld64(rsrc_t r, u32 off) { return __builtin_amdgcn_raw_buffer_load_b64(r, off, 0, 16); }
DI u32 ld32(rsrc_t r, u32 off) { return __builtin_amdgcn_raw_buffer_load_b32(r, off, 0, 16); }
DI void st128(u4 v, rsrc_t r, u32 off) { __builtin_amdgcn_raw_buffer_store_b128(v, r, off, 0, 16); }
DI void st64(u2 v, rsrc_t r, u32 off) { __builtin_amdgcn_raw_buffer_store_b64(v, r, off, 0, 16); }
DI void st32(u32 v, rsrc_t r, u32 off) { __builtin_amdgcn_raw_buffer_store_b32(v, r, off, 0, 16); }
DI u32 wld32(rsrc_t r, u32 voff, u32 soff) { return __builtin_amdgcn_raw_buffer_load_b32(r, voff, soff, 0); }
DI u32 wld16(rsrc_t r, u32 voff, u32 soff) { return (u32)__builtin_amdgcn_raw_buffer_load_b16(r, voff, soff, 0); }
template <class T> DI T lds_ld(u32 a) { return *(const LAS T*)(uintptr_t)a; }
template <class T> DI void lds_st(u32 a, T v) { *(LAS T*)(uintptr_t)a = v; }
DI f4 smf16(u4 a, u8v b, f4 c, int idx) { return __builtin_amdgcn_smfmac_f32_16x16x64_bf16(__builtin_bit_cast(bf8, a), __builtin_bit_cast(bf16v, b), c, idx, 0, 0); }
DI f4 mf16x32(u4 a, u4 b, f4 c) { return __builtin_amdgcn_mfma_f32_16x16x32_bf16(__builtin_bit_cast(bf8, a), __builtin_bit_cast(bf8, b), c, 0, 0, 0); }
DI f4 mf16x16(u2 a, u2 b, f4 c) { return __builtin_amdgcn_mfma_f32_16x16x16bf16_1k(__builtin_bit_cast(s4, a), __builtin_bit_cast(s4, b), c, 0, 0, 0); }
DI u8v cat(u4 a, u4 b) { return __builtin_shufflevector(a, b, 0, 1, 2, 3, 4, 5, 6, 7); }
DI u32 mx4(u4 v) { return max(max(v.x, v.y), max(v.z, v.w)); }
DI bool anyb(bool c) { return __builtin_amdgcn_ballot_w64(c) != 0; }
DI float ex2(float x) { return __builtin_amdgcn_exp2f(x); }
DI float xor32max(float v) { auto r = __builtin_amdgcn_permlane32_swap(__float_as_uint(v), __float_as_uint(v), false, false); return fmaxf(__uint_as_float(r[0]), __uint_as_float(r[1])); }
DI float xor16max(float v) { auto r = __builtin_amdgcn_permlane16_swap(__float_as_uint(v), __float_as_uint(v), false, false); return fmaxf(__uint_as_float(r[0]), __uint_as_float(r[1])); }
DI u32 ror8(u32 v) { return (u32)__builtin_amdgcn_update_dpp(0, (int)v, 0x128, 0xf, 0xf, true); }
DI u32 rt() { unsigned long long t; asm volatile("s_memrealtime %0\n\ts_waitcnt lgkmcnt(0)" : "=s"(t)::"memory"); return (u32)t; }
DI unsigned long long ct() { unsigned long long t; asm volatile("s_memtime %0\n\ts_waitcnt lgkmcnt(0)" : "=s"(t)::"memory"); return t; }
DI u32 ft() {
  unsigned long long a, b, c, d, e, f;
  asm volatile("s_memrealtime %0\n\ts_nop 2\n\ts_memrealtime %1\n\ts_nop 2\n\ts_memrealtime %2\n\ts_nop 2\n\ts_memrealtime %3\n\ts_nop 2\n\ts_memrealtime %4\n\ts_nop 2\n\ts_memrealtime %5\n\ts_waitcnt lgkmcnt(0)"
               : "=&s"(a), "=&s"(b), "=&s"(c), "=&s"(d), "=&s"(e), "=&s"(f)::"memory");
  return ((u32)a + (u32)b + (u32)c + (u32)d + (u32)e + (u32)f) * 4u;
}
DI void cdelay(int rem) {
  asm volatile(
      "s_cmp_gt_u32 %0, 0x1ff\n\ts_cbranch_scc1 9f\n\t"
      "s_bitcmp1_b32 %0, 8\n\ts_cbranch_scc0 1f\n\ts_nop 15\n\ts_nop 15\n\ts_nop 15\n\ts_nop 15\n\ts_nop 3\n\t"
      "1:\n\ts_bitcmp1_b32 %0, 7\n\ts_cbranch_scc0 2f\n\ts_nop 15\n\ts_nop 15\n\ts_nop 3\n\t"
      "2:\n\ts_bitcmp1_b32 %0, 6\n\ts_cbranch_scc0 3f\n\ts_nop 15\n\ts_nop 3\n\t"
      "3:\n\ts_bitcmp1_b32 %0, 5\n\ts_cbranch_scc0 4f\n\ts_nop 11\n\t"
      "4:\n\ts_bitcmp1_b32 %0, 4\n\ts_cbranch_scc0 5f\n\ts_nop 7\n\t"
      "5:\n\ts_bitcmp1_b32 %0, 3\n\ts_cbranch_scc0 9f\n\ts_nop 5\n\t"
      "9:" ::"s"(rem) : "scc", "memory");
}
DI void wait_until(u32 G) {
#pragma nounroll
  for (;;) { int rem = (int)(G - rt() * 24u); if (rem < 760 || rem > 0x8000) break; }
  cdelay((int)(G - ft()) - 380);
}

constexpr u32 R_SLOT = 24 * 8192, R_M = 0, R_MCOPY = 12288, R_O = 6 * 8192, R_OCOPY = 65536;
constexpr u32 R_MEM0 = 4 * R_SLOT, R_FLG = R_MEM0 + 6 * 8192, R_END = R_FLG + 65536;
constexpr u32 F_QKV = 0, F_TY = 16384, F_OT = 3 * 16384, F_XB = 4 * 16384, F_OA = F_XB + 2048, F_QA = 5 * 16384, F_X2 = F_QA + 1024, F_KV = F_QA + 6144, F_SIZE = 6 * 16384;
constexpr u32 R_PRG = 2 * R_END, R_CTL = R_PRG + F_SIZE, F_DBG = 0x100000, F_LIM = 0x300000;
DI u32 rmap(u32 off, u32 pol) { return ((off >> 13) << 14) | (pol << 13) | (off & 8191); }
constexpr u32 SPIN_CAP = 1u << 18;
constexpr float SC_B = 0.08838834764831845f * 1.4426950408889634f;
struct Tm { int A, P, g, pad; };
constexpr u32 XL_M = 0, XL_Q = 12288, XL_Z = XL_Q + 2048, XL_K = 14592, XL_V = 16640, XL_S = 17664, XL_F = 21760, XL_T = 22528, XL_TS = 20480;

DI void dma16(u4 rs, u32 voff, u32 soff, u32 ldsdst) {
  asm volatile("s_mov_b32 m0, %3\n\ts_nop 0\n\tbuffer_load_dwordx4 %0, %1, %2 offen sc1 lds" ::"v"(voff), "s"(rs), "s"(soff), "s"(ldsdst) : "memory");
}
DI u4 mkdesc(const void* p, u32 bytes) { uint64_t a = (uint64_t)p; u4 d = {(u32)a, (u32)(a >> 32) & 0xffffu, bytes, 0x00020000u}; return d; }

#define X_ASM0 \
  "v_mbcnt_lo_u32_b32 v0, -1, 0\n" \
  "v_mbcnt_hi_u32_b32 v0, -1, v0\n" \
  "v_lshlrev_b32 v0, 2, v0\n" \
  "v_add_u32 v0, %0, v0\n" \
  "v_add_u32 v0, 146432, v0\n" \
  "ds_read_b32 v0, v0\n" \
  "s_waitcnt lgkmcnt(0)\n" \
  "v_readlane_b32 s0, v0, 0\n" \
  "v_readlane_b32 s1, v0, 1\n" \
  "v_readlane_b32 s2, v0, 2\n" \
  "v_readlane_b32 s3, v0, 3\n" \
  "v_readlane_b32 s4, v0, 4\n" \
  "v_readlane_b32 s5, v0, 5\n" \
  "v_readlane_b32 s6, v0, 6\n" \
  "v_readlane_b32 s7, v0, 7\n" \
  "v_readlane_b32 s8, v0, 8\n" \
  "v_readlane_b32 s9, v0, 9\n" \
  "v_readlane_b32 s10, v0, 10\n" \
  "v_readlane_b32 s11, v0, 11\n" \
  "v_readlane_b32 s12, v0, 12\n" \
  "v_readlane_b32 s13, v0, 13\n" \
  "v_readlane_b32 s14, v0, 14\n" \
  "v_readlane_b32 s15, v0, 15\n" \
  "v_readlane_b32 s16, v0, 16\n" \
  "v_readlane_b32 s17, v0, 17\n" \
  "v_readlane_b32 s18, v0, 18\n" \
  "v_readlane_b32 s19, v0, 19\n" \
  "v_readlane_b32 s20, v0, 20\n" \
  "v_readlane_b32 s21, v0, 21\n" \
  "v_readlane_b32 s22, v0, 22\n" \
  "v_readlane_b32 s23, v0, 23\n" \
  "v_readlane_b32 s24, v0, 24\n" \
  "v_readlane_b32 s25, v0, 25\n" \
  "v_readlane_b32 s26, v0, 26\n" \
  "v_readlane_b32 s27, v0, 27\n" \
  "v_readlane_b32 s28, v0, 28\n" \
  "v_readlane_b32 s29, v0, 29\n" \
  "v_readlane_b32 s30, v0, 30\n" \
  "v_readlane_b32 s31, v0, 31\n" \
  "v_readlane_b32 s32, v0, 32\n" \
  "v_readlane_b32 s33, v0, 33\n" \
  "v_readlane_b32 s34, v0, 34\n" \
  "v_readlane_b32 s35, v0, 35\n" \
  "v_readlane_b32 s36, v0, 36\n" \
  "v_readlane_b32 s37, v0, 37\n" \
  "v_readlane_b32 s38, v0, 38\n" \
  "v_readlane_b32 s39, v0, 39\n" \
  "v_readlane_b32 s40, v0, 40\n" \
  "v_readlane_b32 s41, v0, 41\n" \
  "v_readlane_b32 s42, v0, 42\n" \
  "v_readlane_b32 s43, v0, 43\n" \
  "v_readlane_b32 s44, v0, 44\n" \
  "s_nop 4\n" \
  "v_mbcnt_lo_u32_b32 v0, -1, 0\n" \
  "v_mbcnt_hi_u32_b32 v0, -1, v0\n" \
  "v_lshlrev_b32 v0, 2, v0\n" \
  "v_add_u32 v0, s28, v0\n" \
  "ds_read_b32 v92, v0 offset:22528\n" \
  "ds_read_b32 v93, v0 offset:23552\n" \
  "ds_read_b32 v94, v0 offset:24576\n" \
  "ds_read_b32 v95, v0 offset:25600\n" \
  "ds_read_b32 v96, v0 offset:26624\n" \
  "ds_read_b32 v97, v0 offset:27648\n" \
  "ds_read_b32 v98, v0 offset:28672\n" \
  "ds_read_b32 v99, v0 offset:29696\n" \
  "ds_read_b32 v100, v0 offset:30720\n" \
  "ds_read_b32 v101, v0 offset:31744\n" \
  "ds_read_b32 v102, v0 offset:32768\n" \
  "ds_read_b32 v103, v0 offset:33792\n" \
  "ds_read_b32 v104, v0 offset:34816\n" \
  "ds_read_b32 v105, v0 offset:35840\n" \
  "ds_read_b32 v106, v0 offset:36864\n" \
  "ds_read_b32 v107, v0 offset:37888\n" \
  "ds_read_b32 v84, v0 offset:38912\n" \
  "ds_read_b32 v85, v0 offset:39936\n" \
  "ds_read_b32 v86, v0 offset:40960\n" \
  "ds_read_b32 v87, v0 offset:41984\n" \
  "ds_read_b32 v88, v0 offset:43008\n" \
  "s_waitcnt lgkmcnt(0)\n" \
  "v_cmp_le_i32 s[84:85], 0, v88\n" \
  "v_cmp_le_i32 s[92:93], 16, v88\n" \
  "v_cmp_le_i32 s[86:87], 1, v88\n" \
  "v_cmp_le_i32 s[94:95], 17, v88\n" \
  "v_cmp_le_i32 s[88:89], 2, v88\n" \
  "v_cmp_le_i32 s[96:97], 18, v88\n" \
  "v_cmp_le_i32 s[90:91], 3, v88\n" \
  "v_cmp_le_i32 s[98:99], 19, v88\n" \
  "v_mov_b32 v108, 0x3f803f80\n" \
  "v_mov_b32 v109, 0x3f803f80\n" \
  "v_mov_b32 v110, 0x3f803f80\n" \
  "v_mov_b32 v111, 0x3f803f80\n" \
  "s_mov_b32 s68, 0x05040100\n" \
  "s_mov_b32 s69, 0x07060302\n" \
  "s_mov_b32 s65, 0\n" \
  "buffer_load_dword v0, v84, s[12:15], s65 offen\n" \
  "buffer_load_dword v2, v84, s[16:19], s65 offen\n" \
  "buffer_load_ushort v4, v86, s[20:23], s65 offen\n" \
  "s_mov_b32 s65, 8192\n" \
  "buffer_load_dword v1, v84, s[12:15], s65 offen\n" \
  "buffer_load_dword v3, v84, s[16:19], s65 offen\n" \
  "buffer_load_ushort v5, v86, s[20:23], s65 offen\n" \
  "s_mov_b32 s65, 196608\n" \
  "buffer_load_dword v6, v84, s[12:15], s65 offen\n" \
  "buffer_load_dword v8, v84, s[16:19], s65 offen\n" \
  "buffer_load_ushort v10, v86, s[20:23], s65 offen\n" \
  "s_mov_b32 s65, 204800\n" \
  "buffer_load_dword v7, v84, s[12:15], s65 offen\n" \
  "buffer_load_dword v9, v84, s[16:19], s65 offen\n" \
  "buffer_load_ushort v11, v86, s[20:23], s65 offen\n" \
  "s_mov_b32 s65, 16384\n" \
  "buffer_load_dword v12, v84, s[12:15], s65 offen\n" \
  "buffer_load_dword v14, v84, s[16:19], s65 offen\n" \
  "buffer_load_ushort v16, v86, s[20:23], s65 offen\n" \
  "s_mov_b32 s65, 24576\n" \
  "buffer_load_dword v13, v84, s[12:15], s65 offen\n" \
  "buffer_load_dword v15, v84, s[16:19], s65 offen\n" \
  "buffer_load_ushort v17, v86, s[20:23], s65 offen\n" \
  "s_mov_b32 s65, 212992\n" \
  "buffer_load_dword v18, v84, s[12:15], s65 offen\n" \
  "buffer_load_dword v20, v84, s[16:19], s65 offen\n" \
  "buffer_load_ushort v22, v86, s[20:23], s65 offen\n" \
  "s_mov_b32 s65, 221184\n" \
  "buffer_load_dword v19, v84, s[12:15], s65 offen\n" \
  "buffer_load_dword v21, v84, s[16:19], s65 offen\n" \
  "buffer_load_ushort v23, v86, s[20:23], s65 offen\n" \
  "s_mov_b32 s65, 786432\n" \
  "buffer_load_dword v24, v84, s[12:15], s65 offen\n" \
  "buffer_load_dword v26, v84, s[16:19], s65 offen\n" \
  "buffer_load_ushort v28, v86, s[20:23], s65 offen\n" \
  "s_mov_b32 s65, 794624\n" \
  "buffer_load_dword v25, v84, s[12:15], s65 offen\n" \
  "buffer_load_dword v27, v84, s[16:19], s65 offen\n" \
  "buffer_load_ushort v29, v86, s[20:23], s65 offen\n" \
  "s_mov_b32 s65, 983040\n" \
  "buffer_load_dword v30, v84, s[12:15], s65 offen\n" \
  "buffer_load_dword v32, v84, s[16:19], s65 offen\n" \
  "buffer_load_ushort v34, v86, s[20:23], s65 offen\n" \
  "s_mov_b32 s65, 991232\n" \
  "buffer_load_dword v31, v84, s[12:15], s65 offen\n" \
  "buffer_load_dword v33, v84, s[16:19], s65 offen\n" \
  "buffer_load_ushort v35, v86, s[20:23], s65 offen\n" \
  "s_mov_b32 s65, 802816\n" \
  "buffer_load_dword v36, v84, s[12:15], s65 offen\n" \
  "buffer_load_dword v38, v84, s[16:19], s65 offen\n" \
  "buffer_load_ushort v40, v86, s[20:23], s65 offen\n" \
  "s_mov_b32 s65, 811008\n" \
  "buffer_load_dword v37, v84, s[12:15], s65 offen\n" \
  "buffer_load_dword v39, v84, s[16:19], s65 offen\n" \
  "buffer_load_ushort v41, v86, s[20:23], s65 offen\n" \
  "s_mov_b32 s65, 999424\n" \
  "buffer_load_dword v42, v84, s[12:15], s65 offen\n" \
  "buffer_load_dword v44, v84, s[16:19], s65 offen\n" \
  "buffer_load_ushort v46, v86, s[20:23], s65 offen\n" \
  "s_mov_b32 s65, 1007616\n" \
  "buffer_load_dword v43, v84, s[12:15], s65 offen\n" \
  "buffer_load_dword v45, v84, s[16:19], s65 offen\n" \
  "buffer_load_ushort v47, v86, s[20:23], s65 offen\n" \
  "s_waitcnt vmcnt(24)\n" \
  "s_mov_b32 s65, 65536\n" \
  "buffer_load_dword v48, v84, s[12:15], s65 offen\n" \
  "buffer_load_dword v50, v84, s[16:19], s65 offen\n" \
  "buffer_load_ushort v52, v86, s[20:23], s65 offen\n" \
  "s_mov_b32 s65, 73728\n" \
  "buffer_load_dword v49, v84, s[12:15], s65 offen\n" \
  "buffer_load_dword v51, v84, s[16:19], s65 offen\n" \
  "buffer_load_ushort v53, v86, s[20:23], s65 offen\n" \
  "s_mov_b32 s65, 262144\n" \
  "buffer_load_dword v54, v84, s[12:15], s65 offen\n" \
  "buffer_load_dword v56, v84, s[16:19], s65 offen\n" \
  "buffer_load_ushort v58, v86, s[20:23], s65 offen\n" \
  "s_mov_b32 s65, 270336\n" \
  "buffer_load_dword v55, v84, s[12:15], s65 offen\n" \
  "buffer_load_dword v57, v84, s[16:19], s65 offen\n" \
  "buffer_load_ushort v59, v86, s[20:23], s65 offen\n" \
  "s_mov_b32 s65, 81920\n" \
  "buffer_load_dword v60, v84, s[12:15], s65 offen\n" \
  "buffer_load_dword v62, v84, s[16:19], s65 offen\n" \
  "buffer_load_ushort v64, v86, s[20:23], s65 offen\n" \
  "s_mov_b32 s65, 90112\n" \
  "buffer_load_dword v61, v84, s[12:15], s65 offen\n" \
  "buffer_load_dword v63, v84, s[16:19], s65 offen\n" \
  "buffer_load_ushort v65, v86, s[20:23], s65 offen\n" \
  "s_mov_b32 s65, 278528\n" \
  "buffer_load_dword v66, v84, s[12:15], s65 offen\n" \
  "buffer_load_dword v68, v84, s[16:19], s65 offen\n" \
  "buffer_load_ushort v70, v86, s[20:23], s65 offen\n" \
  "s_mov_b32 s65, 286720\n" \
  "buffer_load_dword v67, v84, s[12:15], s65 offen\n" \
  "buffer_load_dword v69, v84, s[16:19], s65 offen\n" \
  "buffer_load_ushort v71, v86, s[20:23], s65 offen\n" \
  "v_perm_b32 v72, v1, v0, s68\n" \
  "v_accvgpr_write_b32 a0, v72\n" \
  "v_perm_b32 v72, v1, v0, s69\n" \
  "v_accvgpr_write_b32 a8, v72\n" \
  "v_perm_b32 v72, v3, v2, s68\n" \
  "v_accvgpr_write_b32 a160, v72\n" \
  "v_perm_b32 v72, v3, v2, s69\n" \
  "v_accvgpr_write_b32 a240, v72\n" \
  "v_perm_b32 v176, v5, v4, s68\n" \
  "v_perm_b32 v72, v7, v6, s68\n" \
  "v_accvgpr_write_b32 a1, v72\n" \
  "v_perm_b32 v72, v7, v6, s69\n" \
  "v_accvgpr_write_b32 a9, v72\n" \
  "v_perm_b32 v72, v9, v8, s68\n" \
  "v_accvgpr_write_b32 a161, v72\n" \
  "v_perm_b32 v72, v9, v8, s69\n" \
  "v_accvgpr_write_b32 a241, v72\n" \
  "v_perm_b32 v177, v11, v10, s68\n" \
  "v_perm_b32 v72, v13, v12, s68\n" \
  "v_accvgpr_write_b32 a2, v72\n" \
  "v_perm_b32 v72, v13, v12, s69\n" \
  "v_accvgpr_write_b32 a10, v72\n" \
  "v_perm_b32 v72, v15, v14, s68\n" \
  "v_accvgpr_write_b32 a162, v72\n" \
  "v_perm_b32 v72, v15, v14, s69\n" \
  "v_accvgpr_write_b32 a242, v72\n" \
  "v_perm_b32 v178, v17, v16, s68\n" \
  "v_perm_b32 v72, v19, v18, s68\n" \
  "v_accvgpr_write_b32 a3, v72\n" \
  "v_perm_b32 v72, v19, v18, s69\n" \
  "v_accvgpr_write_b32 a11, v72\n" \
  "v_perm_b32 v72, v21, v20, s68\n" \
  "v_accvgpr_write_b32 a163, v72\n" \
  "v_perm_b32 v72, v21, v20, s69\n" \
  "v_accvgpr_write_b32 a243, v72\n" \
  "v_perm_b32 v179, v23, v22, s68\n" \
  "s_waitcnt vmcnt(24)\n" \
  "s_mov_b32 s65, 851968\n" \
  "buffer_load_dword v0, v84, s[12:15], s65 offen\n" \
  "buffer_load_dword v2, v84, s[16:19], s65 offen\n" \
  "buffer_load_ushort v4, v86, s[20:23], s65 offen\n" \
  "s_mov_b32 s65, 860160\n" \
  "buffer_load_dword v1, v84, s[12:15], s65 offen\n" \
  "buffer_load_dword v3, v84, s[16:19], s65 offen\n" \
  "buffer_load_ushort v5, v86, s[20:23], s65 offen\n" \
  "s_mov_b32 s65, 1048576\n" \
  "buffer_load_dword v6, v84, s[12:15], s65 offen\n" \
  "buffer_load_dword v8, v84, s[16:19], s65 offen\n" \
  "buffer_load_ushort v10, v86, s[20:23], s65 offen\n" \
  "s_mov_b32 s65, 1056768\n" \
  "buffer_load_dword v7, v84, s[12:15], s65 offen\n" \
  "buffer_load_dword v9, v84, s[16:19], s65 offen\n" \
  "buffer_load_ushort v11, v86, s[20:23], s65 offen\n" \
  "s_mov_b32 s65, 868352\n" \
  "buffer_load_dword v12, v84, s[12:15], s65 offen\n" \
  "buffer_load_dword v14, v84, s[16:19], s65 offen\n" \
  "buffer_load_ushort v16, v86, s[20:23], s65 offen\n" \
  "s_mov_b32 s65, 876544\n" \
  "buffer_load_dword v13, v84, s[12:15], s65 offen\n" \
  "buffer_load_dword v15, v84, s[16:19], s65 offen\n" \
  "buffer_load_ushort v17, v86, s[20:23], s65 offen\n" \
  "s_mov_b32 s65, 1064960\n" \
  "buffer_load_dword v18, v84, s[12:15], s65 offen\n" \
  "buffer_load_dword v20, v84, s[16:19], s65 offen\n" \
  "buffer_load_ushort v22, v86, s[20:23], s65 offen\n" \
  "s_mov_b32 s65, 1073152\n" \
  "buffer_load_dword v19, v84, s[12:15], s65 offen\n" \
  "buffer_load_dword v21, v84, s[16:19], s65 offen\n" \
  "buffer_load_ushort v23, v86, s[20:23], s65 offen\n" \
  "v_perm_b32 v72, v25, v24, s68\n" \
  "v_accvgpr_write_b32 a4, v72\n" \
  "v_perm_b32 v72, v25, v24, s69\n" \
  "v_accvgpr_write_b32 a12, v72\n" \
  "v_perm_b32 v72, v27, v26, s68\n" \
  "v_accvgpr_write_b32 a164, v72\n" \
  "v_perm_b32 v72, v27, v26, s69\n" \
  "v_accvgpr_write_b32 a244, v72\n" \
  "v_perm_b32 v180, v29, v28, s68\n" \
  "v_perm_b32 v72, v31, v30, s68\n" \
  "v_accvgpr_write_b32 a5, v72\n" \
  "v_perm_b32 v72, v31, v30, s69\n" \
  "v_accvgpr_write_b32 a13, v72\n" \
  "v_perm_b32 v72, v33, v32, s68\n" \
  "v_accvgpr_write_b32 a165, v72\n" \
  "v_perm_b32 v72, v33, v32, s69\n" \
  "v_accvgpr_write_b32 a245, v72\n" \
  "v_perm_b32 v181, v35, v34, s68\n" \
  "v_perm_b32 v72, v37, v36, s68\n" \
  "v_accvgpr_write_b32 a6, v72\n" \
  "v_perm_b32 v72, v37, v36, s69\n" \
  "v_accvgpr_write_b32 a14, v72\n" \
  "v_perm_b32 v72, v39, v38, s68\n" \
  "v_accvgpr_write_b32 a166, v72\n" \
  "v_perm_b32 v72, v39, v38, s69\n" \
  "v_accvgpr_write_b32 a246, v72\n" \
  "v_perm_b32 v182, v41, v40, s68\n" \
  "v_perm_b32 v72, v43, v42, s68\n" \
  "v_accvgpr_write_b32 a7, v72\n" \
  "v_perm_b32 v72, v43, v42, s69\n" \
  "v_accvgpr_write_b32 a15, v72\n" \
  "v_perm_b32 v72, v45, v44, s68\n" \
  "v_accvgpr_write_b32 a167, v72\n" \
  "v_perm_b32 v72, v45, v44, s69\n" \
  "v_accvgpr_write_b32 a247, v72\n" \
  "v_perm_b32 v183, v47, v46, s68\n" \
  "s_waitcnt vmcnt(24)\n" \
  "s_mov_b32 s65, 131072\n" \
  "buffer_load_dword v24, v84, s[12:15], s65 offen\n" \
  "buffer_load_dword v26, v84, s[16:19], s65 offen\n" \
  "buffer_load_ushort v28, v86, s[20:23], s65 offen\n" \
  "s_mov_b32 s65, 139264\n" \
  "buffer_load_dword v25, v84, s[12:15], s65 offen\n" \
  "buffer_load_dword v27, v84, s[16:19], s65 offen\n" \
  "buffer_load_ushort v29, v86, s[20:23], s65 offen\n" \
  "s_mov_b32 s65, 327680\n" \
  "buffer_load_dword v30, v84, s[12:15], s65 offen\n" \
  "buffer_load_dword v32, v84, s[16:19], s65 offen\n" \
  "buffer_load_ushort v34, v86, s[20:23], s65 offen\n" \
  "s_mov_b32 s65, 335872\n" \
  "buffer_load_dword v31, v84, s[12:15], s65 offen\n" \
  "buffer_load_dword v33, v84, s[16:19], s65 offen\n" \
  "buffer_load_ushort v35, v86, s[20:23], s65 offen\n" \
  "s_mov_b32 s65, 147456\n" \
  "buffer_load_dword v36, v84, s[12:15], s65 offen\n" \
  "buffer_load_dword v38, v84, s[16:19], s65 offen\n" \
  "buffer_load_ushort v40, v86, s[20:23], s65 offen\n" \
  "s_mov_b32 s65, 155648\n" \
  "buffer_load_dword v37, v84, s[12:15], s65 offen\n" \
  "buffer_load_dword v39, v84, s[16:19], s65 offen\n" \
  "buffer_load_ushort v41, v86, s[20:23], s65 offen\n" \
  "s_mov_b32 s65, 344064\n" \
  "buffer_load_dword v42, v84, s[12:15], s65 offen\n" \
  "buffer_load_dword v44, v84, s[16:19], s65 offen\n" \
  "buffer_load_ushort v46, v86, s[20:23], s65 offen\n" \
  "s_mov_b32 s65, 352256\n" \
  "buffer_load_dword v43, v84, s[12:15], s65 offen\n" \
  "buffer_load_dword v45, v84, s[16:19], s65 offen\n" \
  "buffer_load_ushort v47, v86, s[20:23], s65 offen\n" \
  "v_perm_b32 v72, v49, v48, s68\n" \
  "v_accvgpr_write_b32 a16, v72\n" \
  "v_perm_b32 v72, v49, v48, s69\n" \
  "v_accvgpr_write_b32 a24, v72\n" \
  "v_perm_b32 v72, v51, v50, s68\n" \
  "v_accvgpr_write_b32 a168, v72\n" \
  "v_perm_b32 v72, v51, v50, s69\n" \
  "v_accvgpr_write_b32 a248, v72\n" \
  "v_perm_b32 v184, v53, v52, s68\n" \
  "v_perm_b32 v72, v55, v54, s68\n" \
  "v_accvgpr_write_b32 a17, v72\n" \
  "v_perm_b32 v72, v55, v54, s69\n" \
  "v_accvgpr_write_b32 a25, v72\n" \
  "v_perm_b32 v72, v57, v56, s68\n" \
  "v_accvgpr_write_b32 a169, v72\n" \
  "v_perm_b32 v72, v57, v56, s69\n" \
  "v_accvgpr_write_b32 a249, v72\n" \
  "v_perm_b32 v185, v59, v58, s68\n" \
  "v_perm_b32 v72, v61, v60, s68\n" \
  "v_accvgpr_write_b32 a18, v72\n" \
  "v_perm_b32 v72, v61, v60, s69\n" \
  "v_accvgpr_write_b32 a26, v72\n" \
  "v_perm_b32 v72, v63, v62, s68\n" \
  "v_accvgpr_write_b32 a170, v72\n" \
  "v_perm_b32 v72, v63, v62, s69\n" \
  "v_accvgpr_write_b32 a250, v72\n" \
  "v_perm_b32 v186, v65, v64, s68\n" \
  "v_perm_b32 v72, v67, v66, s68\n" \
  "v_accvgpr_write_b32 a19, v72\n" \
  "v_perm_b32 v72, v67, v66, s69\n" \
  "v_accvgpr_write_b32 a27, v72\n" \
  "v_perm_b32 v72, v69, v68, s68\n" \
  "v_accvgpr_write_b32 a171, v72\n" \
  "v_perm_b32 v72, v69, v68, s69\n" \
  "v_accvgpr_write_b32 a251, v72\n" \
  "v_perm_b32 v187, v71, v70, s68\n" \
  "s_waitcnt vmcnt(24)\n" \
  "s_mov_b32 s65, 917504\n" \
  "buffer_load_dword v48, v84, s[12:15], s65 offen\n" \
  "buffer_load_dword v50, v84, s[16:19], s65 offen\n" \
  "buffer_load_ushort v52, v86, s[20:23], s65 offen\n" \
  "s_mov_b32 s65, 925696\n" \
  "buffer_load_dword v49, v84, s[12:15], s65 offen\n" \
  "buffer_load_dword v51, v84, s[16:19], s65 offen\n" \
  "buffer_load_ushort v53, v86, s[20:23], s65 offen\n" \
  "s_mov_b32 s65, 1114112\n" \
  "buffer_load_dword v54, v84, s[12:15], s65 offen\n" \
  "buffer_load_dword v56, v84, s[16:19], s65 offen\n" \
  "buffer_load_ushort v58, v86, s[20:23], s65 offen\n" \
  "s_mov_b32 s65, 1122304\n" \
  "buffer_load_dword v55, v84, s[12:15], s65 offen\n" \
  "buffer_load_dword v57, v84, s[16:19], s65 offen\n" \
  "buffer_load_ushort v59, v86, s[20:23], s65 offen\n" \
  "s_mov_b32 s65, 933888\n" \
  "buffer_load_dword v60, v84, s[12:15], s65 offen\n" \
  "buffer_load_dword v62, v84, s[16:19], s65 offen\n" \
  "buffer_load_ushort v64, v86, s[20:23], s65 offen\n" \
  "s_mov_b32 s65, 942080\n" \
  "buffer_load_dword v61, v84, s[12:15], s65 offen\n" \
  "buffer_load_dword v63, v84, s[16:19], s65 offen\n" \
  "buffer_load_ushort v65, v86, s[20:23], s65 offen\n" \
  "s_mov_b32 s65, 1130496\n" \
  "buffer_load_dword v66, v84, s[12:15], s65 offen\n" \
  "buffer_load_dword v68, v84, s[16:19], s65 offen\n" \
  "buffer_load_ushort v70, v86, s[20:23], s65 offen\n" \
  "s_mov_b32 s65, 1138688\n" \
  "buffer_load_dword v67, v84, s[12:15], s65 offen\n" \
  "buffer_load_dword v69, v84, s[16:19], s65 offen\n" \
  "buffer_load_ushort v71, v86, s[20:23], s65 offen\n" \
  "v_perm_b32 v72, v1, v0, s68\n" \
  "v_accvgpr_write_b32 a20, v72\n" \
  "v_perm_b32 v72, v1, v0, s69\n" \
  "v_accvgpr_write_b32 a28, v72\n" \
  "v_perm_b32 v72, v3, v2, s68\n" \
  "v_accvgpr_write_b32 a172, v72\n" \
  "v_perm_b32 v72, v3, v2, s69\n" \
  "v_accvgpr_write_b32 a252, v72\n" \
  "v_perm_b32 v188, v5, v4, s68\n" \
  "v_perm_b32 v72, v7, v6, s68\n" \
  "v_accvgpr_write_b32 a21, v72\n" \
  "v_perm_b32 v72, v7, v6, s69\n" \
  "v_accvgpr_write_b32 a29, v72\n" \
  "v_perm_b32 v72, v9, v8, s68\n" \
  "v_accvgpr_write_b32 a173, v72\n" \
  "v_perm_b32 v72, v9, v8, s69\n" \
  "v_accvgpr_write_b32 a253, v72\n" \
  "v_perm_b32 v189, v11, v10, s68\n" \
  "v_perm_b32 v72, v13, v12, s68\n" \
  "v_accvgpr_write_b32 a22, v72\n" \
  "v_perm_b32 v72, v13, v12, s69\n" \
  "v_accvgpr_write_b32 a30, v72\n" \
  "v_perm_b32 v72, v15, v14, s68\n" \
  "v_accvgpr_write_b32 a174, v72\n" \
  "v_perm_b32 v72, v15, v14, s69\n" \
  "v_accvgpr_write_b32 a254, v72\n" \
  "v_perm_b32 v190, v17, v16, s68\n" \
  "v_perm_b32 v72, v19, v18, s68\n" \
  "v_accvgpr_write_b32 a23, v72\n" \
  "v_perm_b32 v72, v19, v18, s69\n" \
  "v_accvgpr_write_b32 a31, v72\n" \
  "v_perm_b32 v72, v21, v20, s68\n" \
  "v_accvgpr_write_b32 a175, v72\n" \
  "v_perm_b32 v72, v21, v20, s69\n" \
  "v_accvgpr_write_b32 a255, v72\n" \
  "v_perm_b32 v191, v23, v22, s68\n" \
  "s_waitcnt vmcnt(24)\n" \
  "s_mov_b32 s65, 1572864\n" \
  "buffer_load_dword v0, v84, s[12:15], s65 offen\n" \
  "buffer_load_dword v2, v84, s[16:19], s65 offen\n" \
  "buffer_load_ushort v4, v86, s[20:23], s65 offen\n" \
  "s_mov_b32 s65, 1581056\n" \
  "buffer_load_dword v1, v84, s[12:15], s65 offen\n" \
  "buffer_load_dword v3, v84, s[16:19], s65 offen\n" \
  "buffer_load_ushort v5, v86, s[20:23], s65 offen\n" \
  "s_mov_b32 s65, 1769472\n" \
  "buffer_load_dword v6, v84, s[12:15], s65 offen\n" \
  "buffer_load_dword v8, v84, s[16:19], s65 offen\n" \
  "buffer_load_ushort v10, v86, s[20:23], s65 offen\n" \
  "s_mov_b32 s65, 1777664\n" \
  "buffer_load_dword v7, v84, s[12:15], s65 offen\n" \
  "buffer_load_dword v9, v84, s[16:19], s65 offen\n" \
  "buffer_load_ushort v11, v86, s[20:23], s65 offen\n" \
  "s_mov_b32 s65, 1589248\n" \
  "buffer_load_dword v12, v84, s[12:15], s65 offen\n" \
  "buffer_load_dword v14, v84, s[16:19], s65 offen\n" \
  "buffer_load_ushort v16, v86, s[20:23], s65 offen\n" \
  "s_mov_b32 s65, 1597440\n" \
  "buffer_load_dword v13, v84, s[12:15], s65 offen\n" \
  "buffer_load_dword v15, v84, s[16:19], s65 offen\n" \
  "buffer_load_ushort v17, v86, s[20:23], s65 offen\n" \
  "s_mov_b32 s65, 1785856\n" \
  "buffer_load_dword v18, v84, s[12:15], s65 offen\n" \
  "buffer_load_dword v20, v84, s[16:19], s65 offen\n" \
  "buffer_load_ushort v22, v86, s[20:23], s65 offen\n" \
  "s_mov_b32 s65, 1794048\n" \
  "buffer_load_dword v19, v84, s[12:15], s65 offen\n" \
  "buffer_load_dword v21, v84, s[16:19], s65 offen\n" \
  "buffer_load_ushort v23, v86, s[20:23], s65 offen\n" \
  "v_perm_b32 v72, v25, v24, s68\n" \
  "v_accvgpr_write_b32 a32, v72\n" \
  "v_perm_b32 v72, v25, v24, s69\n" \
  "v_accvgpr_write_b32 a40, v72\n" \
  "v_perm_b32 v72, v27, v26, s68\n" \
  "v_accvgpr_write_b32 a176, v72\n" \
  "v_perm_b32 v112, v27, v26, s69\n" \
  "v_perm_b32 v192, v29, v28, s68\n" \
  "v_perm_b32 v72, v31, v30, s68\n" \
  "v_accvgpr_write_b32 a33, v72\n" \
  "v_perm_b32 v72, v31, v30, s69\n" \
  "v_accvgpr_write_b32 a41, v72\n" \
  "v_perm_b32 v72, v33, v32, s68\n" \
  "v_accvgpr_write_b32 a177, v72\n" \
  "v_perm_b32 v113, v33, v32, s69\n" \
  "v_perm_b32 v193, v35, v34, s68\n" \
  "v_perm_b32 v72, v37, v36, s68\n" \
  "v_accvgpr_write_b32 a34, v72\n" \
  "v_perm_b32 v72, v37, v36, s69\n" \
  "v_accvgpr_write_b32 a42, v72\n" \
  "v_perm_b32 v72, v39, v38, s68\n" \
  "v_accvgpr_write_b32 a178, v72\n" \
  "v_perm_b32 v114, v39, v38, s69\n" \
  "v_perm_b32 v194, v41, v40, s68\n" \
  "v_perm_b32 v72, v43, v42, s68\n" \
  "v_accvgpr_write_b32 a35, v72\n" \
  "v_perm_b32 v72, v43, v42, s69\n" \
  "v_accvgpr_write_b32 a43, v72\n" \
  "v_perm_b32 v72, v45, v44, s68\n" \
  "v_accvgpr_write_b32 a179, v72\n" \
  "v_perm_b32 v115, v45, v44, s69\n" \
  "v_perm_b32 v195, v47, v46, s68\n" \
  "s_waitcnt vmcnt(24)\n" \
  "s_mov_b32 s65, 2359296\n" \
  "buffer_load_dword v24, v84, s[12:15], s65 offen\n" \
  "buffer_load_dword v26, v84, s[16:19], s65 offen\n" \
  "buffer_load_ushort v28, v86, s[20:23], s65 offen\n" \
  "s_mov_b32 s65, 2367488\n" \
  "buffer_load_dword v25, v84, s[12:15], s65 offen\n" \
  "buffer_load_dword v27, v84, s[16:19], s65 offen\n" \
  "buffer_load_ushort v29, v86, s[20:23], s65 offen\n" \
  "s_mov_b32 s65, 2555904\n" \
  "buffer_load_dword v30, v84, s[12:15], s65 offen\n" \
  "buffer_load_dword v32, v84, s[16:19], s65 offen\n" \
  "buffer_load_ushort v34, v86, s[20:23], s65 offen\n" \
  "s_mov_b32 s65, 2564096\n" \
  "buffer_load_dword v31, v84, s[12:15], s65 offen\n" \
  "buffer_load_dword v33, v84, s[16:19], s65 offen\n" \
  "buffer_load_ushort v35, v86, s[20:23], s65 offen\n" \
  "s_mov_b32 s65, 2375680\n" \
  "buffer_load_dword v36, v84, s[12:15], s65 offen\n" \
  "buffer_load_dword v38, v84, s[16:19], s65 offen\n" \
  "buffer_load_ushort v40, v86, s[20:23], s65 offen\n" \
  "s_mov_b32 s65, 2383872\n" \
  "buffer_load_dword v37, v84, s[12:15], s65 offen\n" \
  "buffer_load_dword v39, v84, s[16:19], s65 offen\n" \
  "buffer_load_ushort v41, v86, s[20:23], s65 offen\n" \
  "s_mov_b32 s65, 2572288\n" \
  "buffer_load_dword v42, v84, s[12:15], s65 offen\n" \
  "buffer_load_dword v44, v84, s[16:19], s65 offen\n" \
  "buffer_load_ushort v46, v86, s[20:23], s65 offen\n" \
  "s_mov_b32 s65, 2580480\n" \
  "buffer_load_dword v43, v84, s[12:15], s65 offen\n" \
  "buffer_load_dword v45, v84, s[16:19], s65 offen\n" \
  "buffer_load_ushort v47, v86, s[20:23], s65 offen\n" \
  "v_perm_b32 v72, v49, v48, s68\n" \
  "v_accvgpr_write_b32 a36, v72\n" \
  "v_perm_b32 v72, v49, v48, s69\n" \
  "v_accvgpr_write_b32 a44, v72\n" \
  "v_perm_b32 v72, v51, v50, s68\n" \
  "v_accvgpr_write_b32 a180, v72\n" \
  "v_perm_b32 v116, v51, v50, s69\n" \
  "v_perm_b32 v196, v53, v52, s68\n" \
  "v_perm_b32 v72, v55, v54, s68\n" \
  "v_accvgpr_write_b32 a37, v72\n" \
  "v_perm_b32 v72, v55, v54, s69\n" \
  "v_accvgpr_write_b32 a45, v72\n" \
  "v_perm_b32 v72, v57, v56, s68\n" \
  "v_accvgpr_write_b32 a181, v72\n" \
  "v_perm_b32 v117, v57, v56, s69\n" \
  "v_perm_b32 v197, v59, v58, s68\n" \
  "v_perm_b32 v72, v61, v60, s68\n" \
  "v_accvgpr_write_b32 a38, v72\n" \
  "v_perm_b32 v72, v61, v60, s69\n" \
  "v_accvgpr_write_b32 a46, v72\n" \
  "v_perm_b32 v72, v63, v62, s68\n" \
  "v_accvgpr_write_b32 a182, v72\n" \
  "v_perm_b32 v118, v63, v62, s69\n" \
  "v_perm_b32 v198, v65, v64, s68\n" \
  "v_perm_b32 v72, v67, v66, s68\n" \
  "v_accvgpr_write_b32 a39, v72\n" \
  "v_perm_b32 v72, v67, v66, s69\n" \
  "v_accvgpr_write_b32 a47, v72\n" \
  "v_perm_b32 v72, v69, v68, s68\n" \
  "v_accvgpr_write_b32 a183, v72\n" \
  "v_perm_b32 v119, v69, v68, s69\n" \
  "v_perm_b32 v199, v71, v70, s68\n" \
  "s_waitcnt vmcnt(24)\n" \
  "s_mov_b32 s65, 1638400\n" \
  "buffer_load_dword v48, v84, s[12:15], s65 offen\n" \
  "buffer_load_dword v50, v84, s[16:19], s65 offen\n" \
  "buffer_load_ushort v52, v86, s[20:23], s65 offen\n" \
  "s_mov_b32 s65, 1646592\n" \
  "buffer_load_dword v49, v84, s[12:15], s65 offen\n" \
  "buffer_load_dword v51, v84, s[16:19], s65 offen\n" \
  "buffer_load_ushort v53, v86, s[20:23], s65 offen\n" \
  "s_mov_b32 s65, 1835008\n" \
  "buffer_load_dword v54, v84, s[12:15], s65 offen\n" \
  "buffer_load_dword v56, v84, s[16:19], s65 offen\n" \
  "buffer_load_ushort v58, v86, s[20:23], s65 offen\n" \
  "s_mov_b32 s65, 1843200\n" \
  "buffer_load_dword v55, v84, s[12:15], s65 offen\n" \
  "buffer_load_dword v57, v84, s[16:19], s65 offen\n" \
  "buffer_load_ushort v59, v86, s[20:23], s65 offen\n" \
  "s_mov_b32 s65, 1654784\n" \
  "buffer_load_dword v60, v84, s[12:15], s65 offen\n" \
  "buffer_load_dword v62, v84, s[16:19], s65 offen\n" \
  "buffer_load_ushort v64, v86, s[20:23], s65 offen\n" \
  "s_mov_b32 s65, 1662976\n" \
  "buffer_load_dword v61, v84, s[12:15], s65 offen\n" \
  "buffer_load_dword v63, v84, s[16:19], s65 offen\n" \
  "buffer_load_ushort v65, v86, s[20:23], s65 offen\n" \
  "s_mov_b32 s65, 1851392\n" \
  "buffer_load_dword v66, v84, s[12:15], s65 offen\n" \
  "buffer_load_dword v68, v84, s[16:19], s65 offen\n" \
  "buffer_load_ushort v70, v86, s[20:23], s65 offen\n" \
  "s_mov_b32 s65, 1859584\n" \
  "buffer_load_dword v67, v84, s[12:15], s65 offen\n" \
  "buffer_load_dword v69, v84, s[16:19], s65 offen\n" \
  "buffer_load_ushort v71, v86, s[20:23], s65 offen\n" \
  "v_perm_b32 v72, v1, v0, s68\n" \
  "v_accvgpr_write_b32 a48, v72\n" \
  "v_perm_b32 v72, v1, v0, s69\n" \
  "v_accvgpr_write_b32 a56, v72\n" \
  "v_perm_b32 v72, v3, v2, s68\n" \
  "v_accvgpr_write_b32 a184, v72\n" \
  "v_perm_b32 v120, v3, v2, s69\n" \
  "v_perm_b32 v200, v5, v4, s68\n" \
  "v_perm_b32 v72, v7, v6, s68\n" \
  "v_accvgpr_write_b32 a49, v72\n" \
  "v_perm_b32 v72, v7, v6, s69\n" \
  "v_accvgpr_write_b32 a57, v72\n" \
  "v_perm_b32 v72, v9, v8, s68\n" \
  "v_accvgpr_write_b32 a185, v72\n" \
  "v_perm_b32 v121, v9, v8, s69\n" \
  "v_perm_b32 v201, v11, v10, s68\n" \
  "v_perm_b32 v72, v13, v12, s68\n" \
  "v_accvgpr_write_b32 a50, v72\n" \
  "v_perm_b32 v72, v13, v12, s69\n" \
  "v_accvgpr_write_b32 a58, v72\n" \
  "v_perm_b32 v72, v15, v14, s68\n" \
  "v_accvgpr_write_b32 a186, v72\n" \
  "v_perm_b32 v122, v15, v14, s69\n" \
  "v_perm_b32 v202, v17, v16, s68\n" \
  "v_perm_b32 v72, v19, v18, s68\n" \
  "v_accvgpr_write_b32 a51, v72\n" \
  "v_perm_b32 v72, v19, v18, s69\n" \
  "v_accvgpr_write_b32 a59, v72\n" \
  "v_perm_b32 v72, v21, v20, s68\n" \
  "v_accvgpr_write_b32 a187, v72\n" \
  "v_perm_b32 v123, v21, v20, s69\n" \
  "v_perm_b32 v203, v23, v22, s68\n" \
  "s_waitcnt vmcnt(24)\n" \
  "s_mov_b32 s65, 2424832\n" \
  "buffer_load_dword v0, v84, s[12:15], s65 offen\n" \
  "buffer_load_dword v2, v84, s[16:19], s65 offen\n" \
  "buffer_load_ushort v4, v86, s[20:23], s65 offen\n" \
  "s_mov_b32 s65, 2433024\n" \
  "buffer_load_dword v1, v84, s[12:15], s65 offen\n" \
  "buffer_load_dword v3, v84, s[16:19], s65 offen\n" \
  "buffer_load_ushort v5, v86, s[20:23], s65 offen\n" \
  "s_mov_b32 s65, 2621440\n" \
  "buffer_load_dword v6, v84, s[12:15], s65 offen\n" \
  "buffer_load_dword v8, v84, s[16:19], s65 offen\n" \
  "buffer_load_ushort v10, v86, s[20:23], s65 offen\n" \
  "s_mov_b32 s65, 2629632\n" \
  "buffer_load_dword v7, v84, s[12:15], s65 offen\n" \
  "buffer_load_dword v9, v84, s[16:19], s65 offen\n" \
  "buffer_load_ushort v11, v86, s[20:23], s65 offen\n" \
  "s_mov_b32 s65, 2441216\n" \
  "buffer_load_dword v12, v84, s[12:15], s65 offen\n" \
  "buffer_load_dword v14, v84, s[16:19], s65 offen\n" \
  "buffer_load_ushort v16, v86, s[20:23], s65 offen\n" \
  "s_mov_b32 s65, 2449408\n" \
  "buffer_load_dword v13, v84, s[12:15], s65 offen\n" \
  "buffer_load_dword v15, v84, s[16:19], s65 offen\n" \
  "buffer_load_ushort v17, v86, s[20:23], s65 offen\n" \
  "s_mov_b32 s65, 2637824\n" \
  "buffer_load_dword v18, v84, s[12:15], s65 offen\n" \
  "buffer_load_dword v20, v84, s[16:19], s65 offen\n" \
  "buffer_load_ushort v22, v86, s[20:23], s65 offen\n" \
  "s_mov_b32 s65, 2646016\n" \
  "buffer_load_dword v19, v84, s[12:15], s65 offen\n" \
  "buffer_load_dword v21, v84, s[16:19], s65 offen\n" \
  "buffer_load_ushort v23, v86, s[20:23], s65 offen\n" \
  "v_perm_b32 v72, v25, v24, s68\n" \
  "v_accvgpr_write_b32 a52, v72\n" \
  "v_perm_b32 v72, v25, v24, s69\n" \
  "v_accvgpr_write_b32 a60, v72\n" \
  "v_perm_b32 v72, v27, v26, s68\n" \
  "v_accvgpr_write_b32 a188, v72\n" \
  "v_perm_b32 v124, v27, v26, s69\n" \
  "v_perm_b32 v204, v29, v28, s68\n" \
  "v_perm_b32 v72, v31, v30, s68\n" \
  "v_accvgpr_write_b32 a53, v72\n" \
  "v_perm_b32 v72, v31, v30, s69\n" \
  "v_accvgpr_write_b32 a61, v72\n" \
  "v_perm_b32 v72, v33, v32, s68\n" \
  "v_accvgpr_write_b32 a189, v72\n" \
  "v_perm_b32 v125, v33, v32, s69\n" \
  "v_perm_b32 v205, v35, v34, s68\n" \
  "v_perm_b32 v72, v37, v36, s68\n" \
  "v_accvgpr_write_b32 a54, v72\n" \
  "v_perm_b32 v72, v37, v36, s69\n" \
  "v_accvgpr_write_b32 a62, v72\n" \
  "v_perm_b32 v72, v39, v38, s68\n" \
  "v_accvgpr_write_b32 a190, v72\n" \
  "v_perm_b32 v126, v39, v38, s69\n" \
  "v_perm_b32 v206, v41, v40, s68\n" \
  "v_perm_b32 v72, v43, v42, s68\n" \
  "v_accvgpr_write_b32 a55, v72\n" \
  "v_perm_b32 v72, v43, v42, s69\n" \
  "v_accvgpr_write_b32 a63, v72\n" \
  "v_perm_b32 v72, v45, v44, s68\n" \
  "v_accvgpr_write_b32 a191, v72\n" \
  "v_perm_b32 v127, v45, v44, s69\n" \
  "v_perm_b32 v207, v47, v46, s68\n" \
  "s_waitcnt vmcnt(24)\n" \
  "s_mov_b32 s65, 1703936\n" \
  "buffer_load_dword v24, v84, s[12:15], s65 offen\n" \
  "buffer_load_dword v26, v84, s[16:19], s65 offen\n" \
  "buffer_load_ushort v28, v86, s[20:23], s65 offen\n" \
  "s_mov_b32 s65, 1712128\n" \
  "buffer_load_dword v25, v84, s[12:15], s65 offen\n" \
  "buffer_load_dword v27, v84, s[16:19], s65 offen\n" \
  "buffer_load_ushort v29, v86, s[20:23], s65 offen\n" \
  "s_mov_b32 s65, 1900544\n" \
  "buffer_load_dword v30, v84, s[12:15], s65 offen\n" \
  "buffer_load_dword v32, v84, s[16:19], s65 offen\n" \
  "buffer_load_ushort v34, v86, s[20:23], s65 offen\n" \
  "s_mov_b32 s65, 1908736\n" \
  "buffer_load_dword v31, v84, s[12:15], s65 offen\n" \
  "buffer_load_dword v33, v84, s[16:19], s65 offen\n" \
  "buffer_load_ushort v35, v86, s[20:23], s65 offen\n" \
  "s_mov_b32 s65, 1720320\n" \
  "buffer_load_dword v36, v84, s[12:15], s65 offen\n" \
  "buffer_load_dword v38, v84, s[16:19], s65 offen\n" \
  "buffer_load_ushort v40, v86, s[20:23], s65 offen\n" \
  "s_mov_b32 s65, 1728512\n" \
  "buffer_load_dword v37, v84, s[12:15], s65 offen\n" \
  "buffer_load_dword v39, v84, s[16:19], s65 offen\n" \
  "buffer_load_ushort v41, v86, s[20:23], s65 offen\n" \
  "s_mov_b32 s65, 1916928\n" \
  "buffer_load_dword v42, v84, s[12:15], s65 offen\n" \
  "buffer_load_dword v44, v84, s[16:19], s65 offen\n" \
  "buffer_load_ushort v46, v86, s[20:23], s65 offen\n" \
  "s_mov_b32 s65, 1925120\n" \
  "buffer_load_dword v43, v84, s[12:15], s65 offen\n" \
  "buffer_load_dword v45, v84, s[16:19], s65 offen\n" \
  "buffer_load_ushort v47, v86, s[20:23], s65 offen\n" \
  "v_perm_b32 v72, v49, v48, s68\n" \
  "v_accvgpr_write_b32 a64, v72\n" \
  "v_perm_b32 v72, v49, v48, s69\n" \
  "v_accvgpr_write_b32 a72, v72\n" \
  "v_perm_b32 v72, v51, v50, s68\n" \
  "v_accvgpr_write_b32 a192, v72\n" \
  "v_perm_b32 v128, v51, v50, s69\n" \
  "v_perm_b32 v208, v53, v52, s68\n" \
  "v_perm_b32 v72, v55, v54, s68\n" \
  "v_accvgpr_write_b32 a65, v72\n" \
  "v_perm_b32 v72, v55, v54, s69\n" \
  "v_accvgpr_write_b32 a73, v72\n" \
  "v_perm_b32 v72, v57, v56, s68\n" \
  "v_accvgpr_write_b32 a193, v72\n" \
  "v_perm_b32 v129, v57, v56, s69\n" \
  "v_perm_b32 v209, v59, v58, s68\n" \
  "v_perm_b32 v72, v61, v60, s68\n" \
  "v_accvgpr_write_b32 a66, v72\n" \
  "v_perm_b32 v72, v61, v60, s69\n" \
  "v_accvgpr_write_b32 a74, v72\n" \
  "v_perm_b32 v72, v63, v62, s68\n" \
  "v_accvgpr_write_b32 a194, v72\n" \
  "v_perm_b32 v130, v63, v62, s69\n" \
  "v_perm_b32 v210, v65, v64, s68\n" \
  "v_perm_b32 v72, v67, v66, s68\n" \
  "v_accvgpr_write_b32 a67, v72\n" \
  "v_perm_b32 v72, v67, v66, s69\n" \
  "v_accvgpr_write_b32 a75, v72\n" \
  "v_perm_b32 v72, v69, v68, s68\n" \
  "v_accvgpr_write_b32 a195, v72\n" \
  "v_perm_b32 v131, v69, v68, s69\n" \
  "v_perm_b32 v211, v71, v70, s68\n" \
  "s_waitcnt vmcnt(24)\n" \
  "s_mov_b32 s65, 2490368\n" \
  "buffer_load_dword v48, v84, s[12:15], s65 offen\n" \
  "buffer_load_dword v50, v84, s[16:19], s65 offen\n" \
  "buffer_load_ushort v52, v86, s[20:23], s65 offen\n" \
  "s_mov_b32 s65, 2498560\n" \
  "buffer_load_dword v49, v84, s[12:15], s65 offen\n" \
  "buffer_load_dword v51, v84, s[16:19], s65 offen\n" \
  "buffer_load_ushort v53, v86, s[20:23], s65 offen\n" \
  "s_mov_b32 s65, 2686976\n" \
  "buffer_load_dword v54, v84, s[12:15], s65 offen\n" \
  "buffer_load_dword v56, v84, s[16:19], s65 offen\n" \
  "buffer_load_ushort v58, v86, s[20:23], s65 offen\n" \
  "s_mov_b32 s65, 2695168\n" \
  "buffer_load_dword v55, v84, s[12:15], s65 offen\n" \
  "buffer_load_dword v57, v84, s[16:19], s65 offen\n" \
  "buffer_load_ushort v59, v86, s[20:23], s65 offen\n" \
  "s_mov_b32 s65, 2506752\n" \
  "buffer_load_dword v60, v84, s[12:15], s65 offen\n" \
  "buffer_load_dword v62, v84, s[16:19], s65 offen\n" \
  "buffer_load_ushort v64, v86, s[20:23], s65 offen\n" \
  "s_mov_b32 s65, 2514944\n" \
  "buffer_load_dword v61, v84, s[12:15], s65 offen\n" \
  "buffer_load_dword v63, v84, s[16:19], s65 offen\n" \
  "buffer_load_ushort v65, v86, s[20:23], s65 offen\n" \
  "s_mov_b32 s65, 2703360\n" \
  "buffer_load_dword v66, v84, s[12:15], s65 offen\n" \
  "buffer_load_dword v68, v84, s[16:19], s65 offen\n" \
  "buffer_load_ushort v70, v86, s[20:23], s65 offen\n" \
  "s_mov_b32 s65, 2711552\n" \
  "buffer_load_dword v67, v84, s[12:15], s65 offen\n" \
  "buffer_load_dword v69, v84, s[16:19], s65 offen\n" \
  "buffer_load_ushort v71, v86, s[20:23], s65 offen\n" \
  "v_perm_b32 v72, v1, v0, s68\n" \
  "v_accvgpr_write_b32 a68, v72\n" \
  "v_perm_b32 v72, v1, v0, s69\n" \
  "v_accvgpr_write_b32 a76, v72\n" \
  "v_perm_b32 v72, v3, v2, s68\n" \
  "v_accvgpr_write_b32 a196, v72\n" \
  "v_perm_b32 v132, v3, v2, s69\n" \
  "v_perm_b32 v212, v5, v4, s68\n" \
  "v_perm_b32 v72, v7, v6, s68\n" \
  "v_accvgpr_write_b32 a69, v72\n" \
  "v_perm_b32 v72, v7, v6, s69\n" \
  "v_accvgpr_write_b32 a77, v72\n" \
  "v_perm_b32 v72, v9, v8, s68\n" \
  "v_accvgpr_write_b32 a197, v72\n" \
  "v_perm_b32 v133, v9, v8, s69\n" \
  "v_perm_b32 v213, v11, v10, s68\n" \
  "v_perm_b32 v72, v13, v12, s68\n" \
  "v_accvgpr_write_b32 a70, v72\n" \
  "v_perm_b32 v72, v13, v12, s69\n" \
  "v_accvgpr_write_b32 a78, v72\n" \
  "v_perm_b32 v72, v15, v14, s68\n" \
  "v_accvgpr_write_b32 a198, v72\n" \
  "v_perm_b32 v134, v15, v14, s69\n" \
  "v_perm_b32 v214, v17, v16, s68\n" \
  "v_perm_b32 v72, v19, v18, s68\n" \
  "v_accvgpr_write_b32 a71, v72\n" \
  "v_perm_b32 v72, v19, v18, s69\n" \
  "v_accvgpr_write_b32 a79, v72\n" \
  "v_perm_b32 v72, v21, v20, s68\n" \
  "v_accvgpr_write_b32 a199, v72\n" \
  "v_perm_b32 v135, v21, v20, s69\n" \
  "v_perm_b32 v215, v23, v22, s68\n" \
  "s_waitcnt vmcnt(24)\n" \
  "s_mov_b32 s65, 3145728\n" \
  "buffer_load_dword v0, v84, s[12:15], s65 offen\n" \
  "buffer_load_dword v2, v84, s[16:19], s65 offen\n" \
  "buffer_load_ushort v4, v86, s[20:23], s65 offen\n" \
  "s_mov_b32 s65, 3153920\n" \
  "buffer_load_dword v1, v84, s[12:15], s65 offen\n" \
  "buffer_load_dword v3, v84, s[16:19], s65 offen\n" \
  "buffer_load_ushort v5, v86, s[20:23], s65 offen\n" \
  "s_mov_b32 s65, 3342336\n" \
  "buffer_load_dword v6, v84, s[12:15], s65 offen\n" \
  "buffer_load_dword v8, v84, s[16:19], s65 offen\n" \
  "buffer_load_ushort v10, v86, s[20:23], s65 offen\n" \
  "s_mov_b32 s65, 3350528\n" \
  "buffer_load_dword v7, v84, s[12:15], s65 offen\n" \
  "buffer_load_dword v9, v84, s[16:19], s65 offen\n" \
  "buffer_load_ushort v11, v86, s[20:23], s65 offen\n" \
  "s_mov_b32 s65, 3162112\n" \
  "buffer_load_dword v12, v84, s[12:15], s65 offen\n" \
  "buffer_load_dword v14, v84, s[16:19], s65 offen\n" \
  "buffer_load_ushort v16, v86, s[20:23], s65 offen\n" \
  "s_mov_b32 s65, 3170304\n" \
  "buffer_load_dword v13, v84, s[12:15], s65 offen\n" \
  "buffer_load_dword v15, v84, s[16:19], s65 offen\n" \
  "buffer_load_ushort v17, v86, s[20:23], s65 offen\n" \
  "s_mov_b32 s65, 3358720\n" \
  "buffer_load_dword v18, v84, s[12:15], s65 offen\n" \
  "buffer_load_dword v20, v84, s[16:19], s65 offen\n" \
  "buffer_load_ushort v22, v86, s[20:23], s65 offen\n" \
  "s_mov_b32 s65, 3366912\n" \
  "buffer_load_dword v19, v84, s[12:15], s65 offen\n" \
  "buffer_load_dword v21, v84, s[16:19], s65 offen\n" \
  "buffer_load_ushort v23, v86, s[20:23], s65 offen\n" \
  "v_perm_b32 v72, v25, v24, s68\n" \
  "v_accvgpr_write_b32 a80, v72\n" \
  "v_perm_b32 v72, v25, v24, s69\n" \
  "v_accvgpr_write_b32 a88, v72\n" \
  "v_perm_b32 v72, v27, v26, s68\n" \
  "v_accvgpr_write_b32 a200, v72\n" \
  "v_perm_b32 v136, v27, v26, s69\n" \
  "v_perm_b32 v216, v29, v28, s68\n" \
  "v_perm_b32 v72, v31, v30, s68\n" \
  "v_accvgpr_write_b32 a81, v72\n" \
  "v_perm_b32 v72, v31, v30, s69\n" \
  "v_accvgpr_write_b32 a89, v72\n" \
  "v_perm_b32 v72, v33, v32, s68\n" \
  "v_accvgpr_write_b32 a201, v72\n" \
  "v_perm_b32 v137, v33, v32, s69\n" \
  "v_perm_b32 v217, v35, v34, s68\n" \
  "v_perm_b32 v72, v37, v36, s68\n" \
  "v_accvgpr_write_b32 a82, v72\n" \
  "v_perm_b32 v72, v37, v36, s69\n" \
  "v_accvgpr_write_b32 a90, v72\n" \
  "v_perm_b32 v72, v39, v38, s68\n" \
  "v_accvgpr_write_b32 a202, v72\n" \
  "v_perm_b32 v138, v39, v38, s69\n" \
  "v_perm_b32 v218, v41, v40, s68\n" \
  "v_perm_b32 v72, v43, v42, s68\n" \
  "v_accvgpr_write_b32 a83, v72\n" \
  "v_perm_b32 v72, v43, v42, s69\n" \
  "v_accvgpr_write_b32 a91, v72\n" \
  "v_perm_b32 v72, v45, v44, s68\n" \
  "v_accvgpr_write_b32 a203, v72\n" \
  "v_perm_b32 v139, v45, v44, s69\n" \
  "v_perm_b32 v219, v47, v46, s68\n" \
  "s_waitcnt vmcnt(24)\n" \
  "s_mov_b32 s65, 3932160\n" \
  "buffer_load_dword v24, v84, s[12:15], s65 offen\n" \
  "buffer_load_dword v26, v84, s[16:19], s65 offen\n" \
  "buffer_load_ushort v28, v86, s[20:23], s65 offen\n" \
  "s_mov_b32 s65, 3940352\n" \
  "buffer_load_dword v25, v84, s[12:15], s65 offen\n" \
  "buffer_load_dword v27, v84, s[16:19], s65 offen\n" \
  "buffer_load_ushort v29, v86, s[20:23], s65 offen\n" \
  "s_mov_b32 s65, 4128768\n" \
  "buffer_load_dword v30, v84, s[12:15], s65 offen\n" \
  "buffer_load_dword v32, v84, s[16:19], s65 offen\n" \
  "buffer_load_ushort v34, v86, s[20:23], s65 offen\n" \
  "s_mov_b32 s65, 4136960\n" \
  "buffer_load_dword v31, v84, s[12:15], s65 offen\n" \
  "buffer_load_dword v33, v84, s[16:19], s65 offen\n" \
  "buffer_load_ushort v35, v86, s[20:23], s65 offen\n" \
  "s_mov_b32 s65, 3948544\n" \
  "buffer_load_dword v36, v84, s[12:15], s65 offen\n" \
  "buffer_load_dword v38, v84, s[16:19], s65 offen\n" \
  "buffer_load_ushort v40, v86, s[20:23], s65 offen\n" \
  "s_mov_b32 s65, 3956736\n" \
  "buffer_load_dword v37, v84, s[12:15], s65 offen\n" \
  "buffer_load_dword v39, v84, s[16:19], s65 offen\n" \
  "buffer_load_ushort v41, v86, s[20:23], s65 offen\n" \
  "s_mov_b32 s65, 4145152\n" \
  "buffer_load_dword v42, v84, s[12:15], s65 offen\n" \
  "buffer_load_dword v44, v84, s[16:19], s65 offen\n" \
  "buffer_load_ushort v46, v86, s[20:23], s65 offen\n" \
  "s_mov_b32 s65, 4153344\n" \
  "buffer_load_dword v43, v84, s[12:15], s65 offen\n" \
  "buffer_load_dword v45, v84, s[16:19], s65 offen\n" \
  "buffer_load_ushort v47, v86, s[20:23], s65 offen\n" \
  "v_perm_b32 v72, v49, v48, s68\n" \
  "v_accvgpr_write_b32 a84, v72\n" \
  "v_perm_b32 v72, v49, v48, s69\n" \
  "v_accvgpr_write_b32 a92, v72\n" \
  "v_perm_b32 v72, v51, v50, s68\n" \
  "v_accvgpr_write_b32 a204, v72\n" \
  "v_perm_b32 v140, v51, v50, s69\n" \
  "v_perm_b32 v220, v53, v52, s68\n" \
  "v_perm_b32 v72, v55, v54, s68\n" \
  "v_accvgpr_write_b32 a85, v72\n" \
  "v_perm_b32 v72, v55, v54, s69\n" \
  "v_accvgpr_write_b32 a93, v72\n" \
  "v_perm_b32 v72, v57, v56, s68\n" \
  "v_accvgpr_write_b32 a205, v72\n" \
  "v_perm_b32 v141, v57, v56, s69\n" \
  "v_perm_b32 v221, v59, v58, s68\n" \
  "v_perm_b32 v72, v61, v60, s68\n" \
  "v_accvgpr_write_b32 a86, v72\n" \
  "v_perm_b32 v72, v61, v60, s69\n" \
  "v_accvgpr_write_b32 a94, v72\n" \
  "v_perm_b32 v72, v63, v62, s68\n" \
  "v_accvgpr_write_b32 a206, v72\n" \
  "v_perm_b32 v142, v63, v62, s69\n" \
  "v_perm_b32 v222, v65, v64, s68\n" \
  "v_perm_b32 v72, v67, v66, s68\n" \
  "v_accvgpr_write_b32 a87, v72\n" \
  "v_perm_b32 v72, v67, v66, s69\n" \
  "v_accvgpr_write_b32 a95, v72\n" \
  "v_perm_b32 v72, v69, v68, s68\n" \
  "v_accvgpr_write_b32 a207, v72\n" \
  "v_perm_b32 v143, v69, v68, s69\n" \
  "v_perm_b32 v223, v71, v70, s68\n" \
  "s_waitcnt vmcnt(24)\n" \
  "s_mov_b32 s65, 3211264\n" \
  "buffer_load_dword v48, v84, s[12:15], s65 offen\n" \
  "buffer_load_dword v50, v84, s[16:19], s65 offen\n" \
  "buffer_load_ushort v52, v86, s[20:23], s65 offen\n" \
  "s_mov_b32 s65, 3219456\n" \
  "buffer_load_dword v49, v84, s[12:15], s65 offen\n" \
  "buffer_load_dword v51, v84, s[16:19], s65 offen\n" \
  "buffer_load_ushort v53, v86, s[20:23], s65 offen\n" \
  "s_mov_b32 s65, 3407872\n" \
  "buffer_load_dword v54, v84, s[12:15], s65 offen\n" \
  "buffer_load_dword v56, v84, s[16:19], s65 offen\n" \
  "buffer_load_ushort v58, v86, s[20:23], s65 offen\n" \
  "s_mov_b32 s65, 3416064\n" \
  "buffer_load_dword v55, v84, s[12:15], s65 offen\n" \
  "buffer_load_dword v57, v84, s[16:19], s65 offen\n" \
  "buffer_load_ushort v59, v86, s[20:23], s65 offen\n" \
  "s_mov_b32 s65, 3227648\n" \
  "buffer_load_dword v60, v84, s[12:15], s65 offen\n" \
  "buffer_load_dword v62, v84, s[16:19], s65 offen\n" \
  "buffer_load_ushort v64, v86, s[20:23], s65 offen\n" \
  "s_mov_b32 s65, 3235840\n" \
  "buffer_load_dword v61, v84, s[12:15], s65 offen\n" \
  "buffer_load_dword v63, v84, s[16:19], s65 offen\n" \
  "buffer_load_ushort v65, v86, s[20:23], s65 offen\n" \
  "s_mov_b32 s65, 3424256\n" \
  "buffer_load_dword v66, v84, s[12:15], s65 offen\n" \
  "buffer_load_dword v68, v84, s[16:19], s65 offen\n" \
  "buffer_load_ushort v70, v86, s[20:23], s65 offen\n" \
  "s_mov_b32 s65, 3432448\n" \
  "buffer_load_dword v67, v84, s[12:15], s65 offen\n" \
  "buffer_load_dword v69, v84, s[16:19], s65 offen\n" \
  "buffer_load_ushort v71, v86, s[20:23], s65 offen\n" \
  "v_perm_b32 v72, v1, v0, s68\n" \
  "v_accvgpr_write_b32 a96, v72\n" \
  "v_perm_b32 v72, v1, v0, s69\n" \
  "v_accvgpr_write_b32 a104, v72\n" \
  "v_perm_b32 v72, v3, v2, s68\n" \
  "v_accvgpr_write_b32 a208, v72\n" \
  "v_perm_b32 v144, v3, v2, s69\n" \
  "v_perm_b32 v224, v5, v4, s68\n" \
  "v_perm_b32 v72, v7, v6, s68\n" \
  "v_accvgpr_write_b32 a97, v72\n" \
  "v_perm_b32 v72, v7, v6, s69\n" \
  "v_accvgpr_write_b32 a105, v72\n" \
  "v_perm_b32 v72, v9, v8, s68\n" \
  "v_accvgpr_write_b32 a209, v72\n" \
  "v_perm_b32 v145, v9, v8, s69\n" \
  "v_perm_b32 v225, v11, v10, s68\n" \
  "v_perm_b32 v72, v13, v12, s68\n" \
  "v_accvgpr_write_b32 a98, v72\n" \
  "v_perm_b32 v72, v13, v12, s69\n" \
  "v_accvgpr_write_b32 a106, v72\n" \
  "v_perm_b32 v72, v15, v14, s68\n" \
  "v_accvgpr_write_b32 a210, v72\n" \
  "v_perm_b32 v146, v15, v14, s69\n" \
  "v_perm_b32 v226, v17, v16, s68\n" \
  "v_perm_b32 v72, v19, v18, s68\n" \
  "v_accvgpr_write_b32 a99, v72\n" \
  "v_perm_b32 v72, v19, v18, s69\n" \
  "v_accvgpr_write_b32 a107, v72\n" \
  "v_perm_b32 v72, v21, v20, s68\n" \
  "v_accvgpr_write_b32 a211, v72\n" \
  "v_perm_b32 v147, v21, v20, s69\n" \
  "v_perm_b32 v227, v23, v22, s68\n" \
  "s_waitcnt vmcnt(24)\n" \
  "s_mov_b32 s65, 3997696\n" \
  "buffer_load_dword v0, v84, s[12:15], s65 offen\n" \
  "buffer_load_dword v2, v84, s[16:19], s65 offen\n" \
  "buffer_load_ushort v4, v86, s[20:23], s65 offen\n" \
  "s_mov_b32 s65, 4005888\n" \
  "buffer_load_dword v1, v84, s[12:15], s65 offen\n" \
  "buffer_load_dword v3, v84, s[16:19], s65 offen\n" \
  "buffer_load_ushort v5, v86, s[20:23], s65 offen\n" \
  "s_mov_b32 s65, 4194304\n" \
  "buffer_load_dword v6, v84, s[12:15], s65 offen\n" \
  "buffer_load_dword v8, v84, s[16:19], s65 offen\n" \
  "buffer_load_ushort v10, v86, s[20:23], s65 offen\n" \
  "s_mov_b32 s65, 4202496\n" \
  "buffer_load_dword v7, v84, s[12:15], s65 offen\n" \
  "buffer_load_dword v9, v84, s[16:19], s65 offen\n" \
  "buffer_load_ushort v11, v86, s[20:23], s65 offen\n" \
  "s_mov_b32 s65, 4014080\n" \
  "buffer_load_dword v12, v84, s[12:15], s65 offen\n" \
  "buffer_load_dword v14, v84, s[16:19], s65 offen\n" \
  "buffer_load_ushort v16, v86, s[20:23], s65 offen\n" \
  "s_mov_b32 s65, 4022272\n" \
  "buffer_load_dword v13, v84, s[12:15], s65 offen\n" \
  "buffer_load_dword v15, v84, s[16:19], s65 offen\n" \
  "buffer_load_ushort v17, v86, s[20:23], s65 offen\n" \
  "s_mov_b32 s65, 4210688\n" \
  "buffer_load_dword v18, v84, s[12:15], s65 offen\n" \
  "buffer_load_dword v20, v84, s[16:19], s65 offen\n" \
  "buffer_load_ushort v22, v86, s[20:23], s65 offen\n" \
  "s_mov_b32 s65, 4218880\n" \
  "buffer_load_dword v19, v84, s[12:15], s65 offen\n" \
  "buffer_load_dword v21, v84, s[16:19], s65 offen\n" \
  "buffer_load_ushort v23, v86, s[20:23], s65 offen\n" \
  "v_perm_b32 v72, v25, v24, s68\n" \
  "v_accvgpr_write_b32 a100, v72\n" \
  "v_perm_b32 v72, v25, v24, s69\n" \
  "v_accvgpr_write_b32 a108, v72\n" \
  "v_perm_b32 v72, v27, v26, s68\n" \
  "v_accvgpr_write_b32 a212, v72\n" \
  "v_perm_b32 v148, v27, v26, s69\n" \
  "v_perm_b32 v228, v29, v28, s68\n" \
  "v_perm_b32 v72, v31, v30, s68\n" \
  "v_accvgpr_write_b32 a101, v72\n" \
  "v_perm_b32 v72, v31, v30, s69\n" \
  "v_accvgpr_write_b32 a109, v72\n" \
  "v_perm_b32 v72, v33, v32, s68\n" \
  "v_accvgpr_write_b32 a213, v72\n" \
  "v_perm_b32 v149, v33, v32, s69\n" \
  "v_perm_b32 v229, v35, v34, s68\n" \
  "v_perm_b32 v72, v37, v36, s68\n" \
  "v_accvgpr_write_b32 a102, v72\n" \
  "v_perm_b32 v72, v37, v36, s69\n" \
  "v_accvgpr_write_b32 a110, v72\n" \
  "v_perm_b32 v72, v39, v38, s68\n" \
  "v_accvgpr_write_b32 a214, v72\n" \
  "v_perm_b32 v150, v39, v38, s69\n" \
  "v_perm_b32 v230, v41, v40, s68\n" \
  "v_perm_b32 v72, v43, v42, s68\n" \
  "v_accvgpr_write_b32 a103, v72\n" \
  "v_perm_b32 v72, v43, v42, s69\n" \
  "v_accvgpr_write_b32 a111, v72\n" \
  "v_perm_b32 v72, v45, v44, s68\n" \
  "v_accvgpr_write_b32 a215, v72\n" \
  "v_perm_b32 v151, v45, v44, s69\n" \
  "v_perm_b32 v231, v47, v46, s68\n" \
  "s_waitcnt vmcnt(24)\n" \
  "s_mov_b32 s65, 3276800\n" \
  "buffer_load_dword v24, v84, s[12:15], s65 offen\n" \
  "buffer_load_dword v26, v84, s[16:19], s65 offen\n" \
  "buffer_load_ushort v28, v86, s[20:23], s65 offen\n" \
  "s_mov_b32 s65, 3284992\n" \
  "buffer_load_dword v25, v84, s[12:15], s65 offen\n" \
  "buffer_load_dword v27, v84, s[16:19], s65 offen\n" \
  "buffer_load_ushort v29, v86, s[20:23], s65 offen\n" \
  "s_mov_b32 s65, 3473408\n" \
  "buffer_load_dword v30, v84, s[12:15], s65 offen\n" \
  "buffer_load_dword v32, v84, s[16:19], s65 offen\n" \
  "buffer_load_ushort v34, v86, s[20:23], s65 offen\n" \
  "s_mov_b32 s65, 3481600\n" \
  "buffer_load_dword v31, v84, s[12:15], s65 offen\n" \
  "buffer_load_dword v33, v84, s[16:19], s65 offen\n" \
  "buffer_load_ushort v35, v86, s[20:23], s65 offen\n" \
  "s_mov_b32 s65, 3293184\n" \
  "buffer_load_dword v36, v84, s[12:15], s65 offen\n" \
  "buffer_load_dword v38, v84, s[16:19], s65 offen\n" \
  "buffer_load_ushort v40, v86, s[20:23], s65 offen\n" \
  "s_mov_b32 s65, 3301376\n" \
  "buffer_load_dword v37, v84, s[12:15], s65 offen\n" \
  "buffer_load_dword v39, v84, s[16:19], s65 offen\n" \
  "buffer_load_ushort v41, v86, s[20:23], s65 offen\n" \
  "s_mov_b32 s65, 3489792\n" \
  "buffer_load_dword v42, v84, s[12:15], s65 offen\n" \
  "buffer_load_dword v44, v84, s[16:19], s65 offen\n" \
  "buffer_load_ushort v46, v86, s[20:23], s65 offen\n" \
  "s_mov_b32 s65, 3497984\n" \
  "buffer_load_dword v43, v84, s[12:15], s65 offen\n" \
  "buffer_load_dword v45, v84, s[16:19], s65 offen\n" \
  "buffer_load_ushort v47, v86, s[20:23], s65 offen\n" \
  "v_perm_b32 v72, v49, v48, s68\n" \
  "v_accvgpr_write_b32 a112, v72\n" \
  "v_perm_b32 v72, v49, v48, s69\n" \
  "v_accvgpr_write_b32 a120, v72\n" \
  "v_perm_b32 v72, v51, v50, s68\n" \
  "v_accvgpr_write_b32 a216, v72\n" \
  "v_perm_b32 v152, v51, v50, s69\n" \
  "v_perm_b32 v232, v53, v52, s68\n" \
  "v_perm_b32 v72, v55, v54, s68\n" \
  "v_accvgpr_write_b32 a113, v72\n" \
  "v_perm_b32 v72, v55, v54, s69\n" \
  "v_accvgpr_write_b32 a121, v72\n" \
  "v_perm_b32 v72, v57, v56, s68\n" \
  "v_accvgpr_write_b32 a217, v72\n" \
  "v_perm_b32 v153, v57, v56, s69\n" \
  "v_perm_b32 v233, v59, v58, s68\n" \
  "v_perm_b32 v72, v61, v60, s68\n" \
  "v_accvgpr_write_b32 a114, v72\n" \
  "v_perm_b32 v72, v61, v60, s69\n" \
  "v_accvgpr_write_b32 a122, v72\n" \
  "v_perm_b32 v72, v63, v62, s68\n" \
  "v_accvgpr_write_b32 a218, v72\n" \
  "v_perm_b32 v154, v63, v62, s69\n" \
  "v_perm_b32 v234, v65, v64, s68\n" \
  "v_perm_b32 v72, v67, v66, s68\n" \
  "v_accvgpr_write_b32 a115, v72\n" \
  "v_perm_b32 v72, v67, v66, s69\n" \
  "v_accvgpr_write_b32 a123, v72\n" \
  "v_perm_b32 v72, v69, v68, s68\n" \
  "v_accvgpr_write_b32 a219, v72\n" \
  "v_perm_b32 v155, v69, v68, s69\n" \
  "v_perm_b32 v235, v71, v70, s68\n" \
  "s_waitcnt vmcnt(24)\n" \
  "s_mov_b32 s65, 4063232\n" \
  "buffer_load_dword v48, v84, s[12:15], s65 offen\n" \
  "buffer_load_dword v50, v84, s[16:19], s65 offen\n" \
  "buffer_load_ushort v52, v86, s[20:23], s65 offen\n" \
  "s_mov_b32 s65, 4071424\n" \
  "buffer_load_dword v49, v84, s[12:15], s65 offen\n" \
  "buffer_load_dword v51, v84, s[16:19], s65 offen\n" \
  "buffer_load_ushort v53, v86, s[20:23], s65 offen\n" \
  "s_mov_b32 s65, 4259840\n" \
  "buffer_load_dword v54, v84, s[12:15], s65 offen\n" \
  "buffer_load_dword v56, v84, s[16:19], s65 offen\n" \
  "buffer_load_ushort v58, v86, s[20:23], s65 offen\n" \
  "s_mov_b32 s65, 4268032\n" \
  "buffer_load_dword v55, v84, s[12:15], s65 offen\n" \
  "buffer_load_dword v57, v84, s[16:19], s65 offen\n" \
  "buffer_load_ushort v59, v86, s[20:23], s65 offen\n" \
  "s_mov_b32 s65, 4079616\n" \
  "buffer_load_dword v60, v84, s[12:15], s65 offen\n" \
  "buffer_load_dword v62, v84, s[16:19], s65 offen\n" \
  "buffer_load_ushort v64, v86, s[20:23], s65 offen\n" \
  "s_mov_b32 s65, 4087808\n" \
  "buffer_load_dword v61, v84, s[12:15], s65 offen\n" \
  "buffer_load_dword v63, v84, s[16:19], s65 offen\n" \
  "buffer_load_ushort v65, v86, s[20:23], s65 offen\n" \
  "s_mov_b32 s65, 4276224\n" \
  "buffer_load_dword v66, v84, s[12:15], s65 offen\n" \
  "buffer_load_dword v68, v84, s[16:19], s65 offen\n" \
  "buffer_load_ushort v70, v86, s[20:23], s65 offen\n" \
  "s_mov_b32 s65, 4284416\n" \
  "buffer_load_dword v67, v84, s[12:15], s65 offen\n" \
  "buffer_load_dword v69, v84, s[16:19], s65 offen\n" \
  "buffer_load_ushort v71, v86, s[20:23], s65 offen\n" \
  "v_perm_b32 v72, v1, v0, s68\n" \
  "v_accvgpr_write_b32 a116, v72\n" \
  "v_perm_b32 v72, v1, v0, s69\n" \
  "v_accvgpr_write_b32 a124, v72\n" \
  "v_perm_b32 v72, v3, v2, s68\n" \
  "v_accvgpr_write_b32 a220, v72\n" \
  "v_perm_b32 v156, v3, v2, s69\n" \
  "v_perm_b32 v236, v5, v4, s68\n" \
  "v_perm_b32 v72, v7, v6, s68\n" \
  "v_accvgpr_write_b32 a117, v72\n" \
  "v_perm_b32 v72, v7, v6, s69\n" \
  "v_accvgpr_write_b32 a125, v72\n" \
  "v_perm_b32 v72, v9, v8, s68\n" \
  "v_accvgpr_write_b32 a221, v72\n" \
  "v_perm_b32 v157, v9, v8, s69\n" \
  "v_perm_b32 v237, v11, v10, s68\n" \
  "v_perm_b32 v72, v13, v12, s68\n" \
  "v_accvgpr_write_b32 a118, v72\n" \
  "v_perm_b32 v72, v13, v12, s69\n" \
  "v_accvgpr_write_b32 a126, v72\n" \
  "v_perm_b32 v72, v15, v14, s68\n" \
  "v_accvgpr_write_b32 a222, v72\n" \
  "v_perm_b32 v158, v15, v14, s69\n" \
  "v_perm_b32 v238, v17, v16, s68\n" \
  "v_perm_b32 v72, v19, v18, s68\n" \
  "v_accvgpr_write_b32 a119, v72\n" \
  "v_perm_b32 v72, v19, v18, s69\n" \
  "v_accvgpr_write_b32 a127, v72\n" \
  "v_perm_b32 v72, v21, v20, s68\n" \
  "v_accvgpr_write_b32 a223, v72\n" \
  "v_perm_b32 v159, v21, v20, s69\n" \
  "v_perm_b32 v239, v23, v22, s68\n" \
  "s_waitcnt vmcnt(24)\n" \
  "s_mov_b32 s65, 4718592\n" \
  "buffer_load_dword v0, v85, s[12:15], s65 offen\n" \
  "buffer_load_dword v2, v85, s[16:19], s65 offen\n" \
  "buffer_load_ushort v4, v87, s[20:23], s65 offen\n" \
  "s_mov_b32 s65, 4726784\n" \
  "buffer_load_dword v1, v85, s[12:15], s65 offen\n" \
  "buffer_load_dword v3, v85, s[16:19], s65 offen\n" \
  "buffer_load_ushort v5, v87, s[20:23], s65 offen\n" \
  "s_mov_b32 s65, 4784128\n" \
  "buffer_load_dword v6, v85, s[12:15], s65 offen\n" \
  "buffer_load_dword v8, v85, s[16:19], s65 offen\n" \
  "buffer_load_ushort v10, v87, s[20:23], s65 offen\n" \
  "s_mov_b32 s65, 4792320\n" \
  "buffer_load_dword v7, v85, s[12:15], s65 offen\n" \
  "buffer_load_dword v9, v85, s[16:19], s65 offen\n" \
  "buffer_load_ushort v11, v87, s[20:23], s65 offen\n" \
  "s_mov_b32 s65, 4734976\n" \
  "buffer_load_dword v12, v85, s[12:15], s65 offen\n" \
  "buffer_load_dword v14, v85, s[16:19], s65 offen\n" \
  "buffer_load_ushort v16, v87, s[20:23], s65 offen\n" \
  "s_mov_b32 s65, 4743168\n" \
  "buffer_load_dword v13, v85, s[12:15], s65 offen\n" \
  "buffer_load_dword v15, v85, s[16:19], s65 offen\n" \
  "buffer_load_ushort v17, v87, s[20:23], s65 offen\n" \
  "s_mov_b32 s65, 4800512\n" \
  "buffer_load_dword v18, v85, s[12:15], s65 offen\n" \
  "buffer_load_dword v20, v85, s[16:19], s65 offen\n" \
  "buffer_load_ushort v22, v87, s[20:23], s65 offen\n" \
  "s_mov_b32 s65, 4808704\n" \
  "buffer_load_dword v19, v85, s[12:15], s65 offen\n" \
  "buffer_load_dword v21, v85, s[16:19], s65 offen\n" \
  "buffer_load_ushort v23, v87, s[20:23], s65 offen\n" \
  "v_perm_b32 v72, v25, v24, s68\n" \
  "v_accvgpr_write_b32 a128, v72\n" \
  "v_perm_b32 v72, v25, v24, s69\n" \
  "v_accvgpr_write_b32 a136, v72\n" \
  "v_perm_b32 v72, v27, v26, s68\n" \
  "v_accvgpr_write_b32 a224, v72\n" \
  "v_perm_b32 v160, v27, v26, s69\n" \
  "v_perm_b32 v240, v29, v28, s68\n" \
  "v_perm_b32 v72, v31, v30, s68\n" \
  "v_accvgpr_write_b32 a129, v72\n" \
  "v_perm_b32 v72, v31, v30, s69\n" \
  "v_accvgpr_write_b32 a137, v72\n" \
  "v_perm_b32 v72, v33, v32, s68\n" \
  "v_accvgpr_write_b32 a225, v72\n" \
  "v_perm_b32 v161, v33, v32, s69\n" \
  "v_perm_b32 v241, v35, v34, s68\n" \
  "v_perm_b32 v72, v37, v36, s68\n" \
  "v_accvgpr_write_b32 a130, v72\n" \
  "v_perm_b32 v72, v37, v36, s69\n" \
  "v_accvgpr_write_b32 a138, v72\n" \
  "v_perm_b32 v72, v39, v38, s68\n" \
  "v_accvgpr_write_b32 a226, v72\n" \
  "v_perm_b32 v162, v39, v38, s69\n" \
  "v_perm_b32 v242, v41, v40, s68\n" \
  "v_perm_b32 v72, v43, v42, s68\n" \
  "v_accvgpr_write_b32 a131, v72\n" \
  "v_perm_b32 v72, v43, v42, s69\n" \
  "v_accvgpr_write_b32 a139, v72\n" \
  "v_perm_b32 v72, v45, v44, s68\n" \
  "v_accvgpr_write_b32 a227, v72\n" \
  "v_perm_b32 v163, v45, v44, s69\n" \
  "v_perm_b32 v243, v47, v46, s68\n" \
  "s_waitcnt vmcnt(24)\n" \
  "s_mov_b32 s65, 4980736\n" \
  "buffer_load_dword v24, v85, s[12:15], s65 offen\n" \
  "buffer_load_dword v26, v85, s[16:19], s65 offen\n" \
  "buffer_load_ushort v28, v87, s[20:23], s65 offen\n" \
  "s_mov_b32 s65, 4988928\n" \
  "buffer_load_dword v25, v85, s[12:15], s65 offen\n" \
  "buffer_load_dword v27, v85, s[16:19], s65 offen\n" \
  "buffer_load_ushort v29, v87, s[20:23], s65 offen\n" \
  "s_mov_b32 s65, 5046272\n" \
  "buffer_load_dword v30, v85, s[12:15], s65 offen\n" \
  "buffer_load_dword v32, v85, s[16:19], s65 offen\n" \
  "buffer_load_ushort v34, v87, s[20:23], s65 offen\n" \
  "s_mov_b32 s65, 5054464\n" \
  "buffer_load_dword v31, v85, s[12:15], s65 offen\n" \
  "buffer_load_dword v33, v85, s[16:19], s65 offen\n" \
  "buffer_load_ushort v35, v87, s[20:23], s65 offen\n" \
  "s_mov_b32 s65, 4997120\n" \
  "buffer_load_dword v36, v85, s[12:15], s65 offen\n" \
  "buffer_load_dword v38, v85, s[16:19], s65 offen\n" \
  "buffer_load_ushort v40, v87, s[20:23], s65 offen\n" \
  "s_mov_b32 s65, 5005312\n" \
  "buffer_load_dword v37, v85, s[12:15], s65 offen\n" \
  "buffer_load_dword v39, v85, s[16:19], s65 offen\n" \
  "buffer_load_ushort v41, v87, s[20:23], s65 offen\n" \
  "s_mov_b32 s65, 5062656\n" \
  "buffer_load_dword v42, v85, s[12:15], s65 offen\n" \
  "buffer_load_dword v44, v85, s[16:19], s65 offen\n" \
  "buffer_load_ushort v46, v87, s[20:23], s65 offen\n" \
  "s_mov_b32 s65, 5070848\n" \
  "buffer_load_dword v43, v85, s[12:15], s65 offen\n" \
  "buffer_load_dword v45, v85, s[16:19], s65 offen\n" \
  "buffer_load_ushort v47, v87, s[20:23], s65 offen\n" \
  "v_perm_b32 v72, v49, v48, s68\n" \
  "v_accvgpr_write_b32 a132, v72\n" \
  "v_perm_b32 v72, v49, v48, s69\n" \
  "v_accvgpr_write_b32 a140, v72\n" \
  "v_perm_b32 v72, v51, v50, s68\n" \
  "v_accvgpr_write_b32 a228, v72\n" \
  "v_perm_b32 v164, v51, v50, s69\n" \
  "v_perm_b32 v244, v53, v52, s68\n" \
  "v_perm_b32 v72, v55, v54, s68\n" \
  "v_accvgpr_write_b32 a133, v72\n" \
  "v_perm_b32 v72, v55, v54, s69\n" \
  "v_accvgpr_write_b32 a141, v72\n" \
  "v_perm_b32 v72, v57, v56, s68\n" \
  "v_accvgpr_write_b32 a229, v72\n" \
  "v_perm_b32 v165, v57, v56, s69\n" \
  "v_perm_b32 v245, v59, v58, s68\n" \
  "v_perm_b32 v72, v61, v60, s68\n" \
  "v_accvgpr_write_b32 a134, v72\n" \
  "v_perm_b32 v72, v61, v60, s69\n" \
  "v_accvgpr_write_b32 a142, v72\n" \
  "v_perm_b32 v72, v63, v62, s68\n" \
  "v_accvgpr_write_b32 a230, v72\n" \
  "v_perm_b32 v166, v63, v62, s69\n" \
  "v_perm_b32 v246, v65, v64, s68\n" \
  "v_perm_b32 v72, v67, v66, s68\n" \
  "v_accvgpr_write_b32 a135, v72\n" \
  "v_perm_b32 v72, v67, v66, s69\n" \
  "v_accvgpr_write_b32 a143, v72\n" \
  "v_perm_b32 v72, v69, v68, s68\n" \
  "v_accvgpr_write_b32 a231, v72\n" \
  "v_perm_b32 v167, v69, v68, s69\n" \
  "v_perm_b32 v247, v71, v70, s68\n" \
  "s_waitcnt vmcnt(24)\n" \
  "v_perm_b32 v72, v1, v0, s68\n" \
  "v_accvgpr_write_b32 a144, v72\n" \
  "v_perm_b32 v72, v1, v0, s69\n" \
  "v_accvgpr_write_b32 a152, v72\n" \
  "v_perm_b32 v72, v3, v2, s68\n" \
  "v_accvgpr_write_b32 a232, v72\n" \
  "v_perm_b32 v168, v3, v2, s69\n" \
  "v_perm_b32 v248, v5, v4, s68\n" \
  "v_perm_b32 v72, v7, v6, s68\n" \
  "v_accvgpr_write_b32 a145, v72\n" \
  "v_perm_b32 v72, v7, v6, s69\n" \
  "v_accvgpr_write_b32 a153, v72\n" \
  "v_perm_b32 v72, v9, v8, s68\n" \
  "v_accvgpr_write_b32 a233, v72\n" \
  "v_perm_b32 v169, v9, v8, s69\n" \
  "v_perm_b32 v249, v11, v10, s68\n" \
  "v_perm_b32 v72, v13, v12, s68\n" \
  "v_accvgpr_write_b32 a146, v72\n" \
  "v_perm_b32 v72, v13, v12, s69\n" \
  "v_accvgpr_write_b32 a154, v72\n" \
  "v_perm_b32 v72, v15, v14, s68\n" \
  "v_accvgpr_write_b32 a234, v72\n" \
  "v_perm_b32 v170, v15, v14, s69\n" \
  "v_perm_b32 v250, v17, v16, s68\n" \
  "v_perm_b32 v72, v19, v18, s68\n" \
  "v_accvgpr_write_b32 a147, v72\n" \
  "v_perm_b32 v72, v19, v18, s69\n" \
  "v_accvgpr_write_b32 a155, v72\n" \
  "v_perm_b32 v72, v21, v20, s68\n" \
  "v_accvgpr_write_b32 a235, v72\n" \
  "v_perm_b32 v171, v21, v20, s69\n" \
  "v_perm_b32 v251, v23, v22, s68\n" \
  "s_waitcnt vmcnt(0)\n" \
  "v_perm_b32 v72, v25, v24, s68\n" \
  "v_accvgpr_write_b32 a148, v72\n" \
  "v_perm_b32 v72, v25, v24, s69\n" \
  "v_accvgpr_write_b32 a156, v72\n" \
  "v_perm_b32 v72, v27, v26, s68\n" \
  "v_accvgpr_write_b32 a236, v72\n" \
  "v_perm_b32 v172, v27, v26, s69\n" \
  "v_perm_b32 v252, v29, v28, s68\n" \
  "v_perm_b32 v72, v31, v30, s68\n" \
  "v_accvgpr_write_b32 a149, v72\n" \
  "v_perm_b32 v72, v31, v30, s69\n" \
  "v_accvgpr_write_b32 a157, v72\n" \
  "v_perm_b32 v72, v33, v32, s68\n" \
  "v_accvgpr_write_b32 a237, v72\n" \
  "v_perm_b32 v173, v33, v32, s69\n" \
  "v_perm_b32 v253, v35, v34, s68\n" \
  "v_perm_b32 v72, v37, v36, s68\n" \
  "v_accvgpr_write_b32 a150, v72\n" \
  "v_perm_b32 v72, v37, v36, s69\n" \
  "v_accvgpr_write_b32 a158, v72\n" \
  "v_perm_b32 v72, v39, v38, s68\n" \
  "v_accvgpr_write_b32 a238, v72\n" \
  "v_perm_b32 v174, v39, v38, s69\n" \
  "v_perm_b32 v254, v41, v40, s68\n" \
  "v_perm_b32 v72, v43, v42, s68\n" \
  "v_accvgpr_write_b32 a151, v72\n" \
  "v_perm_b32 v72, v43, v42, s69\n" \
  "v_accvgpr_write_b32 a159, v72\n" \
  "v_perm_b32 v72, v45, v44, s68\n" \
  "v_accvgpr_write_b32 a239, v72\n" \
  "v_perm_b32 v175, v45, v44, s69\n" \
  "v_perm_b32 v255, v47, v46, s68\n" \
  "v_mov_b32 v77, 0\n" \
  "v_mov_b32 v79, 0\n" \
  "s_mov_b32 s60, 1048576\n" \
  "s_mov_b32 s59, 0\n" \
  "s_mov_b32 s54, 0x3c000000\n" \
  "s_mov_b32 s47, 4\n" \
  "s_mov_b32 s46, 0\n" \
  "s_mov_b32 s53, 0xffbfffbf\n" \
  "s_mov_b32 s58, 0\n" \
  "s_mov_b32 s71, s34\n" \
  "s_barrier\n" \
  "LLAYER_%=:\n" \
  "s_mov_b32 s56, 0\n" \
  "s_mul_i32 s57, s71, 63\n" \
  "s_lshl_b32 s67, s71, 6\n" \
  "s_add_i32 s67, s67, 1\n" \
  "LRa_%=:\n" \
  "s_cmp_ge_u32 s59, s67\n" \
  "s_cbranch_scc1 LRax_%=\n" \
  "v_lshrrev_b32 v80, 5, v103\n" \
  "v_lshlrev_b32 v80, 14, v80\n" \
  "v_lshrrev_b32 v81, 2, v103\n" \
  "v_and_or_b32 v80, v81, s47, v80\n" \
  "s_mov_b64 exec, 63\n" \
  "buffer_load_dword v80, v80, s[24:27], s36 offen sc1\n" \
  "s_mov_b64 exec, -1\n" \
  "s_waitcnt vmcnt(0)\n" \
  "v_readlane_b32 s59, v80, 0\n" \
  "v_readlane_b32 s65, v80, 1\n" \
  "v_readlane_b32 s66, v80, 2\n" \
  "s_min_u32 s59, s59, s65\n" \
  "s_min_u32 s59, s59, s66\n" \
  "v_readlane_b32 s65, v80, 3\n" \
  "v_readlane_b32 s66, v80, s43\n" \
  "s_min_u32 s59, s59, s65\n" \
  "s_min_u32 s59, s59, s66\n" \
  "s_cmp_eq_u32 s60, 0\n" \
  "s_cbranch_scc1 LRax_%=\n" \
  "s_sub_u32 s60, s60, 1\n" \
  "s_branch LRa_%=\n" \
  "LRax_%=:\n" \
  "s_lshl_b32 s67, s28, 2\n" \
  "s_add_i32 s67, s67, 22528\n" \
  "s_mov_b32 s68, s29\n" \
  "s_add_i32 m0, s67, 0\n" \
  "s_add_i32 s65, s68, 0\n" \
  "buffer_load_dwordx4 v92, s[4:7], s65 offen sc1 lds\n" \
  "s_add_i32 m0, s67, 4096\n" \
  "s_add_i32 s65, s68, 4096\n" \
  "buffer_load_dwordx4 v92, s[4:7], s65 offen sc1 lds\n" \
  "s_add_i32 m0, s67, 8192\n" \
  "s_add_i32 s65, s68, 8192\n" \
  "buffer_load_dwordx4 v92, s[4:7], s65 offen sc1 lds\n" \
  "s_add_i32 m0, s67, 12288\n" \
  "s_add_i32 s65, s68, 12288\n" \
  "buffer_load_dwordx4 v92, s[4:7], s65 offen sc1 lds\n" \
  "s_add_i32 m0, s67, 16384\n" \
  "s_add_i32 s65, s68, s30\n" \
  "buffer_load_dwordx4 v92, s[4:7], s65 offen sc1 lds\n" \
  "s_waitcnt vmcnt(0)\n" \
  "s_barrier\n" \
  "s_mov_b32 s61, 22528\n" \
  "s_mov_b32 s62, 1572864\n" \
  "s_waitcnt lgkmcnt(0)\n" \
  "s_memrealtime s[72:73]\n" \
  "s_nop 2\n" \
  "s_memrealtime s[74:75]\n" \
  "s_nop 2\n" \
  "s_memrealtime s[76:77]\n" \
  "s_memtime vcc\n" \
  "s_nop 1\n" \
  "s_memrealtime s[78:79]\n" \
  "s_nop 2\n" \
  "s_memrealtime s[80:81]\n" \
  "s_nop 2\n" \
  "s_memrealtime s[82:83]\n" \
  "s_waitcnt lgkmcnt(0)\n" \
  "s_add_i32 s72, s72, s74\n" \
  "s_add_i32 s72, s72, s76\n" \
  "s_add_i32 s72, s72, s78\n" \
  "s_add_i32 s72, s72, s80\n" \
  "s_add_i32 s72, s72, s82\n" \
  "s_lshl_b32 s72, s72, 2\n" \
  "s_sub_i32 s52, s72, vcc_lo\n" \
  "s_add_i32 s58, s72, 502\n" \
  "LSTEP_%=:\n" \
  "s_and_b32 s63, s57, 3\n" \
  "s_mul_i32 s63, s63, 393216\n" \
  "s_add_i32 s68, s56, 1\n" \
  "s_and_b32 s68, s68, 63\n" \
  "s_mul_i32 s68, s68, 786432\n" \
  "s_add_i32 s68, s68, s29\n" \
  "s_xor_b32 s69, s61, 61440\n" \
  "s_lshl_b32 s67, s28, 2\n" \
  "s_add_i32 s69, s69, s67\n" \
  "s_lshl_b32 s65, s71, 6\n" \
  "s_add_i32 s65, s65, s56\n" \
  "s_add_i32 s65, s65, 2\n" \
  "s_cmp_lt_u32 s59, s65\n" \
  "s_cselect_b32 s70, 1, 0\n" \
  "s_cmp_eq_u32 s56, 63\n" \
  "s_cselect_b32 s70, 2, s70\n" \
  "s_cselect_b32 s65, 8192, 0\n" \
  "s_add_i32 s63, s63, s65\n" \
  "s_add_i32 s64, s63, 131072\n" \
  "s_lshr_b32 s65, s57, 4\n" \
  "s_lshl_b32 s65, s65, 14\n" \
  "s_and_b32 s66, s57, 15\n" \
  "s_lshl_b32 s66, s66, 9\n" \
  "s_add_i32 s55, s65, s66\n" \
  "s_add_i32 s55, s55, s40\n" \
  "s_waitcnt lgkmcnt(0)\n" \
  "LAGAIN_%=:\n" \
  "s_cmp_eq_u32 s70, 2\n" \
  "s_cbranch_scc1 LNOLEADb_%=\n" \
  "s_cmp_eq_u32 s33, 0\n" \
  "s_cbranch_scc1 LNOLEADb_%=\n" \
  "v_mov_b32 v80, s58\n" \
  "v_lshlrev_b32 v81, 3, v103\n" \
  "s_mov_b64 exec, 15\n" \
  "buffer_store_dword v80, v81, s[0:3], s55 offen sc1\n" \
  "s_mov_b64 exec, -1\n" \
  "LNOLEADb_%=:\n" \
  "s_mov_b32 m0, s67\n" \
  "s_waitcnt lgkmcnt(0)\n" \
  "s_cmp_lg_u32 s37, 0\n" \
  "s_cbranch_scc1 LGO_%=\n" \
  "s_sub_i32 s74, s58, s52\n" \
  "s_getpc_b64 s[76:77]\n" \
  "LPx_%=:\n" \
  "s_add_u32 s76, s76, LEx_%=-LPx_%=\n" \
  "s_addc_u32 s77, s77, 0\n" \
  "s_sub_i32 s74, s74, 128\n" \
  "LWx_%=:\n" \
  "s_memtime s[72:73]\n" \
  "s_waitcnt lgkmcnt(0)\n" \
  "s_sub_i32 s72, s74, s72\n" \
  "s_cmp_gt_u32 s72, 0x8000\n" \
  "s_cselect_b32 s72, 0, s72\n" \
  "s_cmp_gt_u32 s72, 255\n" \
  "s_cbranch_scc1 LWx_%=\n" \
  "s_lshr_b32 s72, s72, 3\n" \
  "s_lshl_b32 s72, s72, 2\n" \
  "s_sub_u32 s76, s76, s72\n" \
  "s_subb_u32 s77, s77, 0\n" \
  "s_setpc_b64 s[76:77]\n" \
  "s_nop 1\n" \
  "s_nop 1\n" \
  "s_nop 1\n" \
  "s_nop 1\n" \
  "s_nop 1\n" \
  "s_nop 1\n" \
  "s_nop 1\n" \
  "s_nop 1\n" \
  "s_nop 1\n" \
  "s_nop 1\n" \
  "s_nop 1\n" \
  "s_nop 1\n" \
  "s_nop 1\n" \
  "s_nop 1\n" \
  "s_nop 1\n" \
  "s_nop 1\n" \
  "s_nop 1\n" \
  "s_nop 1\n" \
  "s_nop 1\n" \
  "s_nop 1\n" \
  "s_nop 1\n" \
  "s_nop 1\n" \
  "s_nop 1\n" \
  "s_nop 1\n" \
  "s_nop 1\n" \
  "s_nop 1\n" \
  "s_nop 1\n" \
  "s_nop 1\n" \
  "s_nop 1\n" \
  "s_nop 1\n" \
  "s_nop 1\n" \
  "s_nop 1\n" \
  "LEx_%=:\n" \
  "LGO_%=:\n" \
  "s_barrier\n" \
  "buffer_load_dwordx4 v93, s[0:3], s62 offen sc1 lds\n" \
  "s_add_i32 m0, s67, 4096\n" \
  "s_nop 0\n" \
  "buffer_load_dwordx4 v94, s[0:3], s62 offen sc1 lds\n" \
  "s_add_i32 m0, s67, 8192\n" \
  "s_nop 0\n" \
  "buffer_load_dwordx4 v95, s[0:3], s62 offen sc1 lds\n" \
  "s_memrealtime s[72:73]\n" \
  "s_lshl_b32 s14, s28, 3\n" \
  "s_add_i32 s14, s14, s61\n" \
  "s_add_i32 s15, s28, s61\n" \
  "s_memrealtime s[74:75]\n" \
  "s_add_i32 s65, s57, 1\n" \
  "s_and_b32 s65, s65, 3\n" \
  "s_mul_i32 s65, s65, 393216\n" \
  "s_memrealtime s[76:77]\n" \
  "s_memtime s[12:13]\n" \
  "s_and_b32 s66, s63, 8192\n" \
  "s_add_i32 s65, s65, s66\n" \
  "s_memrealtime s[78:79]\n" \
  "s_add_i32 s66, s65, 131072\n" \
  "s_add_i32 s16, s68, s44\n" \
  "s_mov_b32 m0, s69\n" \
  "s_memrealtime s[80:81]\n" \
  "s_cmp_eq_u32 s70, 0\n" \
  "s_cselect_b32 s18, 0xffffff, 1\n" \
  "s_mov_b32 s19, 0\n" \
  "s_memrealtime s[82:83]\n" \
  "v_mov_b32 v86, s53\n" \
  "v_mov_b32 v87, s53\n" \
  "v_add_u32 v80, s14, v105\n" \
  "v_add_u32 v81, s15, v106\n" \
  "v_lshlrev_b32 v84, 3, v103\n" \
  "buffer_store_dwordx2 v[86:87], v104, s[0:3], s65 offen sc1\n" \
  "buffer_store_dwordx2 v[86:87], v104, s[0:3], s66 offen sc1\n" \
  "s_mov_b64 exec, s[18:19]\n" \
  "buffer_load_dword v84, s[4:7], s16 offen sc1 lds\n" \
  "s_mov_b64 exec, -1\n" \
  "s_waitcnt lgkmcnt(0)\n" \
  "ds_read_b128 v[48:51], v80 offset:8192\n" \
  "ds_read_b128 v[52:55], v80 offset:8448\n" \
  "ds_read_b128 v[72:75], v81 offset:16384\n" \
  "v_mov_b64 v[40:41], 0\n" \
  "v_mov_b64 v[42:43], 0\n" \
  "v_mov_b64 v[44:45], 0\n" \
  "v_mov_b64 v[46:47], 0\n" \
  "v_mov_b64 v[56:57], 0\n" \
  "v_mov_b64 v[58:59], 0\n" \
  "v_mov_b64 v[60:61], 0\n" \
  "v_mov_b64 v[62:63], 0\n" \
  "v_mov_b64 v[64:65], 0\n" \
  "v_mov_b64 v[66:67], 0\n" \
  "v_mov_b64 v[68:69], 0\n" \
  "v_mov_b64 v[70:71], 0\n" \
  "s_waitcnt vmcnt(5)\n" \
  "s_barrier\n" \
  "ds_read_b128 v[0:3], v96 offset:0\n" \
  "ds_read_b128 v[4:7], v96 offset:16\n" \
  "ds_read_b128 v[8:11], v96 offset:32\n" \
  "s_waitcnt vmcnt(4)\n" \
  "s_barrier\n" \
  "ds_read_b128 v[12:15], v96 offset:3072\n" \
  "ds_read_b128 v[16:19], v96 offset:3088\n" \
  "ds_read_b128 v[20:23], v96 offset:3104\n" \
  "s_waitcnt lgkmcnt(5)\n" \
  "v_smfmac_f32_16x16x64_bf16 v[40:43], v[0:3], a[0:7], v107\n" \
  "s_add_i32 s72, s72, s74\n" \
  "v_smfmac_f32_16x16x64_bf16 v[44:47], v[0:3], a[8:15], v107\n" \
  "s_add_i32 s72, s72, s76\n" \
  "s_waitcnt lgkmcnt(4)\n" \
  "v_smfmac_f32_16x16x64_bf16 v[40:43], v[4:7], a[16:23], v107\n" \
  "s_waitcnt vmcnt(3)\n" \
  "v_smfmac_f32_16x16x64_bf16 v[44:47], v[4:7], a[24:31], v107\n" \
  "s_barrier\n" \
  "s_waitcnt lgkmcnt(3)\n" \
  "v_smfmac_f32_16x16x64_bf16 v[40:43], v[8:11], a[32:39], v107\n" \
  "ds_read_b128 v[24:27], v96 offset:6144\n" \
  "v_smfmac_f32_16x16x64_bf16 v[44:47], v[8:11], a[40:47], v107\n" \
  "ds_read_b128 v[28:31], v96 offset:6160\n" \
  "s_waitcnt lgkmcnt(4)\n" \
  "v_smfmac_f32_16x16x64_bf16 v[40:43], v[12:15], a[48:55], v107\n" \
  "ds_read_b128 v[32:35], v96 offset:6176\n" \
  "v_smfmac_f32_16x16x64_bf16 v[44:47], v[12:15], a[56:63], v107\n" \
  "ds_read_b128 v[36:39], v97\n" \
  "s_waitcnt lgkmcnt(5)\n" \
  "v_smfmac_f32_16x16x64_bf16 v[40:43], v[16:19], a[64:71], v107\n" \
  "s_add_i32 s72, s72, s78\n" \
  "s_add_i32 s72, s72, s80\n" \
  "v_smfmac_f32_16x16x64_bf16 v[44:47], v[16:19], a[72:79], v107\n" \
  "s_add_i32 s72, s72, s82\n" \
  "s_waitcnt lgkmcnt(4)\n" \
  "v_smfmac_f32_16x16x64_bf16 v[40:43], v[20:23], a[80:87], v107\n" \
  "s_lshl_b32 s72, s72, 2\n" \
  "s_sub_i32 s72, s72, s12\n" \
  "v_smfmac_f32_16x16x64_bf16 v[44:47], v[20:23], a[88:95], v107\n" \
  "s_sub_i32 s65, s72, s52\n" \
  "s_waitcnt lgkmcnt(3)\n" \
  "v_smfmac_f32_16x16x64_bf16 v[40:43], v[24:27], a[96:103], v107\n" \
  "s_max_i32 s65, s65, -64\n" \
  "s_min_i32 s65, s65, 64\n" \
  "v_smfmac_f32_16x16x64_bf16 v[44:47], v[24:27], a[104:111], v107\n" \
  "s_ashr_i32 s65, s65, 2\n" \
  "s_waitcnt lgkmcnt(2)\n" \
  "v_smfmac_f32_16x16x64_bf16 v[40:43], v[28:31], a[112:119], v107\n" \
  "s_bitcmp1_b32 s58, 1\n" \
  "s_waitcnt lgkmcnt(1)\n" \
  "v_smfmac_f32_16x16x64_bf16 v[40:43], v[32:35], a[128:135], v107\n" \
  "s_cselect_b32 s65, 0, s65\n" \
  "s_waitcnt lgkmcnt(0)\n" \
  "v_smfmac_f32_16x16x64_bf16 v[40:43], v[36:39], a[144:151], v107\n" \
  "s_add_i32 s46, s46, s65\n" \
  "v_smfmac_f32_16x16x64_bf16 v[44:47], v[28:31], a[120:127], v107\n" \
  "s_add_i32 s52, s72, s46\n" \
  "v_smfmac_f32_16x16x64_bf16 v[44:47], v[32:35], a[136:143], v107\n" \
  "v_smfmac_f32_16x16x64_bf16 v[44:47], v[36:39], a[152:159], v107\n" \
  "v_add_f32 v40, v40, v41\n" \
  "v_smfmac_f32_16x16x64_bf16 v[56:59], v[0:3], a[160:167], v107\n" \
  "v_add_f32 v42, v42, v43\n" \
  "v_smfmac_f32_16x16x64_bf16 v[60:63], v[0:3], a[240:247], v107\n" \
  "s_nop 0\n" \
  "v_add_f32 v44, v44, v45\n" \
  "v_smfmac_f32_16x16x64_bf16 v[56:59], v[4:7], a[168:175], v107\n" \
  "v_add_f32 v46, v46, v47\n" \
  "v_smfmac_f32_16x16x64_bf16 v[60:63], v[4:7], a[248:255], v107\n" \
  "v_cvt_pk_bf16_f32 v44, v40, v44\n" \
  "v_smfmac_f32_16x16x64_bf16 v[56:59], v[8:11], a[176:183], v107\n" \
  "v_cvt_pk_bf16_f32 v46, v42, v46\n" \
  "v_smfmac_f32_16x16x64_bf16 v[60:63], v[8:11], v[112:119], v107\n" \
  "ds_write2_b32 v98, v44, v46 offset1:4\n" \
  "v_smfmac_f32_16x16x64_bf16 v[56:59], v[12:15], a[184:191], v107\n" \
  "ds_read_b128 v[80:83], v100\n" \
  "v_smfmac_f32_16x16x64_bf16 v[60:63], v[12:15], v[120:127], v107\n" \
  "ds_read_b128 v[84:87], v101\n" \
  "v_smfmac_f32_16x16x64_bf16 v[56:59], v[16:19], a[192:199], v107\n" \
  "v_xor_b32 v40, 1024, v92\n" \
  "v_smfmac_f32_16x16x64_bf16 v[60:63], v[16:19], v[128:135], v107\n" \
  "v_xor_b32 v76, 3072, v92\n" \
  "v_smfmac_f32_16x16x64_bf16 v[56:59], v[20:23], a[200:207], v107\n" \
  "s_waitcnt lgkmcnt(1)\n" \
  "v_mfma_f32_16x16x32_bf16 v[88:91], v[48:51], v[80:83], 0\n" \
  "s_waitcnt lgkmcnt(0)\n" \
  "v_mfma_f32_16x16x32_bf16 v[88:91], v[52:55], v[84:87], v[88:91]\n" \
  "s_barrier\n" \
  "v_smfmac_f32_16x16x64_bf16 v[60:63], v[20:23], v[136:143], v107\n" \
  "ds_read_b128 v[80:83], v99 offset:0\n" \
  "v_smfmac_f32_16x16x64_bf16 v[56:59], v[24:27], a[208:215], v107\n" \
  "ds_read_b128 v[48:51], v99 offset:1024\n" \
  "v_smfmac_f32_16x16x64_bf16 v[60:63], v[24:27], v[144:151], v107\n" \
  "ds_write_b64 v92, v[88:89] offset:17664\n" \
  "v_smfmac_f32_16x16x64_bf16 v[56:59], v[28:31], a[216:223], v107\n" \
  "ds_write_b64 v92, v[90:91] offset:17672\n" \
  "v_smfmac_f32_16x16x64_bf16 v[56:59], v[32:35], a[224:231], v107\n" \
  "ds_read_b128 v[84:87], v99 offset:512\n" \
  "v_smfmac_f32_16x16x64_bf16 v[56:59], v[36:39], a[232:239], v107\n" \
  "ds_read_b128 v[52:55], v99 offset:1536\n" \
  "v_smfmac_f32_16x16x64_bf16 v[60:63], v[28:31], v[152:159], v107\n" \
  "v_xor_b32 v44, 2048, v92\n" \
  "v_smfmac_f32_16x16x64_bf16 v[60:63], v[32:35], v[160:167], v107\n" \
  "v_smfmac_f32_16x16x64_bf16 v[60:63], v[36:39], v[168:175], v107\n" \
  "v_add_f32 v56, v56, v57\n" \
  "v_smfmac_f32_16x16x64_bf16 v[64:67], v[0:3], v[176:183], v107\n" \
  "v_add_f32 v58, v58, v59\n" \
  "v_smfmac_f32_16x16x64_bf16 v[64:67], v[4:7], v[184:191], v107\n" \
  "s_nop 0\n" \
  "v_add_f32 v60, v60, v61\n" \
  "v_add_f32 v62, v62, v63\n" \
  "v_cvt_pk_bf16_f32 v56, v56, v60\n" \
  "v_cvt_pk_bf16_f32 v58, v58, v62\n" \
  "v_smfmac_f32_16x16x64_bf16 v[64:67], v[8:11], v[192:199], v107\n" \
  "ds_write2_b32 v102, v56, v58 offset1:8\n" \
  "s_waitcnt lgkmcnt(1)\n" \
  "v_smfmac_f32_16x16x64_bf16 v[64:67], v[12:15], v[200:207], v107\n" \
  "s_barrier\n" \
  "ds_read_b128 v[40:43], v40 offset:17664\n" \
  "v_smfmac_f32_16x16x64_bf16 v[64:67], v[16:19], v[208:215], v107\n" \
  "ds_read_b128 v[44:47], v44 offset:17664\n" \
  "ds_read_b128 v[0:3], v76 offset:17664\n" \
  "v_smfmac_f32_16x16x64_bf16 v[64:67], v[20:23], v[216:223], v107\n" \
  "s_waitcnt lgkmcnt(3)\n" \
  "s_barrier\n" \
  "ds_read_b128 v[56:59], v103 offset:14592\n" \
  "v_smfmac_f32_16x16x64_bf16 v[64:67], v[24:27], v[224:231], v107\n" \
  "ds_read_b128 v[60:63], v103 offset:15616\n" \
  "v_smfmac_f32_16x16x64_bf16 v[64:67], v[28:31], v[232:239], v107\n" \
  "s_waitcnt vmcnt(1) lgkmcnt(4)\n" \
  "v_add_f32 v88, v88, v40\n" \
  "v_add_f32 v89, v89, v41\n" \
  "v_add_f32 v90, v90, v42\n" \
  "v_add_f32 v91, v91, v43\n" \
  "s_waitcnt lgkmcnt(2)\n" \
  "v_pk_add_f32 v[44:45], v[44:45], v[0:1]\n" \
  "v_pk_add_f32 v[46:47], v[46:47], v[2:3]\n" \
  "v_pk_add_f32 v[88:89], v[88:89], v[44:45]\n" \
  "v_pk_add_f32 v[90:91], v[90:91], v[46:47]\n" \
  "s_waitcnt lgkmcnt(1)\n" \
  "v_smfmac_f32_16x16x64_bf16 v[68:71], v[56:59], v[80:87], v107\n" \
  "s_waitcnt lgkmcnt(0)\n" \
  "v_exp_f32 v40, v88\n" \
  "v_smfmac_f32_16x16x64_bf16 v[68:71], v[60:63], v[48:55], v107\n" \
  "v_exp_f32 v41, v89\n" \
  "v_smfmac_f32_16x16x64_bf16 v[64:67], v[32:35], v[240:247], v107\n" \
  "v_exp_f32 v42, v90\n" \
  "v_smfmac_f32_16x16x64_bf16 v[64:67], v[36:39], v[248:255], v107\n" \
  "v_exp_f32 v43, v91\n" \
  "v_add_f32 v68, v68, v69\n" \
  "v_add_f32 v70, v70, v71\n" \
  "v_cvt_pk_bf16_f32 v8, v40, v41\n" \
  "v_cvt_pk_bf16_f32 v9, v42, v43\n" \
  "v_mul_f32 v68, 0x3e0293ee, v68\n" \
  "v_mov_b32_dpp v10, v8 row_ror:8 row_mask:0xf bank_mask:0xf\n" \
  "v_mov_b32_dpp v11, v9 row_ror:8 row_mask:0xf bank_mask:0xf\n" \
  "v_mul_f32 v70, 0x3e0293ee, v70\n" \
  "v_exp_f32 v69, v68\n" \
  "v_mfma_f32_16x16x32_bf16 v[44:47], v[108:111], v[8:11], 0\n" \
  "v_exp_f32 v71, v70\n" \
  "v_mfma_f32_16x16x32_bf16 v[12:15], v[72:75], v[8:11], 0\n" \
  "v_cvt_pk_bf16_f32 v76, v69, v71\n" \
  "v_add_f32 v64, v64, v65\n" \
  "v_add_f32 v66, v66, v67\n" \
  "v_mfma_f32_16x16x16_bf16 v[44:47], v[108:109], v[76:77], v[44:47]\n" \
  "v_cvt_pk_bf16_f32 v78, v64, v66\n" \
  "v_mov_b32 v6, v108\n" \
  "v_mov_b32 v6, v108\n" \
  "v_mfma_f32_16x16x16_bf16 v[12:15], v[78:79], v[76:77], v[12:15]\n" \
  "v_mov_b32 v6, v108\n" \
  "v_mov_b32 v6, v108\n" \
  "v_mov_b32 v6, v108\n" \
  "v_mov_b32 v6, v108\n" \
  "v_subrev_u32 v4, 0x21800000, v44\n" \
  "v_cmpx_gt_u32 vcc, s54, v4\n" \
  "LFIN_%=:\n" \
  "v_rcp_f32 v44, v44\n" \
  "v_mov_b32 v6, v108\n" \
  "v_pk_mul_f32 v[12:13], v[12:13], v[44:45] op_sel_hi:[1,0]\n" \
  "v_pk_mul_f32 v[14:15], v[14:15], v[44:45] op_sel_hi:[1,0]\n" \
  "v_cvt_pk_bf16_f32 v84, v12, v13\n" \
  "v_cvt_pk_bf16_f32 v85, v14, v15\n" \
  "buffer_store_dwordx2 v[84:85], v104, s[0:3], s63 offen sc1\n" \
  "buffer_store_dwordx2 v[84:85], v104, s[0:3], s64 offen sc1\n" \
  "s_mov_b64 exec, -1\n" \
  "s_cmp_lg_u64 vcc, -1\n" \
  "s_cbranch_scc1 LBAD_%=\n" \
  "v_lshrrev_b32 v88, 2, v92\n" \
  "s_and_b32 s65, s28, 256\n" \
  "s_add_i32 s65, s65, s61\n" \
  "v_add_u32 v80, s65, v105\n" \
  "v_add_u32 v81, s61, v105\n" \
  "ds_write_b32 v88, v78 offset:16640\n" \
  "ds_read_b128 v[0:3], v80 offset:0\n" \
  "ds_read_b128 v[4:7], v80 offset:2048\n" \
  "ds_read_b128 v[8:11], v80 offset:4096\n" \
  "ds_read_b128 v[12:15], v80 offset:6144\n" \
  "ds_read_b128 v[16:19], v81 offset:8192\n" \
  "ds_read_b128 v[20:23], v81 offset:8448\n" \
  "ds_read_b128 v[24:27], v81 offset:10240\n" \
  "ds_read_b128 v[28:31], v81 offset:10496\n" \
  "ds_read_b128 v[32:35], v81 offset:12288\n" \
  "ds_read_b128 v[36:39], v81 offset:12544\n" \
  "ds_read_b128 v[40:43], v81 offset:14336\n" \
  "ds_read_b128 v[44:47], v81 offset:14592\n" \
  "v_lshrrev_b32 v89, 5, v103\n" \
  "v_lshlrev_b32 v89, 14, v89\n" \
  "v_lshrrev_b32 v90, 2, v103\n" \
  "v_and_or_b32 v89, v90, s47, v89\n" \
  "s_mov_b32 s62, s63\n" \
  "s_add_i32 s64, s55, s41\n" \
  "s_mov_b64 exec, 63\n" \
  "buffer_load_dword v88, off, s[0:3], s64 sc1\n" \
  "buffer_load_dword v89, v89, s[24:27], s36 offen sc1\n" \
  "s_mov_b64 exec, -1\n" \
  "s_cmp_lg_u32 s70, 1\n" \
  "s_cbranch_scc1 LRbx_%=\n" \
  "s_lshl_b32 s67, s71, 6\n" \
  "s_add_i32 s67, s67, s56\n" \
  "s_add_i32 s67, s67, 2\n" \
  "LRb_%=:\n" \
  "s_cmp_ge_u32 s59, s67\n" \
  "s_cbranch_scc1 LRbx_%=\n" \
  "v_lshrrev_b32 v82, 5, v103\n" \
  "v_lshlrev_b32 v82, 14, v82\n" \
  "v_lshrrev_b32 v83, 2, v103\n" \
  "v_and_or_b32 v82, v83, s47, v82\n" \
  "s_mov_b64 exec, 63\n" \
  "buffer_load_dword v82, v82, s[24:27], s36 offen sc1\n" \
  "s_mov_b64 exec, -1\n" \
  "s_waitcnt vmcnt(0)\n" \
  "v_readlane_b32 s59, v82, 0\n" \
  "v_readlane_b32 s65, v82, 1\n" \
  "v_readlane_b32 s66, v82, 2\n" \
  "s_min_u32 s59, s59, s65\n" \
  "s_min_u32 s59, s59, s66\n" \
  "v_readlane_b32 s65, v82, 3\n" \
  "v_readlane_b32 s66, v82, s43\n" \
  "s_min_u32 s59, s59, s65\n" \
  "s_min_u32 s59, s59, s66\n" \
  "s_cmp_eq_u32 s60, 0\n" \
  "s_cbranch_scc1 LRbx_%=\n" \
  "s_sub_u32 s60, s60, 1\n" \
  "s_branch LRb_%=\n" \
  "LRbx_%=:\n" \
  "s_add_i32 m0, s69, 0\n" \
  "s_add_i32 s65, s68, 0\n" \
  "buffer_load_dwordx4 v92, s[4:7], s65 offen sc1 lds\n" \
  "s_and_b32 s66, s28, 512\n" \
  "s_add_i32 s65, s66, s61\n" \
  "v_add_u32 v82, s65, v106\n" \
  "v_lshrrev_b32 v83, 2, v103\n" \
  "v_add_u32 v83, s66, v83\n" \
  "v_mov_b64 v[68:69], 0\n" \
  "v_mov_b64 v[70:71], 0\n" \
  "v_mov_b32 v85, 0\n" \
  "s_add_i32 m0, s69, 4096\n" \
  "s_add_i32 s65, s68, 4096\n" \
  "buffer_load_dwordx4 v92, s[4:7], s65 offen sc1 lds\n" \
  "s_waitcnt lgkmcnt(0)\n" \
  "s_barrier\n" \
  "v_smfmac_f32_16x16x64_bf16 v[68:71], v[56:59], v[0:7], v107\n" \
  "ds_read_b128 v[64:67], v82 offset:16384\n" \
  "v_smfmac_f32_16x16x64_bf16 v[68:71], v[60:63], v[8:15], v107\n" \
  "ds_read_b128 v[72:75], v82 offset:16640\n" \
  "v_mfma_f32_16x16x32_bf16 v[48:51], v[16:19], v[0:3], 0\n" \
  "ds_read_b32 v78, v83 offset:16640\n" \
  "v_mfma_f32_16x16x32_bf16 v[52:55], v[20:23], v[0:3], 0\n" \
  "ds_read_b32 v84, v83 offset:16896\n" \
  "v_mfma_f32_16x16x32_bf16 v[48:51], v[24:27], v[4:7], v[48:51]\n" \
  "s_add_i32 m0, s69, 8192\n" \
  "s_add_i32 s65, s68, 8192\n" \
  "buffer_load_dwordx4 v92, s[4:7], s65 offen sc1 lds\n" \
  "v_mfma_f32_16x16x32_bf16 v[52:55], v[28:31], v[4:7], v[52:55]\n" \
  "v_mfma_f32_16x16x32_bf16 v[48:51], v[32:35], v[8:11], v[48:51]\n" \
  "v_add_f32 v68, v68, v69\n" \
  "v_add_f32 v70, v70, v71\n" \
  "v_mfma_f32_16x16x32_bf16 v[52:55], v[36:39], v[8:11], v[52:55]\n" \
  "v_mul_f32 v68, 0x3e0293ee, v68\n" \
  "v_mul_f32 v70, 0x3e0293ee, v70\n" \
  "v_mfma_f32_16x16x32_bf16 v[48:51], v[40:43], v[12:15], v[48:51]\n" \
  "v_mov_b32 v16, 0xff800000\n" \
  "v_mfma_f32_16x16x32_bf16 v[52:55], v[44:47], v[12:15], v[52:55]\n" \
  "s_add_i32 m0, s69, 12288\n" \
  "s_add_i32 s65, s68, 12288\n" \
  "buffer_load_dwordx4 v92, s[4:7], s65 offen sc1 lds\n" \
  "s_nop 1\n" \
  "v_cndmask_b32 v48, v16, v48, s[84:85]\n" \
  "v_cndmask_b32 v49, v16, v49, s[86:87]\n" \
  "v_cndmask_b32 v50, v16, v50, s[88:89]\n" \
  "v_cndmask_b32 v51, v16, v51, s[90:91]\n" \
  "v_max3_f32 v17, v68, v70, v48\n" \
  "v_max3_f32 v17, v17, v49, v50\n" \
  "v_cndmask_b32 v52, v16, v52, s[92:93]\n" \
  "v_cndmask_b32 v53, v16, v53, s[94:95]\n" \
  "v_cndmask_b32 v54, v16, v54, s[96:97]\n" \
  "v_cndmask_b32 v55, v16, v55, s[98:99]\n" \
  "v_max3_f32 v17, v17, v51, v52\n" \
  "v_max3_f32 v17, v17, v53, v54\n" \
  "v_max_f32 v17, v17, v55\n" \
  "v_mov_b32 v18, v17\n" \
  "s_nop 1\n" \
  "v_permlane16_swap_b32 v17, v18\n" \
  "v_max_f32 v17, v17, v18\n" \
  "v_mov_b32 v18, v17\n" \
  "s_nop 1\n" \
  "v_permlane32_swap_b32 v17, v18\n" \
  "v_max_f32 v17, v17, v18\n" \
  "v_sub_f32 v48, v48, v17\n" \
  "v_sub_f32 v49, v49, v17\n" \
  "v_sub_f32 v50, v50, v17\n" \
  "v_sub_f32 v51, v51, v17\n" \
  "v_sub_f32 v52, v52, v17\n" \
  "v_sub_f32 v53, v53, v17\n" \
  "v_sub_f32 v54, v54, v17\n" \
  "v_sub_f32 v55, v55, v17\n" \
  "v_sub_f32 v68, v68, v17\n" \
  "v_sub_f32 v70, v70, v17\n" \
  "s_add_i32 m0, s69, 16384\n" \
  "s_add_i32 s65, s68, s30\n" \
  "buffer_load_dwordx4 v92, s[4:7], s65 offen sc1 lds\n" \
  "v_exp_f32 v48, v48\n" \
  "v_exp_f32 v49, v49\n" \
  "v_exp_f32 v50, v50\n" \
  "v_exp_f32 v51, v51\n" \
  "v_exp_f32 v52, v52\n" \
  "v_exp_f32 v53, v53\n" \
  "v_exp_f32 v54, v54\n" \
  "v_exp_f32 v55, v55\n" \
  "v_exp_f32 v68, v68\n" \
  "v_exp_f32 v70, v70\n" \
  "s_lshl_b32 s67, s71, 6\n" \
  "s_add_i32 s67, s67, s56\n" \
  "v_mov_b32 v18, s67\n" \
  "v_lshlrev_b32 v19, 18, v92\n" \
  "buffer_store_dword v18, v19, s[24:27], s39 offen sc1\n" \
  "v_cvt_pk_bf16_f32 v20, v48, v49\n" \
  "v_cvt_pk_bf16_f32 v21, v50, v51\n" \
  "v_cvt_pk_bf16_f32 v22, v52, v53\n" \
  "v_cvt_pk_bf16_f32 v23, v54, v55\n" \
  "v_cvt_pk_bf16_f32 v76, v68, v70\n" \
  "s_waitcnt lgkmcnt(0)\n" \
  "v_mfma_f32_16x16x32_bf16 v[24:27], v[108:111], v[20:23], 0\n" \
  "v_mfma_f32_16x16x32_bf16 v[28:31], v[64:67], v[20:23], 0\n" \
  "v_mfma_f32_16x16x32_bf16 v[32:35], v[72:75], v[20:23], 0\n" \
  "v_mfma_f32_16x16x16_bf16 v[24:27], v[108:109], v[76:77], v[24:27]\n" \
  "v_mfma_f32_16x16x16_bf16 v[28:31], v[78:79], v[76:77], v[28:31]\n" \
  "v_mfma_f32_16x16x16_bf16 v[32:35], v[84:85], v[76:77], v[32:35]\n" \
  "s_lshl_b32 s67, s56, 18\n" \
  "s_add_i32 s67, s67, s31\n" \
  "v_rcp_f32 v24, v24\n" \
  "s_nop 0\n" \
  "v_mul_f32 v28, v28, v24\n" \
  "v_mul_f32 v29, v29, v24\n" \
  "v_mul_f32 v30, v30, v24\n" \
  "v_mul_f32 v31, v31, v24\n" \
  "v_mul_f32 v32, v32, v24\n" \
  "v_mul_f32 v33, v33, v24\n" \
  "v_mul_f32 v34, v34, v24\n" \
  "v_mul_f32 v35, v35, v24\n" \
  "v_cvt_pk_bf16_f32 v28, v28, v29\n" \
  "v_cvt_pk_bf16_f32 v29, v30, v31\n" \
  "v_cvt_pk_bf16_f32 v30, v32, v33\n" \
  "v_cvt_pk_bf16_f32 v31, v34, v35\n" \
  "buffer_store_dwordx4 v[28:31], v103, s[8:11], s67 offen sc1\n" \
  "s_waitcnt vmcnt(7)\n" \
  "v_readfirstlane_b32 s68, v88\n" \
  "v_readlane_b32 s67, v89, 0\n" \
  "v_readlane_b32 s65, v89, 1\n" \
  "v_readlane_b32 s66, v89, 2\n" \
  "s_min_u32 s67, s67, s65\n" \
  "s_min_u32 s67, s67, s66\n" \
  "v_readlane_b32 s65, v89, 3\n" \
  "v_readlane_b32 s66, v89, s43\n" \
  "s_min_u32 s67, s67, s65\n" \
  "s_min_u32 s67, s67, s66\n" \
  "s_max_u32 s59, s59, s67\n" \
  "s_cmp_eq_u32 s68, s53\n" \
  "s_cselect_b32 s58, s58, s68\n" \
  "s_waitcnt vmcnt(2)\n" \
  "s_cmp_eq_u32 s56, 63\n" \
  "s_cbranch_scc1 LEND_%=\n" \
  "s_bitcmp1_b32 s58, 1\n" \
  "s_cselect_b32 s65, s42, 0\n" \
  "s_add_i32 s58, s58, s65\n" \
  "s_add_i32 s58, s58, s32\n" \
  "s_and_b32 s58, s58, -4\n" \
  "s_add_i32 s56, s56, 1\n" \
  "s_add_i32 s57, s57, 1\n" \
  "s_xor_b32 s61, s61, 61440\n" \
  "s_branch LSTEP_%=\n" \
  "LBAD_%=:\n" \
  "s_not_b64 s[76:77], vcc\n" \
  "ds_read_b128 v[0:3], v96 offset:0\n" \
  "ds_read_b128 v[4:7], v96 offset:16\n" \
  "ds_read_b128 v[8:11], v96 offset:32\n" \
  "ds_read_b128 v[12:15], v96 offset:3072\n" \
  "ds_read_b128 v[16:19], v96 offset:3088\n" \
  "ds_read_b128 v[20:23], v96 offset:3104\n" \
  "ds_read_b128 v[24:27], v96 offset:6144\n" \
  "ds_read_b128 v[28:31], v96 offset:6160\n" \
  "ds_read_b128 v[32:35], v96 offset:6176\n" \
  "ds_read_b128 v[36:39], v97\n" \
  "s_waitcnt lgkmcnt(0)\n" \
  "v_xor_b32 v0, s53, v0\n" \
  "v_xor_b32 v1, s53, v1\n" \
  "v_xor_b32 v2, s53, v2\n" \
  "v_xor_b32 v3, s53, v3\n" \
  "v_xor_b32 v4, s53, v4\n" \
  "v_xor_b32 v5, s53, v5\n" \
  "v_xor_b32 v6, s53, v6\n" \
  "v_xor_b32 v7, s53, v7\n" \
  "v_xor_b32 v8, s53, v8\n" \
  "v_xor_b32 v9, s53, v9\n" \
  "v_xor_b32 v10, s53, v10\n" \
  "v_xor_b32 v11, s53, v11\n" \
  "v_xor_b32 v12, s53, v12\n" \
  "v_xor_b32 v13, s53, v13\n" \
  "v_xor_b32 v14, s53, v14\n" \
  "v_xor_b32 v15, s53, v15\n" \
  "v_xor_b32 v16, s53, v16\n" \
  "v_xor_b32 v17, s53, v17\n" \
  "v_xor_b32 v18, s53, v18\n" \
  "v_xor_b32 v19, s53, v19\n" \
  "v_xor_b32 v20, s53, v20\n" \
  "v_xor_b32 v21, s53, v21\n" \
  "v_xor_b32 v22, s53, v22\n" \
  "v_xor_b32 v23, s53, v23\n" \
  "v_xor_b32 v24, s53, v24\n" \
  "v_xor_b32 v25, s53, v25\n" \
  "v_xor_b32 v26, s53, v26\n" \
  "v_xor_b32 v27, s53, v27\n" \
  "v_xor_b32 v28, s53, v28\n" \
  "v_xor_b32 v29, s53, v29\n" \
  "v_xor_b32 v30, s53, v30\n" \
  "v_xor_b32 v31, s53, v31\n" \
  "v_xor_b32 v32, s53, v32\n" \
  "v_xor_b32 v33, s53, v33\n" \
  "v_xor_b32 v34, s53, v34\n" \
  "v_xor_b32 v35, s53, v35\n" \
  "v_xor_b32 v36, s53, v36\n" \
  "v_xor_b32 v37, s53, v37\n" \
  "v_xor_b32 v38, s53, v38\n" \
  "v_xor_b32 v39, s53, v39\n" \
  "v_min_u32 v0, v0, v1\n" \
  "v_min3_u32 v0, v0, v2, v3\n" \
  "v_min3_u32 v0, v0, v4, v5\n" \
  "v_min3_u32 v0, v0, v6, v7\n" \
  "v_min3_u32 v0, v0, v8, v9\n" \
  "v_min3_u32 v0, v0, v10, v11\n" \
  "v_min3_u32 v0, v0, v12, v13\n" \
  "v_min3_u32 v0, v0, v14, v15\n" \
  "v_min3_u32 v0, v0, v16, v17\n" \
  "v_min3_u32 v0, v0, v18, v19\n" \
  "v_min3_u32 v0, v0, v20, v21\n" \
  "v_min3_u32 v0, v0, v22, v23\n" \
  "v_min3_u32 v0, v0, v24, v25\n" \
  "v_min3_u32 v0, v0, v26, v27\n" \
  "v_min3_u32 v0, v0, v28, v29\n" \
  "v_min3_u32 v0, v0, v30, v31\n" \
  "v_min3_u32 v0, v0, v32, v33\n" \
  "v_min3_u32 v0, v0, v34, v35\n" \
  "v_min3_u32 v0, v0, v36, v37\n" \
  "v_min3_u32 v0, v0, v38, v39\n" \
  "v_cmp_eq_u32 vcc, 0, v0\n" \
  "s_cbranch_vccz LBIG_%=\n" \
  "s_cmp_eq_u32 s60, 0\n" \
  "s_cbranch_scc1 LDEADCHK_%=\n" \
  "LPOLL_%=:\n" \
  "buffer_load_dwordx4 v[0:3], v93, s[0:3], s62 offen sc1\n" \
  "buffer_load_dwordx4 v[4:7], v94, s[0:3], s62 offen sc1\n" \
  "buffer_load_dwordx4 v[8:11], v95, s[0:3], s62 offen sc1\n" \
  "s_sub_u32 s60, s60, 1\n" \
  "s_waitcnt vmcnt(0)\n" \
  "v_xor_b32 v0, s53, v0\n" \
  "v_xor_b32 v1, s53, v1\n" \
  "v_xor_b32 v2, s53, v2\n" \
  "v_xor_b32 v3, s53, v3\n" \
  "v_xor_b32 v4, s53, v4\n" \
  "v_xor_b32 v5, s53, v5\n" \
  "v_xor_b32 v6, s53, v6\n" \
  "v_xor_b32 v7, s53, v7\n" \
  "v_xor_b32 v8, s53, v8\n" \
  "v_xor_b32 v9, s53, v9\n" \
  "v_xor_b32 v10, s53, v10\n" \
  "v_xor_b32 v11, s53, v11\n" \
  "v_min_u32 v0, v0, v1\n" \
  "v_min3_u32 v0, v0, v2, v3\n" \
  "v_min3_u32 v0, v0, v4, v5\n" \
  "v_min3_u32 v0, v0, v6, v7\n" \
  "v_min3_u32 v0, v0, v8, v9\n" \
  "v_min3_u32 v0, v0, v10, v11\n" \
  "v_cmp_eq_u32 vcc, 0, v0\n" \
  "s_cbranch_vccz LDEADCHK_%=\n" \
  "s_cmp_lg_u32 s60, 0\n" \
  "s_cbranch_scc1 LPOLL_%=\n" \
  "LDEADCHK_%=:\n" \
  "v_mov_b32 v0, s60\n" \
  "v_mov_b32 v1, s28\n" \
  "ds_write_b32 v1, v0 offset:16640\n" \
  "s_waitcnt lgkmcnt(0)\n" \
  "s_barrier\n" \
  "v_mov_b32 v1, 0\n" \
  "ds_read2st64_b32 v[2:3], v1 offset0:65 offset1:66\n" \
  "ds_read2st64_b32 v[4:5], v1 offset0:67 offset1:68\n" \
  "s_memtime s[72:73]\n" \
  "s_waitcnt lgkmcnt(0)\n" \
  "v_min3_u32 v2, v2, v3, v4\n" \
  "v_min_u32 v2, v2, v5\n" \
  "s_add_i32 s58, s72, s52\n" \
  "v_readfirstlane_b32 s60, v2\n" \
  "s_and_b32 s58, s58, -4\n" \
  "s_add_i32 s58, s58, 302\n" \
  "s_lshl_b32 s67, s28, 2\n" \
  "s_cmp_lg_u32 s60, 0\n" \
  "s_cbranch_scc1 LAGAIN_%=\n" \
  "s_endpgm\n" \
  "LBIG_%=:\n" \
  "s_nop 7\n" \
  "v_max3_f32 v48, v88, v89, v90\n" \
  "v_max3_f32 v49, v91, v68, v70\n" \
  "v_max_f32 v48, v48, v49\n" \
  "s_nop 1\n" \
  "v_mov_b32_dpp v49, v48 row_ror:8 row_mask:0xf bank_mask:0xf\n" \
  "v_max_f32 v48, v48, v49\n" \
  "v_mov_b32 v49, v48\n" \
  "s_nop 1\n" \
  "v_permlane16_swap_b32 v48, v49\n" \
  "v_max_f32 v48, v48, v49\n" \
  "v_mov_b32 v49, v48\n" \
  "s_nop 1\n" \
  "v_permlane32_swap_b32 v48, v49\n" \
  "v_max_f32 v48, v48, v49\n" \
  "v_sub_f32 v88, v88, v48\n" \
  "v_sub_f32 v89, v89, v48\n" \
  "v_sub_f32 v90, v90, v48\n" \
  "v_sub_f32 v91, v91, v48\n" \
  "v_sub_f32 v68, v68, v48\n" \
  "v_sub_f32 v70, v70, v48\n" \
  "v_exp_f32 v88, v88\n" \
  "v_exp_f32 v89, v89\n" \
  "v_exp_f32 v90, v90\n" \
  "v_exp_f32 v91, v91\n" \
  "v_exp_f32 v68, v68\n" \
  "v_exp_f32 v70, v70\n" \
  "v_cvt_pk_bf16_f32 v8, v88, v89\n" \
  "v_cvt_pk_bf16_f32 v9, v90, v91\n" \
  "v_cvt_pk_bf16_f32 v76, v68, v70\n" \
  "v_mov_b32_dpp v10, v8 row_ror:8 row_mask:0xf bank_mask:0xf\n" \
  "v_mov_b32_dpp v11, v9 row_ror:8 row_mask:0xf bank_mask:0xf\n" \
  "s_nop 1\n" \
  "v_mfma_f32_16x16x32_bf16 v[44:47], v[108:111], v[8:11], 0\n" \
  "v_mfma_f32_16x16x32_bf16 v[12:15], v[72:75], v[8:11], 0\n" \
  "v_mfma_f32_16x16x16_bf16 v[44:47], v[108:109], v[76:77], v[44:47]\n" \
  "v_mfma_f32_16x16x16_bf16 v[12:15], v[78:79], v[76:77], v[12:15]\n" \
  "s_nop 9\n" \
  "s_mov_b64 vcc, -1\n" \
  "s_mov_b64 exec, s[76:77]\n" \
  "s_branch LFIN_%=\n" \
  "LEND_%=:\n" \
  "s_add_i32 s71, s71, 1\n" \
  "s_lshl_b32 s65, s71, 6\n" \
  "v_mov_b32 v0, s65\n" \
  "s_waitcnt vmcnt(0)\n" \
  "s_barrier\n" \
  "s_cmp_lg_u32 s37, 0\n" \
  "s_cbranch_scc1 LE2_%=\n" \
  "s_mov_b64 exec, 1\n" \
  "buffer_store_dword v0, off, s[24:27], s39 sc1\n" \
  "s_mov_b64 exec, -1\n" \
  "LE2_%=:\n" \
  "s_cmp_lt_u32 s71, s35\n" \
  "s_cbranch_scc1 LLAYER_%=\n" \
  "s_endpgm\n"
#define Y_ASM0 \
  "v_mbcnt_lo_u32_b32 v0, -1, 0\n" \
  "v_mbcnt_hi_u32_b32 v0, -1, v0\n" \
  "v_lshlrev_b32 v0, 2, v0\n" \
  "v_add_u32 v0, %0, v0\n" \
  "v_add_u32 v0, 146432, v0\n" \
  "ds_read_b32 v0, v0\n" \
  "s_waitcnt lgkmcnt(0)\n" \
  "v_readlane_b32 s0, v0, 0\n" \
  "v_readlane_b32 s1, v0, 1\n" \
  "v_readlane_b32 s2, v0, 2\n" \
  "v_readlane_b32 s3, v0, 3\n" \
  "v_readlane_b32 s4, v0, 4\n" \
  "v_readlane_b32 s5, v0, 5\n" \
  "v_readlane_b32 s6, v0, 6\n" \
  "v_readlane_b32 s7, v0, 7\n" \
  "v_readlane_b32 s8, v0, 8\n" \
  "v_readlane_b32 s9, v0, 9\n" \
  "v_readlane_b32 s10, v0, 10\n" \
  "v_readlane_b32 s11, v0, 11\n" \
  "v_readlane_b32 s12, v0, 12\n" \
  "v_readlane_b32 s13, v0, 13\n" \
  "v_readlane_b32 s14, v0, 14\n" \
  "v_readlane_b32 s15, v0, 15\n" \
  "v_readlane_b32 s16, v0, 16\n" \
  "v_readlane_b32 s17, v0, 17\n" \
  "v_readlane_b32 s18, v0, 18\n" \
  "v_readlane_b32 s19, v0, 19\n" \
  "v_readlane_b32 s20, v0, 20\n" \
  "s_nop 4\n" \
  "v_mbcnt_lo_u32_b32 v0, -1, 0\n" \
  "v_mbcnt_hi_u32_b32 v0, -1, v0\n" \
  "v_lshlrev_b32 v0, 2, v0\n" \
  "v_add_u32 v0, s12, v0\n" \
  "ds_read_b32 v100, v0 offset:16384\n" \
  "ds_read_b32 v101, v0 offset:17408\n" \
  "ds_read_b32 v102, v0 offset:18432\n" \
  "ds_read_b32 v103, v0 offset:19456\n" \
  "ds_read_b32 v104, v0 offset:20480\n" \
  "ds_read_b32 v105, v0 offset:21504\n" \
  "ds_read_b32 v106, v0 offset:22528\n" \
  "ds_read_b32 v107, v0 offset:23552\n" \
  "ds_read_b32 v108, v0 offset:24576\n" \
  "ds_read_b32 v109, v0 offset:25600\n" \
  "ds_read_b32 v110, v0 offset:26624\n" \
  "ds_read_b32 v111, v0 offset:27648\n" \
  "s_waitcnt lgkmcnt(0)\n" \
  "s_mov_b32 s63, 0x05040100\n" \
  "s_mov_b32 s64, 0x07060302\n" \
  "s_mov_b32 s61, 0\n" \
  "buffer_load_dword v0, v107, s[4:7], s61 offen offset:0\n" \
  "buffer_load_ushort v2, v108, s[4:7], s61 offen offset:0\n" \
  "buffer_load_dword v1, v107, s[4:7], s61 offen offset:1280\n" \
  "buffer_load_ushort v3, v108, s[4:7], s61 offen offset:1280\n" \
  "s_mov_b32 s61, 10240\n" \
  "buffer_load_dword v4, v107, s[4:7], s61 offen offset:0\n" \
  "buffer_load_ushort v6, v108, s[4:7], s61 offen offset:0\n" \
  "buffer_load_dword v5, v107, s[4:7], s61 offen offset:1280\n" \
  "buffer_load_ushort v7, v108, s[4:7], s61 offen offset:1280\n" \
  "s_mov_b32 s61, 2560\n" \
  "buffer_load_dword v8, v107, s[4:7], s61 offen offset:0\n" \
  "buffer_load_ushort v10, v108, s[4:7], s61 offen offset:0\n" \
  "buffer_load_dword v9, v107, s[4:7], s61 offen offset:1280\n" \
  "buffer_load_ushort v11, v108, s[4:7], s61 offen offset:1280\n" \
  "s_mov_b32 s61, 12800\n" \
  "buffer_load_dword v12, v107, s[4:7], s61 offen offset:0\n" \
  "buffer_load_ushort v14, v108, s[4:7], s61 offen offset:0\n" \
  "buffer_load_dword v13, v107, s[4:7], s61 offen offset:1280\n" \
  "buffer_load_ushort v15, v108, s[4:7], s61 offen offset:1280\n" \
  "s_mov_b32 s61, 40960\n" \
  "buffer_load_dword v16, v107, s[4:7], s61 offen offset:0\n" \
  "buffer_load_ushort v18, v108, s[4:7], s61 offen offset:0\n" \
  "buffer_load_dword v17, v107, s[4:7], s61 offen offset:1280\n" \
  "buffer_load_ushort v19, v108, s[4:7], s61 offen offset:1280\n" \
  "s_mov_b32 s61, 51200\n" \
  "buffer_load_dword v20, v107, s[4:7], s61 offen offset:0\n" \
  "buffer_load_ushort v22, v108, s[4:7], s61 offen offset:0\n" \
  "buffer_load_dword v21, v107, s[4:7], s61 offen offset:1280\n" \
  "buffer_load_ushort v23, v108, s[4:7], s61 offen offset:1280\n" \
  "s_mov_b32 s61, 43520\n" \
  "buffer_load_dword v24, v107, s[4:7], s61 offen offset:0\n" \
  "buffer_load_ushort v26, v108, s[4:7], s61 offen offset:0\n" \
  "buffer_load_dword v25, v107, s[4:7], s61 offen offset:1280\n" \
  "buffer_load_ushort v27, v108, s[4:7], s61 offen offset:1280\n" \
  "s_mov_b32 s61, 53760\n" \
  "buffer_load_dword v28, v107, s[4:7], s61 offen offset:0\n" \
  "buffer_load_ushort v30, v108, s[4:7], s61 offen offset:0\n" \
  "buffer_load_dword v29, v107, s[4:7], s61 offen offset:1280\n" \
  "buffer_load_ushort v31, v108, s[4:7], s61 offen offset:1280\n" \
  "s_mov_b32 s61, 163840\n" \
  "buffer_load_dword v32, v107, s[4:7], s61 offen offset:0\n" \
  "buffer_load_ushort v34, v108, s[4:7], s61 offen offset:0\n" \
  "buffer_load_dword v33, v107, s[4:7], s61 offen offset:1280\n" \
  "buffer_load_ushort v35, v108, s[4:7], s61 offen offset:1280\n" \
  "s_mov_b32 s61, 174080\n" \
  "buffer_load_dword v36, v107, s[4:7], s61 offen offset:0\n" \
  "buffer_load_ushort v38, v108, s[4:7], s61 offen offset:0\n" \
  "buffer_load_dword v37, v107, s[4:7], s61 offen offset:1280\n" \
  "buffer_load_ushort v39, v108, s[4:7], s61 offen offset:1280\n" \
  "s_mov_b32 s61, 166400\n" \
  "buffer_load_dword v40, v107, s[4:7], s61 offen offset:0\n" \
  "buffer_load_ushort v42, v108, s[4:7], s61 offen offset:0\n" \
  "buffer_load_dword v41, v107, s[4:7], s61 offen offset:1280\n" \
  "buffer_load_ushort v43, v108, s[4:7], s61 offen offset:1280\n" \
  "s_mov_b32 s61, 176640\n" \
  "buffer_load_dword v44, v107, s[4:7], s61 offen offset:0\n" \
  "buffer_load_ushort v46, v108, s[4:7], s61 offen offset:0\n" \
  "buffer_load_dword v45, v107, s[4:7], s61 offen offset:1280\n" \
  "buffer_load_ushort v47, v108, s[4:7], s61 offen offset:1280\n" \
  "s_waitcnt vmcnt(32)\n" \
  "s_mov_b32 s61, 204800\n" \
  "buffer_load_dword v48, v107, s[4:7], s61 offen offset:0\n" \
  "buffer_load_ushort v50, v108, s[4:7], s61 offen offset:0\n" \
  "buffer_load_dword v49, v107, s[4:7], s61 offen offset:1280\n" \
  "buffer_load_ushort v51, v108, s[4:7], s61 offen offset:1280\n" \
  "s_mov_b32 s61, 215040\n" \
  "buffer_load_dword v52, v107, s[4:7], s61 offen offset:0\n" \
  "buffer_load_ushort v54, v108, s[4:7], s61 offen offset:0\n" \
  "buffer_load_dword v53, v107, s[4:7], s61 offen offset:1280\n" \
  "buffer_load_ushort v55, v108, s[4:7], s61 offen offset:1280\n" \
  "s_mov_b32 s61, 207360\n" \
  "buffer_load_dword v56, v107, s[4:7], s61 offen offset:0\n" \
  "buffer_load_ushort v58, v108, s[4:7], s61 offen offset:0\n" \
  "buffer_load_dword v57, v107, s[4:7], s61 offen offset:1280\n" \
  "buffer_load_ushort v59, v108, s[4:7], s61 offen offset:1280\n" \
  "s_mov_b32 s61, 217600\n" \
  "buffer_load_dword v60, v107, s[4:7], s61 offen offset:0\n" \
  "buffer_load_ushort v62, v108, s[4:7], s61 offen offset:0\n" \
  "buffer_load_dword v61, v107, s[4:7], s61 offen offset:1280\n" \
  "buffer_load_ushort v63, v108, s[4:7], s61 offen offset:1280\n" \
  "v_perm_b32 v72, v1, v0, s63\n" \
  "v_accvgpr_write_b32 a0, v72\n" \
  "v_perm_b32 v72, v1, v0, s64\n" \
  "v_accvgpr_write_b32 a8, v72\n" \
  "v_perm_b32 v72, v3, v2, s63\n" \
  "v_accvgpr_write_b32 a16, v72\n" \
  "v_perm_b32 v72, v5, v4, s63\n" \
  "v_accvgpr_write_b32 a1, v72\n" \
  "v_perm_b32 v72, v5, v4, s64\n" \
  "v_accvgpr_write_b32 a9, v72\n" \
  "v_perm_b32 v72, v7, v6, s63\n" \
  "v_accvgpr_write_b32 a17, v72\n" \
  "v_perm_b32 v72, v9, v8, s63\n" \
  "v_accvgpr_write_b32 a2, v72\n" \
  "v_perm_b32 v72, v9, v8, s64\n" \
  "v_accvgpr_write_b32 a10, v72\n" \
  "v_perm_b32 v72, v11, v10, s63\n" \
  "v_accvgpr_write_b32 a18, v72\n" \
  "v_perm_b32 v72, v13, v12, s63\n" \
  "v_accvgpr_write_b32 a3, v72\n" \
  "v_perm_b32 v72, v13, v12, s64\n" \
  "v_accvgpr_write_b32 a11, v72\n" \
  "v_perm_b32 v72, v15, v14, s63\n" \
  "v_accvgpr_write_b32 a19, v72\n" \
  "s_waitcnt vmcnt(32)\n" \
  "s_mov_b32 s61, 327680\n" \
  "buffer_load_dword v0, v107, s[4:7], s61 offen offset:0\n" \
  "buffer_load_ushort v2, v108, s[4:7], s61 offen offset:0\n" \
  "buffer_load_dword v1, v107, s[4:7], s61 offen offset:1280\n" \
  "buffer_load_ushort v3, v108, s[4:7], s61 offen offset:1280\n" \
  "s_mov_b32 s61, 337920\n" \
  "buffer_load_dword v4, v107, s[4:7], s61 offen offset:0\n" \
  "buffer_load_ushort v6, v108, s[4:7], s61 offen offset:0\n" \
  "buffer_load_dword v5, v107, s[4:7], s61 offen offset:1280\n" \
  "buffer_load_ushort v7, v108, s[4:7], s61 offen offset:1280\n" \
  "s_mov_b32 s61, 330240\n" \
  "buffer_load_dword v8, v107, s[4:7], s61 offen offset:0\n" \
  "buffer_load_ushort v10, v108, s[4:7], s61 offen offset:0\n" \
  "buffer_load_dword v9, v107, s[4:7], s61 offen offset:1280\n" \
  "buffer_load_ushort v11, v108, s[4:7], s61 offen offset:1280\n" \
  "s_mov_b32 s61, 340480\n" \
  "buffer_load_dword v12, v107, s[4:7], s61 offen offset:0\n" \
  "buffer_load_ushort v14, v108, s[4:7], s61 offen offset:0\n" \
  "buffer_load_dword v13, v107, s[4:7], s61 offen offset:1280\n" \
  "buffer_load_ushort v15, v108, s[4:7], s61 offen offset:1280\n" \
  "v_perm_b32 v72, v17, v16, s63\n" \
  "v_accvgpr_write_b32 a4, v72\n" \
  "v_perm_b32 v72, v17, v16, s64\n" \
  "v_accvgpr_write_b32 a12, v72\n" \
  "v_perm_b32 v72, v19, v18, s63\n" \
  "v_accvgpr_write_b32 a20, v72\n" \
  "v_perm_b32 v72, v21, v20, s63\n" \
  "v_accvgpr_write_b32 a5, v72\n" \
  "v_perm_b32 v72, v21, v20, s64\n" \
  "v_accvgpr_write_b32 a13, v72\n" \
  "v_perm_b32 v72, v23, v22, s63\n" \
  "v_accvgpr_write_b32 a21, v72\n" \
  "v_perm_b32 v72, v25, v24, s63\n" \
  "v_accvgpr_write_b32 a6, v72\n" \
  "v_perm_b32 v72, v25, v24, s64\n" \
  "v_accvgpr_write_b32 a14, v72\n" \
  "v_perm_b32 v72, v27, v26, s63\n" \
  "v_accvgpr_write_b32 a22, v72\n" \
  "v_perm_b32 v72, v29, v28, s63\n" \
  "v_accvgpr_write_b32 a7, v72\n" \
  "v_perm_b32 v72, v29, v28, s64\n" \
  "v_accvgpr_write_b32 a15, v72\n" \
  "v_perm_b32 v72, v31, v30, s63\n" \
  "v_accvgpr_write_b32 a23, v72\n" \
  "s_waitcnt vmcnt(32)\n" \
  "s_mov_b32 s61, 368640\n" \
  "buffer_load_dword v16, v107, s[4:7], s61 offen offset:0\n" \
  "buffer_load_ushort v18, v108, s[4:7], s61 offen offset:0\n" \
  "buffer_load_dword v17, v107, s[4:7], s61 offen offset:1280\n" \
  "buffer_load_ushort v19, v108, s[4:7], s61 offen offset:1280\n" \
  "s_mov_b32 s61, 378880\n" \
  "buffer_load_dword v20, v107, s[4:7], s61 offen offset:0\n" \
  "buffer_load_ushort v22, v108, s[4:7], s61 offen offset:0\n" \
  "buffer_load_dword v21, v107, s[4:7], s61 offen offset:1280\n" \
  "buffer_load_ushort v23, v108, s[4:7], s61 offen offset:1280\n" \
  "s_mov_b32 s61, 371200\n" \
  "buffer_load_dword v24, v107, s[4:7], s61 offen offset:0\n" \
  "buffer_load_ushort v26, v108, s[4:7], s61 offen offset:0\n" \
  "buffer_load_dword v25, v107, s[4:7], s61 offen offset:1280\n" \
  "buffer_load_ushort v27, v108, s[4:7], s61 offen offset:1280\n" \
  "s_mov_b32 s61, 381440\n" \
  "buffer_load_dword v28, v107, s[4:7], s61 offen offset:0\n" \
  "buffer_load_ushort v30, v108, s[4:7], s61 offen offset:0\n" \
  "buffer_load_dword v29, v107, s[4:7], s61 offen offset:1280\n" \
  "buffer_load_ushort v31, v108, s[4:7], s61 offen offset:1280\n" \
  "v_perm_b32 v72, v33, v32, s63\n" \
  "v_accvgpr_write_b32 a24, v72\n" \
  "v_perm_b32 v72, v33, v32, s64\n" \
  "v_accvgpr_write_b32 a32, v72\n" \
  "v_perm_b32 v72, v35, v34, s63\n" \
  "v_accvgpr_write_b32 a40, v72\n" \
  "v_perm_b32 v72, v37, v36, s63\n" \
  "v_accvgpr_write_b32 a25, v72\n" \
  "v_perm_b32 v72, v37, v36, s64\n" \
  "v_accvgpr_write_b32 a33, v72\n" \
  "v_perm_b32 v72, v39, v38, s63\n" \
  "v_accvgpr_write_b32 a41, v72\n" \
  "v_perm_b32 v72, v41, v40, s63\n" \
  "v_accvgpr_write_b32 a26, v72\n" \
  "v_perm_b32 v72, v41, v40, s64\n" \
  "v_accvgpr_write_b32 a34, v72\n" \
  "v_perm_b32 v72, v43, v42, s63\n" \
  "v_accvgpr_write_b32 a42, v72\n" \
  "v_perm_b32 v72, v45, v44, s63\n" \
  "v_accvgpr_write_b32 a27, v72\n" \
  "v_perm_b32 v72, v45, v44, s64\n" \
  "v_accvgpr_write_b32 a35, v72\n" \
  "v_perm_b32 v72, v47, v46, s63\n" \
  "v_accvgpr_write_b32 a43, v72\n" \
  "s_waitcnt vmcnt(32)\n" \
  "s_mov_b32 s61, 491520\n" \
  "buffer_load_dword v32, v107, s[4:7], s61 offen offset:0\n" \
  "buffer_load_ushort v34, v108, s[4:7], s61 offen offset:0\n" \
  "buffer_load_dword v33, v107, s[4:7], s61 offen offset:1280\n" \
  "buffer_load_ushort v35, v108, s[4:7], s61 offen offset:1280\n" \
  "s_mov_b32 s61, 501760\n" \
  "buffer_load_dword v36, v107, s[4:7], s61 offen offset:0\n" \
  "buffer_load_ushort v38, v108, s[4:7], s61 offen offset:0\n" \
  "buffer_load_dword v37, v107, s[4:7], s61 offen offset:1280\n" \
  "buffer_load_ushort v39, v108, s[4:7], s61 offen offset:1280\n" \
  "s_mov_b32 s61, 494080\n" \
  "buffer_load_dword v40, v107, s[4:7], s61 offen offset:0\n" \
  "buffer_load_ushort v42, v108, s[4:7], s61 offen offset:0\n" \
  "buffer_load_dword v41, v107, s[4:7], s61 offen offset:1280\n" \
  "buffer_load_ushort v43, v108, s[4:7], s61 offen offset:1280\n" \
  "s_mov_b32 s61, 504320\n" \
  "buffer_load_dword v44, v107, s[4:7], s61 offen offset:0\n" \
  "buffer_load_ushort v46, v108, s[4:7], s61 offen offset:0\n" \
  "buffer_load_dword v45, v107, s[4:7], s61 offen offset:1280\n" \
  "buffer_load_ushort v47, v108, s[4:7], s61 offen offset:1280\n" \
  "v_perm_b32 v72, v49, v48, s63\n" \
  "v_accvgpr_write_b32 a28, v72\n" \
  "v_perm_b32 v72, v49, v48, s64\n" \
  "v_accvgpr_write_b32 a36, v72\n" \
  "v_perm_b32 v72, v51, v50, s63\n" \
  "v_accvgpr_write_b32 a44, v72\n" \
  "v_perm_b32 v72, v53, v52, s63\n" \
  "v_accvgpr_write_b32 a29, v72\n" \
  "v_perm_b32 v72, v53, v52, s64\n" \
  "v_accvgpr_write_b32 a37, v72\n" \
  "v_perm_b32 v72, v55, v54, s63\n" \
  "v_accvgpr_write_b32 a45, v72\n" \
  "v_perm_b32 v72, v57, v56, s63\n" \
  "v_accvgpr_write_b32 a30, v72\n" \
  "v_perm_b32 v72, v57, v56, s64\n" \
  "v_accvgpr_write_b32 a38, v72\n" \
  "v_perm_b32 v72, v59, v58, s63\n" \
  "v_accvgpr_write_b32 a46, v72\n" \
  "v_perm_b32 v72, v61, v60, s63\n" \
  "v_accvgpr_write_b32 a31, v72\n" \
  "v_perm_b32 v72, v61, v60, s64\n" \
  "v_accvgpr_write_b32 a39, v72\n" \
  "v_perm_b32 v72, v63, v62, s63\n" \
  "v_accvgpr_write_b32 a47, v72\n" \
  "s_waitcnt vmcnt(32)\n" \
  "s_mov_b32 s61, 532480\n" \
  "buffer_load_dword v48, v107, s[4:7], s61 offen offset:0\n" \
  "buffer_load_ushort v50, v108, s[4:7], s61 offen offset:0\n" \
  "buffer_load_dword v49, v107, s[4:7], s61 offen offset:1280\n" \
  "buffer_load_ushort v51, v108, s[4:7], s61 offen offset:1280\n" \
  "s_mov_b32 s61, 542720\n" \
  "buffer_load_dword v52, v107, s[4:7], s61 offen offset:0\n" \
  "buffer_load_ushort v54, v108, s[4:7], s61 offen offset:0\n" \
  "buffer_load_dword v53, v107, s[4:7], s61 offen offset:1280\n" \
  "buffer_load_ushort v55, v108, s[4:7], s61 offen offset:1280\n" \
  "s_mov_b32 s61, 535040\n" \
  "buffer_load_dword v56, v107, s[4:7], s61 offen offset:0\n" \
  "buffer_load_ushort v58, v108, s[4:7], s61 offen offset:0\n" \
  "buffer_load_dword v57, v107, s[4:7], s61 offen offset:1280\n" \
  "buffer_load_ushort v59, v108, s[4:7], s61 offen offset:1280\n" \
  "s_mov_b32 s61, 545280\n" \
  "buffer_load_dword v60, v107, s[4:7], s61 offen offset:0\n" \
  "buffer_load_ushort v62, v108, s[4:7], s61 offen offset:0\n" \
  "buffer_load_dword v61, v107, s[4:7], s61 offen offset:1280\n" \
  "buffer_load_ushort v63, v108, s[4:7], s61 offen offset:1280\n" \
  "v_perm_b32 v72, v1, v0, s63\n" \
  "v_accvgpr_write_b32 a48, v72\n" \
  "v_perm_b32 v72, v1, v0, s64\n" \
  "v_accvgpr_write_b32 a56, v72\n" \
  "v_perm_b32 v72, v3, v2, s63\n" \
  "v_accvgpr_write_b32 a64, v72\n" \
  "v_perm_b32 v72, v5, v4, s63\n" \
  "v_accvgpr_write_b32 a49, v72\n" \
  "v_perm_b32 v72, v5, v4, s64\n" \
  "v_accvgpr_write_b32 a57, v72\n" \
  "v_perm_b32 v72, v7, v6, s63\n" \
  "v_accvgpr_write_b32 a65, v72\n" \
  "v_perm_b32 v72, v9, v8, s63\n" \
  "v_accvgpr_write_b32 a50, v72\n" \
  "v_perm_b32 v72, v9, v8, s64\n" \
  "v_accvgpr_write_b32 a58, v72\n" \
  "v_perm_b32 v72, v11, v10, s63\n" \
  "v_accvgpr_write_b32 a66, v72\n" \
  "v_perm_b32 v72, v13, v12, s63\n" \
  "v_accvgpr_write_b32 a51, v72\n" \
  "v_perm_b32 v72, v13, v12, s64\n" \
  "v_accvgpr_write_b32 a59, v72\n" \
  "v_perm_b32 v72, v15, v14, s63\n" \
  "v_accvgpr_write_b32 a67, v72\n" \
  "s_waitcnt vmcnt(32)\n" \
  "s_mov_b32 s61, 655360\n" \
  "buffer_load_dword v0, v107, s[4:7], s61 offen offset:0\n" \
  "buffer_load_ushort v2, v108, s[4:7], s61 offen offset:0\n" \
  "buffer_load_dword v1, v107, s[4:7], s61 offen offset:1280\n" \
  "buffer_load_ushort v3, v108, s[4:7], s61 offen offset:1280\n" \
  "s_mov_b32 s61, 665600\n" \
  "buffer_load_dword v4, v107, s[4:7], s61 offen offset:0\n" \
  "buffer_load_ushort v6, v108, s[4:7], s61 offen offset:0\n" \
  "buffer_load_dword v5, v107, s[4:7], s61 offen offset:1280\n" \
  "buffer_load_ushort v7, v108, s[4:7], s61 offen offset:1280\n" \
  "s_mov_b32 s61, 657920\n" \
  "buffer_load_dword v8, v107, s[4:7], s61 offen offset:0\n" \
  "buffer_load_ushort v10, v108, s[4:7], s61 offen offset:0\n" \
  "buffer_load_dword v9, v107, s[4:7], s61 offen offset:1280\n" \
  "buffer_load_ushort v11, v108, s[4:7], s61 offen offset:1280\n" \
  "s_mov_b32 s61, 668160\n" \
  "buffer_load_dword v12, v107, s[4:7], s61 offen offset:0\n" \
  "buffer_load_ushort v14, v108, s[4:7], s61 offen offset:0\n" \
  "buffer_load_dword v13, v107, s[4:7], s61 offen offset:1280\n" \
  "buffer_load_ushort v15, v108, s[4:7], s61 offen offset:1280\n" \
  "v_perm_b32 v72, v17, v16, s63\n" \
  "v_accvgpr_write_b32 a52, v72\n" \
  "v_perm_b32 v72, v17, v16, s64\n" \
  "v_accvgpr_write_b32 a60, v72\n" \
  "v_perm_b32 v72, v19, v18, s63\n" \
  "v_accvgpr_write_b32 a68, v72\n" \
  "v_perm_b32 v72, v21, v20, s63\n" \
  "v_accvgpr_write_b32 a53, v72\n" \
  "v_perm_b32 v72, v21, v20, s64\n" \
  "v_accvgpr_write_b32 a61, v72\n" \
  "v_perm_b32 v72, v23, v22, s63\n" \
  "v_accvgpr_write_b32 a69, v72\n" \
  "v_perm_b32 v72, v25, v24, s63\n" \
  "v_accvgpr_write_b32 a54, v72\n" \
  "v_perm_b32 v72, v25, v24, s64\n" \
  "v_accvgpr_write_b32 a62, v72\n" \
  "v_perm_b32 v72, v27, v26, s63\n" \
  "v_accvgpr_write_b32 a70, v72\n" \
  "v_perm_b32 v72, v29, v28, s63\n" \
  "v_accvgpr_write_b32 a55, v72\n" \
  "v_perm_b32 v72, v29, v28, s64\n" \
  "v_accvgpr_write_b32 a63, v72\n" \
  "v_perm_b32 v72, v31, v30, s63\n" \
  "v_accvgpr_write_b32 a71, v72\n" \
  "s_waitcnt vmcnt(32)\n" \
  "s_mov_b32 s61, 696320\n" \
  "buffer_load_dword v16, v107, s[4:7], s61 offen offset:0\n" \
  "buffer_load_ushort v18, v108, s[4:7], s61 offen offset:0\n" \
  "buffer_load_dword v17, v107, s[4:7], s61 offen offset:1280\n" \
  "buffer_load_ushort v19, v108, s[4:7], s61 offen offset:1280\n" \
  "s_mov_b32 s61, 706560\n" \
  "buffer_load_dword v20, v107, s[4:7], s61 offen offset:0\n" \
  "buffer_load_ushort v22, v108, s[4:7], s61 offen offset:0\n" \
  "buffer_load_dword v21, v107, s[4:7], s61 offen offset:1280\n" \
  "buffer_load_ushort v23, v108, s[4:7], s61 offen offset:1280\n" \
  "s_mov_b32 s61, 698880\n" \
  "buffer_load_dword v24, v107, s[4:7], s61 offen offset:0\n" \
  "buffer_load_ushort v26, v108, s[4:7], s61 offen offset:0\n" \
  "buffer_load_dword v25, v107, s[4:7], s61 offen offset:1280\n" \
  "buffer_load_ushort v27, v108, s[4:7], s61 offen offset:1280\n" \
  "s_mov_b32 s61, 709120\n" \
  "buffer_load_dword v28, v107, s[4:7], s61 offen offset:0\n" \
  "buffer_load_ushort v30, v108, s[4:7], s61 offen offset:0\n" \
  "buffer_load_dword v29, v107, s[4:7], s61 offen offset:1280\n" \
  "buffer_load_ushort v31, v108, s[4:7], s61 offen offset:1280\n" \
  "v_perm_b32 v72, v33, v32, s63\n" \
  "v_accvgpr_write_b32 a72, v72\n" \
  "v_perm_b32 v72, v33, v32, s64\n" \
  "v_accvgpr_write_b32 a80, v72\n" \
  "v_perm_b32 v72, v35, v34, s63\n" \
  "v_accvgpr_write_b32 a88, v72\n" \
  "v_perm_b32 v72, v37, v36, s63\n" \
  "v_accvgpr_write_b32 a73, v72\n" \
  "v_perm_b32 v72, v37, v36, s64\n" \
  "v_accvgpr_write_b32 a81, v72\n" \
  "v_perm_b32 v72, v39, v38, s63\n" \
  "v_accvgpr_write_b32 a89, v72\n" \
  "v_perm_b32 v72, v41, v40, s63\n" \
  "v_accvgpr_write_b32 a74, v72\n" \
  "v_perm_b32 v72, v41, v40, s64\n" \
  "v_accvgpr_write_b32 a82, v72\n" \
  "v_perm_b32 v72, v43, v42, s63\n" \
  "v_accvgpr_write_b32 a90, v72\n" \
  "v_perm_b32 v72, v45, v44, s63\n" \
  "v_accvgpr_write_b32 a75, v72\n" \
  "v_perm_b32 v72, v45, v44, s64\n" \
  "v_accvgpr_write_b32 a83, v72\n" \
  "v_perm_b32 v72, v47, v46, s63\n" \
  "v_accvgpr_write_b32 a91, v72\n" \
  "s_waitcnt vmcnt(32)\n" \
  "s_mov_b32 s61, 819200\n" \
  "buffer_load_dword v32, v107, s[4:7], s61 offen offset:0\n" \
  "buffer_load_ushort v34, v108, s[4:7], s61 offen offset:0\n" \
  "buffer_load_dword v33, v107, s[4:7], s61 offen offset:1280\n" \
  "buffer_load_ushort v35, v108, s[4:7], s61 offen offset:1280\n" \
  "s_mov_b32 s61, 829440\n" \
  "buffer_load_dword v36, v107, s[4:7], s61 offen offset:0\n" \
  "buffer_load_ushort v38, v108, s[4:7], s61 offen offset:0\n" \
  "buffer_load_dword v37, v107, s[4:7], s61 offen offset:1280\n" \
  "buffer_load_ushort v39, v108, s[4:7], s61 offen offset:1280\n" \
  "s_mov_b32 s61, 821760\n" \
  "buffer_load_dword v40, v107, s[4:7], s61 offen offset:0\n" \
  "buffer_load_ushort v42, v108, s[4:7], s61 offen offset:0\n" \
  "buffer_load_dword v41, v107, s[4:7], s61 offen offset:1280\n" \
  "buffer_load_ushort v43, v108, s[4:7], s61 offen offset:1280\n" \
  "s_mov_b32 s61, 832000\n" \
  "buffer_load_dword v44, v107, s[4:7], s61 offen offset:0\n" \
  "buffer_load_ushort v46, v108, s[4:7], s61 offen offset:0\n" \
  "buffer_load_dword v45, v107, s[4:7], s61 offen offset:1280\n" \
  "buffer_load_ushort v47, v108, s[4:7], s61 offen offset:1280\n" \
  "v_perm_b32 v72, v49, v48, s63\n" \
  "v_accvgpr_write_b32 a76, v72\n" \
  "v_perm_b32 v72, v49, v48, s64\n" \
  "v_accvgpr_write_b32 a84, v72\n" \
  "v_perm_b32 v72, v51, v50, s63\n" \
  "v_accvgpr_write_b32 a92, v72\n" \
  "v_perm_b32 v72, v53, v52, s63\n" \
  "v_accvgpr_write_b32 a77, v72\n" \
  "v_perm_b32 v72, v53, v52, s64\n" \
  "v_accvgpr_write_b32 a85, v72\n" \
  "v_perm_b32 v72, v55, v54, s63\n" \
  "v_accvgpr_write_b32 a93, v72\n" \
  "v_perm_b32 v72, v57, v56, s63\n" \
  "v_accvgpr_write_b32 a78, v72\n" \
  "v_perm_b32 v72, v57, v56, s64\n" \
  "v_accvgpr_write_b32 a86, v72\n" \
  "v_perm_b32 v72, v59, v58, s63\n" \
  "v_accvgpr_write_b32 a94, v72\n" \
  "v_perm_b32 v72, v61, v60, s63\n" \
  "v_accvgpr_write_b32 a79, v72\n" \
  "v_perm_b32 v72, v61, v60, s64\n" \
  "v_accvgpr_write_b32 a87, v72\n" \
  "v_perm_b32 v72, v63, v62, s63\n" \
  "v_accvgpr_write_b32 a95, v72\n" \
  "s_waitcnt vmcnt(32)\n" \
  "s_mov_b32 s61, 860160\n" \
  "buffer_load_dword v48, v107, s[4:7], s61 offen offset:0\n" \
  "buffer_load_ushort v50, v108, s[4:7], s61 offen offset:0\n" \
  "buffer_load_dword v49, v107, s[4:7], s61 offen offset:1280\n" \
  "buffer_load_ushort v51, v108, s[4:7], s61 offen offset:1280\n" \
  "s_mov_b32 s61, 870400\n" \
  "buffer_load_dword v52, v107, s[4:7], s61 offen offset:0\n" \
  "buffer_load_ushort v54, v108, s[4:7], s61 offen offset:0\n" \
  "buffer_load_dword v53, v107, s[4:7], s61 offen offset:1280\n" \
  "buffer_load_ushort v55, v108, s[4:7], s61 offen offset:1280\n" \
  "s_mov_b32 s61, 862720\n" \
  "buffer_load_dword v56, v107, s[4:7], s61 offen offset:0\n" \
  "buffer_load_ushort v58, v108, s[4:7], s61 offen offset:0\n" \
  "buffer_load_dword v57, v107, s[4:7], s61 offen offset:1280\n" \
  "buffer_load_ushort v59, v108, s[4:7], s61 offen offset:1280\n" \
  "s_mov_b32 s61, 872960\n" \
  "buffer_load_dword v60, v107, s[4:7], s61 offen offset:0\n" \
  "buffer_load_ushort v62, v108, s[4:7], s61 offen offset:0\n" \
  "buffer_load_dword v61, v107, s[4:7], s61 offen offset:1280\n" \
  "buffer_load_ushort v63, v108, s[4:7], s61 offen offset:1280\n" \
  "v_perm_b32 v72, v1, v0, s63\n" \
  "v_accvgpr_write_b32 a96, v72\n" \
  "v_perm_b32 v72, v1, v0, s64\n" \
  "v_accvgpr_write_b32 a104, v72\n" \
  "v_perm_b32 v72, v3, v2, s63\n" \
  "v_accvgpr_write_b32 a112, v72\n" \
  "v_perm_b32 v72, v5, v4, s63\n" \
  "v_accvgpr_write_b32 a97, v72\n" \
  "v_perm_b32 v72, v5, v4, s64\n" \
  "v_accvgpr_write_b32 a105, v72\n" \
  "v_perm_b32 v72, v7, v6, s63\n" \
  "v_accvgpr_write_b32 a113, v72\n" \
  "v_perm_b32 v72, v9, v8, s63\n" \
  "v_accvgpr_write_b32 a98, v72\n" \
  "v_perm_b32 v72, v9, v8, s64\n" \
  "v_accvgpr_write_b32 a106, v72\n" \
  "v_perm_b32 v72, v11, v10, s63\n" \
  "v_accvgpr_write_b32 a114, v72\n" \
  "v_perm_b32 v72, v13, v12, s63\n" \
  "v_accvgpr_write_b32 a99, v72\n" \
  "v_perm_b32 v72, v13, v12, s64\n" \
  "v_accvgpr_write_b32 a107, v72\n" \
  "v_perm_b32 v72, v15, v14, s63\n" \
  "v_accvgpr_write_b32 a115, v72\n" \
  "s_waitcnt vmcnt(32)\n" \
  "s_mov_b32 s61, 983040\n" \
  "buffer_load_dword v0, v107, s[4:7], s61 offen offset:0\n" \
  "buffer_load_ushort v2, v108, s[4:7], s61 offen offset:0\n" \
  "buffer_load_dword v1, v107, s[4:7], s61 offen offset:1280\n" \
  "buffer_load_ushort v3, v108, s[4:7], s61 offen offset:1280\n" \
  "s_mov_b32 s61, 993280\n" \
  "buffer_load_dword v4, v107, s[4:7], s61 offen offset:0\n" \
  "buffer_load_ushort v6, v108, s[4:7], s61 offen offset:0\n" \
  "buffer_load_dword v5, v107, s[4:7], s61 offen offset:1280\n" \
  "buffer_load_ushort v7, v108, s[4:7], s61 offen offset:1280\n" \
  "s_mov_b32 s61, 985600\n" \
  "buffer_load_dword v8, v107, s[4:7], s61 offen offset:0\n" \
  "buffer_load_ushort v10, v108, s[4:7], s61 offen offset:0\n" \
  "buffer_load_dword v9, v107, s[4:7], s61 offen offset:1280\n" \
  "buffer_load_ushort v11, v108, s[4:7], s61 offen offset:1280\n" \
  "s_mov_b32 s61, 995840\n" \
  "buffer_load_dword v12, v107, s[4:7], s61 offen offset:0\n" \
  "buffer_load_ushort v14, v108, s[4:7], s61 offen offset:0\n" \
  "buffer_load_dword v13, v107, s[4:7], s61 offen offset:1280\n" \
  "buffer_load_ushort v15, v108, s[4:7], s61 offen offset:1280\n" \
  "v_perm_b32 v72, v17, v16, s63\n" \
  "v_accvgpr_write_b32 a100, v72\n" \
  "v_perm_b32 v72, v17, v16, s64\n" \
  "v_accvgpr_write_b32 a108, v72\n" \
  "v_perm_b32 v72, v19, v18, s63\n" \
  "v_accvgpr_write_b32 a116, v72\n" \
  "v_perm_b32 v72, v21, v20, s63\n" \
  "v_accvgpr_write_b32 a101, v72\n" \
  "v_perm_b32 v72, v21, v20, s64\n" \
  "v_accvgpr_write_b32 a109, v72\n" \
  "v_perm_b32 v72, v23, v22, s63\n" \
  "v_accvgpr_write_b32 a117, v72\n" \
  "v_perm_b32 v72, v25, v24, s63\n" \
  "v_accvgpr_write_b32 a102, v72\n" \
  "v_perm_b32 v72, v25, v24, s64\n" \
  "v_accvgpr_write_b32 a110, v72\n" \
  "v_perm_b32 v72, v27, v26, s63\n" \
  "v_accvgpr_write_b32 a118, v72\n" \
  "v_perm_b32 v72, v29, v28, s63\n" \
  "v_accvgpr_write_b32 a103, v72\n" \
  "v_perm_b32 v72, v29, v28, s64\n" \
  "v_accvgpr_write_b32 a111, v72\n" \
  "v_perm_b32 v72, v31, v30, s63\n" \
  "v_accvgpr_write_b32 a119, v72\n" \
  "s_waitcnt vmcnt(32)\n" \
  "s_mov_b32 s61, 1024000\n" \
  "buffer_load_dword v16, v107, s[4:7], s61 offen offset:0\n" \
  "buffer_load_ushort v18, v108, s[4:7], s61 offen offset:0\n" \
  "buffer_load_dword v17, v107, s[4:7], s61 offen offset:1280\n" \
  "buffer_load_ushort v19, v108, s[4:7], s61 offen offset:1280\n" \
  "s_mov_b32 s61, 1034240\n" \
  "buffer_load_dword v20, v107, s[4:7], s61 offen offset:0\n" \
  "buffer_load_ushort v22, v108, s[4:7], s61 offen offset:0\n" \
  "buffer_load_dword v21, v107, s[4:7], s61 offen offset:1280\n" \
  "buffer_load_ushort v23, v108, s[4:7], s61 offen offset:1280\n" \
  "s_mov_b32 s61, 1026560\n" \
  "buffer_load_dword v24, v107, s[4:7], s61 offen offset:0\n" \
  "buffer_load_ushort v26, v108, s[4:7], s61 offen offset:0\n" \
  "buffer_load_dword v25, v107, s[4:7], s61 offen offset:1280\n" \
  "buffer_load_ushort v27, v108, s[4:7], s61 offen offset:1280\n" \
  "s_mov_b32 s61, 1036800\n" \
  "buffer_load_dword v28, v107, s[4:7], s61 offen offset:0\n" \
  "buffer_load_ushort v30, v108, s[4:7], s61 offen offset:0\n" \
  "buffer_load_dword v29, v107, s[4:7], s61 offen offset:1280\n" \
  "buffer_load_ushort v31, v108, s[4:7], s61 offen offset:1280\n" \
  "v_perm_b32 v72, v33, v32, s63\n" \
  "v_accvgpr_write_b32 a120, v72\n" \
  "v_perm_b32 v72, v33, v32, s64\n" \
  "v_accvgpr_write_b32 a128, v72\n" \
  "v_perm_b32 v72, v35, v34, s63\n" \
  "v_accvgpr_write_b32 a136, v72\n" \
  "v_perm_b32 v72, v37, v36, s63\n" \
  "v_accvgpr_write_b32 a121, v72\n" \
  "v_perm_b32 v72, v37, v36, s64\n" \
  "v_accvgpr_write_b32 a129, v72\n" \
  "v_perm_b32 v72, v39, v38, s63\n" \
  "v_accvgpr_write_b32 a137, v72\n" \
  "v_perm_b32 v72, v41, v40, s63\n" \
  "v_accvgpr_write_b32 a122, v72\n" \
  "v_perm_b32 v72, v41, v40, s64\n" \
  "v_accvgpr_write_b32 a130, v72\n" \
  "v_perm_b32 v72, v43, v42, s63\n" \
  "v_accvgpr_write_b32 a138, v72\n" \
  "v_perm_b32 v72, v45, v44, s63\n" \
  "v_accvgpr_write_b32 a123, v72\n" \
  "v_perm_b32 v72, v45, v44, s64\n" \
  "v_accvgpr_write_b32 a131, v72\n" \
  "v_perm_b32 v72, v47, v46, s63\n" \
  "v_accvgpr_write_b32 a139, v72\n" \
  "s_waitcnt vmcnt(32)\n" \
  "s_mov_b32 s61, 1146880\n" \
  "buffer_load_dword v32, v107, s[4:7], s61 offen offset:0\n" \
  "buffer_load_ushort v34, v108, s[4:7], s61 offen offset:0\n" \
  "buffer_load_dword v33, v107, s[4:7], s61 offen offset:1280\n" \
  "buffer_load_ushort v35, v108, s[4:7], s61 offen offset:1280\n" \
  "s_mov_b32 s61, 1157120\n" \
  "buffer_load_dword v36, v107, s[4:7], s61 offen offset:0\n" \
  "buffer_load_ushort v38, v108, s[4:7], s61 offen offset:0\n" \
  "buffer_load_dword v37, v107, s[4:7], s61 offen offset:1280\n" \
  "buffer_load_ushort v39, v108, s[4:7], s61 offen offset:1280\n" \
  "s_mov_b32 s61, 1149440\n" \
  "buffer_load_dword v40, v107, s[4:7], s61 offen offset:0\n" \
  "buffer_load_ushort v42, v108, s[4:7], s61 offen offset:0\n" \
  "buffer_load_dword v41, v107, s[4:7], s61 offen offset:1280\n" \
  "buffer_load_ushort v43, v108, s[4:7], s61 offen offset:1280\n" \
  "s_mov_b32 s61, 1159680\n" \
  "buffer_load_dword v44, v107, s[4:7], s61 offen offset:0\n" \
  "buffer_load_ushort v46, v108, s[4:7], s61 offen offset:0\n" \
  "buffer_load_dword v45, v107, s[4:7], s61 offen offset:1280\n" \
  "buffer_load_ushort v47, v108, s[4:7], s61 offen offset:1280\n" \
  "v_perm_b32 v72, v49, v48, s63\n" \
  "v_accvgpr_write_b32 a124, v72\n" \
  "v_perm_b32 v72, v49, v48, s64\n" \
  "v_accvgpr_write_b32 a132, v72\n" \
  "v_perm_b32 v72, v51, v50, s63\n" \
  "v_accvgpr_write_b32 a140, v72\n" \
  "v_perm_b32 v72, v53, v52, s63\n" \
  "v_accvgpr_write_b32 a125, v72\n" \
  "v_perm_b32 v72, v53, v52, s64\n" \
  "v_accvgpr_write_b32 a133, v72\n" \
  "v_perm_b32 v72, v55, v54, s63\n" \
  "v_accvgpr_write_b32 a141, v72\n" \
  "v_perm_b32 v72, v57, v56, s63\n" \
  "v_accvgpr_write_b32 a126, v72\n" \
  "v_perm_b32 v72, v57, v56, s64\n" \
  "v_accvgpr_write_b32 a134, v72\n" \
  "v_perm_b32 v72, v59, v58, s63\n" \
  "v_accvgpr_write_b32 a142, v72\n" \
  "v_perm_b32 v72, v61, v60, s63\n" \
  "v_accvgpr_write_b32 a127, v72\n" \
  "v_perm_b32 v72, v61, v60, s64\n" \
  "v_accvgpr_write_b32 a135, v72\n" \
  "v_perm_b32 v72, v63, v62, s63\n" \
  "v_accvgpr_write_b32 a143, v72\n" \
  "s_waitcnt vmcnt(32)\n" \
  "s_mov_b32 s61, 1187840\n" \
  "buffer_load_dword v48, v107, s[4:7], s61 offen offset:0\n" \
  "buffer_load_ushort v50, v108, s[4:7], s61 offen offset:0\n" \
  "buffer_load_dword v49, v107, s[4:7], s61 offen offset:1280\n" \
  "buffer_load_ushort v51, v108, s[4:7], s61 offen offset:1280\n" \
  "s_mov_b32 s61, 1198080\n" \
  "buffer_load_dword v52, v107, s[4:7], s61 offen offset:0\n" \
  "buffer_load_ushort v54, v108, s[4:7], s61 offen offset:0\n" \
  "buffer_load_dword v53, v107, s[4:7], s61 offen offset:1280\n" \
  "buffer_load_ushort v55, v108, s[4:7], s61 offen offset:1280\n" \
  "s_mov_b32 s61, 1190400\n" \
  "buffer_load_dword v56, v107, s[4:7], s61 offen offset:0\n" \
  "buffer_load_ushort v58, v108, s[4:7], s61 offen offset:0\n" \
  "buffer_load_dword v57, v107, s[4:7], s61 offen offset:1280\n" \
  "buffer_load_ushort v59, v108, s[4:7], s61 offen offset:1280\n" \
  "s_mov_b32 s61, 1200640\n" \
  "buffer_load_dword v60, v107, s[4:7], s61 offen offset:0\n" \
  "buffer_load_ushort v62, v108, s[4:7], s61 offen offset:0\n" \
  "buffer_load_dword v61, v107, s[4:7], s61 offen offset:1280\n" \
  "buffer_load_ushort v63, v108, s[4:7], s61 offen offset:1280\n" \
  "v_perm_b32 v72, v1, v0, s63\n" \
  "v_accvgpr_write_b32 a144, v72\n" \
  "v_perm_b32 v72, v1, v0, s64\n" \
  "v_accvgpr_write_b32 a152, v72\n" \
  "v_perm_b32 v72, v3, v2, s63\n" \
  "v_accvgpr_write_b32 a160, v72\n" \
  "v_perm_b32 v72, v5, v4, s63\n" \
  "v_accvgpr_write_b32 a145, v72\n" \
  "v_perm_b32 v72, v5, v4, s64\n" \
  "v_accvgpr_write_b32 a153, v72\n" \
  "v_perm_b32 v72, v7, v6, s63\n" \
  "v_accvgpr_write_b32 a161, v72\n" \
  "v_perm_b32 v72, v9, v8, s63\n" \
  "v_accvgpr_write_b32 a146, v72\n" \
  "v_perm_b32 v72, v9, v8, s64\n" \
  "v_accvgpr_write_b32 a154, v72\n" \
  "v_perm_b32 v72, v11, v10, s63\n" \
  "v_accvgpr_write_b32 a162, v72\n" \
  "v_perm_b32 v72, v13, v12, s63\n" \
  "v_accvgpr_write_b32 a147, v72\n" \
  "v_perm_b32 v72, v13, v12, s64\n" \
  "v_accvgpr_write_b32 a155, v72\n" \
  "v_perm_b32 v72, v15, v14, s63\n" \
  "v_accvgpr_write_b32 a163, v72\n" \
  "s_waitcnt vmcnt(32)\n" \
  "v_perm_b32 v72, v17, v16, s63\n" \
  "v_accvgpr_write_b32 a148, v72\n" \
  "v_perm_b32 v72, v17, v16, s64\n" \
  "v_accvgpr_write_b32 a156, v72\n" \
  "v_perm_b32 v72, v19, v18, s63\n" \
  "v_accvgpr_write_b32 a164, v72\n" \
  "v_perm_b32 v72, v21, v20, s63\n" \
  "v_accvgpr_write_b32 a149, v72\n" \
  "v_perm_b32 v72, v21, v20, s64\n" \
  "v_accvgpr_write_b32 a157, v72\n" \
  "v_perm_b32 v72, v23, v22, s63\n" \
  "v_accvgpr_write_b32 a165, v72\n" \
  "v_perm_b32 v72, v25, v24, s63\n" \
  "v_accvgpr_write_b32 a150, v72\n" \
  "v_perm_b32 v72, v25, v24, s64\n" \
  "v_accvgpr_write_b32 a158, v72\n" \
  "v_perm_b32 v72, v27, v26, s63\n" \
  "v_accvgpr_write_b32 a166, v72\n" \
  "v_perm_b32 v72, v29, v28, s63\n" \
  "v_accvgpr_write_b32 a151, v72\n" \
  "v_perm_b32 v72, v29, v28, s64\n" \
  "v_accvgpr_write_b32 a159, v72\n" \
  "v_perm_b32 v72, v31, v30, s63\n" \
  "v_accvgpr_write_b32 a167, v72\n" \
  "s_waitcnt vmcnt(16)\n" \
  "v_perm_b32 v72, v33, v32, s63\n" \
  "v_accvgpr_write_b32 a168, v72\n" \
  "v_perm_b32 v72, v33, v32, s64\n" \
  "v_accvgpr_write_b32 a176, v72\n" \
  "v_perm_b32 v72, v35, v34, s63\n" \
  "v_accvgpr_write_b32 a184, v72\n" \
  "v_perm_b32 v72, v37, v36, s63\n" \
  "v_accvgpr_write_b32 a169, v72\n" \
  "v_perm_b32 v72, v37, v36, s64\n" \
  "v_accvgpr_write_b32 a177, v72\n" \
  "v_perm_b32 v72, v39, v38, s63\n" \
  "v_accvgpr_write_b32 a185, v72\n" \
  "v_perm_b32 v72, v41, v40, s63\n" \
  "v_accvgpr_write_b32 a170, v72\n" \
  "v_perm_b32 v72, v41, v40, s64\n" \
  "v_accvgpr_write_b32 a178, v72\n" \
  "v_perm_b32 v72, v43, v42, s63\n" \
  "v_accvgpr_write_b32 a186, v72\n" \
  "v_perm_b32 v72, v45, v44, s63\n" \
  "v_accvgpr_write_b32 a171, v72\n" \
  "v_perm_b32 v72, v45, v44, s64\n" \
  "v_accvgpr_write_b32 a179, v72\n" \
  "v_perm_b32 v72, v47, v46, s63\n" \
  "v_accvgpr_write_b32 a187, v72\n" \
  "s_waitcnt vmcnt(0)\n" \
  "v_perm_b32 v72, v49, v48, s63\n" \
  "v_accvgpr_write_b32 a172, v72\n" \
  "v_perm_b32 v72, v49, v48, s64\n" \
  "v_accvgpr_write_b32 a180, v72\n" \
  "v_perm_b32 v72, v51, v50, s63\n" \
  "v_accvgpr_write_b32 a188, v72\n" \
  "v_perm_b32 v72, v53, v52, s63\n" \
  "v_accvgpr_write_b32 a173, v72\n" \
  "v_perm_b32 v72, v53, v52, s64\n" \
  "v_accvgpr_write_b32 a181, v72\n" \
  "v_perm_b32 v72, v55, v54, s63\n" \
  "v_accvgpr_write_b32 a189, v72\n" \
  "v_perm_b32 v72, v57, v56, s63\n" \
  "v_accvgpr_write_b32 a174, v72\n" \
  "v_perm_b32 v72, v57, v56, s64\n" \
  "v_accvgpr_write_b32 a182, v72\n" \
  "v_perm_b32 v72, v59, v58, s63\n" \
  "v_accvgpr_write_b32 a190, v72\n" \
  "v_perm_b32 v72, v61, v60, s63\n" \
  "v_accvgpr_write_b32 a175, v72\n" \
  "v_perm_b32 v72, v61, v60, s64\n" \
  "v_accvgpr_write_b32 a183, v72\n" \
  "v_perm_b32 v72, v63, v62, s63\n" \
  "v_accvgpr_write_b32 a191, v72\n" \
  "s_mov_b32 s58, 1048576\n" \
  "s_mov_b32 s56, s17\n" \
  "s_mov_b32 s66, 0xffbfffbf\n" \
  "v_mov_b32 v76, 0xffbfffbf\n" \
  "v_mov_b32 v77, 0xffbfffbf\n" \
  "v_mov_b32 v78, 0xffbfffbf\n" \
  "v_mov_b32 v79, 0xffbfffbf\n" \
  "LSTEP_%=:\n" \
  "s_and_b32 s59, s56, 3\n" \
  "s_mul_i32 s59, s59, 393216\n" \
  "s_lshr_b32 s61, s56, 4\n" \
  "s_lshl_b32 s61, s61, 14\n" \
  "s_and_b32 s62, s56, 15\n" \
  "s_lshl_b32 s62, s62, 9\n" \
  "s_add_i32 s60, s61, s62\n" \
  "s_add_i32 s60, s60, s19\n" \
  "s_mov_b32 s65, 0\n" \
  "v_mov_b64 v[32:33], 0\n" \
  "v_mov_b64 v[34:35], 0\n" \
  "v_mov_b64 v[36:37], 0\n" \
  "v_mov_b64 v[38:39], 0\n" \
  "v_mov_b64 v[40:41], 0\n" \
  "v_mov_b64 v[42:43], 0\n" \
  "s_cmp_lg_u32 s13, 0\n" \
  "s_cbranch_scc1 LGO_%=\n" \
  "s_cmp_eq_u32 s56, 0\n" \
  "s_cbranch_scc1 LFa_%=\n" \
  "s_cmp_lg_u32 s56, 63\n" \
  "s_cbranch_scc1 LCy_%=\n" \
  "LFa_%=:\n" \
  "s_mov_b64 exec, 1\n" \
  "buffer_load_dword v72, off, s[0:3], s60 sc1\n" \
  "s_mov_b64 exec, -1\n" \
  "s_waitcnt vmcnt(0)\n" \
  "v_readfirstlane_b32 s57, v72\n" \
  "s_cmp_lg_u32 s57, s66\n" \
  "s_cbranch_scc1 LFax_%=\n" \
  "s_cmp_eq_u32 s58, 0\n" \
  "s_cbranch_scc1 LFax_%=\n" \
  "s_sub_u32 s58, s58, 1\n" \
  "s_branch LFa_%=\n" \
  "LFax_%=:\n" \
  "s_bitcmp1_b32 s57, 1\n" \
  "s_cselect_b32 s61, s16, 0\n" \
  "s_add_i32 s57, s57, s61\n" \
  "s_add_i32 s57, s57, s14\n" \
  "LCy_%=:\n" \
  "s_memrealtime s[72:73]\n" \
  "s_waitcnt lgkmcnt(0)\n" \
  "s_mul_i32 s72, s72, 24\n" \
  "s_sub_i32 s72, s57, s72\n" \
  "s_sub_i32 s72, s72, 1000\n" \
  "s_cmp_lt_u32 s72, 0x8000\n" \
  "s_cbranch_scc1 LCy_%=\n" \
  "s_waitcnt lgkmcnt(0)\n" \
  "s_memrealtime s[72:73]\n" \
  "s_nop 2\n" \
  "s_memrealtime s[74:75]\n" \
  "s_nop 2\n" \
  "s_memrealtime s[76:77]\n" \
  "s_memtime vcc\n" \
  "s_nop 1\n" \
  "s_memrealtime s[78:79]\n" \
  "s_nop 2\n" \
  "s_memrealtime s[80:81]\n" \
  "s_nop 2\n" \
  "s_memrealtime s[82:83]\n" \
  "s_waitcnt lgkmcnt(0)\n" \
  "s_add_i32 s72, s72, s74\n" \
  "s_add_i32 s72, s72, s76\n" \
  "s_add_i32 s72, s72, s78\n" \
  "s_add_i32 s72, s72, s80\n" \
  "s_add_i32 s72, s72, s82\n" \
  "s_lshl_b32 s72, s72, 2\n" \
  "s_sub_i32 s67, s72, vcc_lo\n" \
  "s_sub_i32 s74, s57, s67\n" \
  "s_getpc_b64 s[76:77]\n" \
  "LPy_%=:\n" \
  "s_add_u32 s76, s76, LEy_%=-LPy_%=\n" \
  "s_addc_u32 s77, s77, 0\n" \
  "s_sub_i32 s74, s74, 128\n" \
  "LWy_%=:\n" \
  "s_memtime s[72:73]\n" \
  "s_waitcnt lgkmcnt(0)\n" \
  "s_sub_i32 s72, s74, s72\n" \
  "s_cmp_gt_u32 s72, 0x8000\n" \
  "s_cselect_b32 s72, 0, s72\n" \
  "s_cmp_gt_u32 s72, 255\n" \
  "s_cbranch_scc1 LWy_%=\n" \
  "s_lshr_b32 s72, s72, 3\n" \
  "s_lshl_b32 s72, s72, 2\n" \
  "s_sub_u32 s76, s76, s72\n" \
  "s_subb_u32 s77, s77, 0\n" \
  "s_setpc_b64 s[76:77]\n" \
  "s_nop 1\n" \
  "s_nop 1\n" \
  "s_nop 1\n" \
  "s_nop 1\n" \
  "s_nop 1\n" \
  "s_nop 1\n" \
  "s_nop 1\n" \
  "s_nop 1\n" \
  "s_nop 1\n" \
  "s_nop 1\n" \
  "s_nop 1\n" \
  "s_nop 1\n" \
  "s_nop 1\n" \
  "s_nop 1\n" \
  "s_nop 1\n" \
  "s_nop 1\n" \
  "s_nop 1\n" \
  "s_nop 1\n" \
  "s_nop 1\n" \
  "s_nop 1\n" \
  "s_nop 1\n" \
  "s_nop 1\n" \
  "s_nop 1\n" \
  "s_nop 1\n" \
  "s_nop 1\n" \
  "s_nop 1\n" \
  "s_nop 1\n" \
  "s_nop 1\n" \
  "s_nop 1\n" \
  "s_nop 1\n" \
  "s_nop 1\n" \
  "s_nop 1\n" \
  "LEy_%=:\n" \
  "LGO_%=:\n" \
  "s_waitcnt vmcnt(0)\n" \
  "s_barrier\n" \
  "LAGAIN_%=:\n" \
  "buffer_load_dwordx4 v[0:3], v100, s[0:3], s59 offen offset:0 sc1\n" \
  "s_nop 11\n" \
  "buffer_load_dwordx4 v[4:7], v100, s[0:3], s59 offen offset:1024 sc1\n" \
  "s_nop 11\n" \
  "buffer_load_dwordx4 v[8:11], v100, s[0:3], s59 offen offset:2048 sc1\n" \
  "s_nop 11\n" \
  "buffer_load_dwordx4 v[12:15], v100, s[0:3], s59 offen offset:3072 sc1\n" \
  "s_nop 11\n" \
  "buffer_load_dwordx4 v[16:19], v101, s[0:3], s59 offen offset:0 sc1\n" \
  "s_nop 11\n" \
  "buffer_load_dwordx4 v[20:23], v101, s[0:3], s59 offen offset:1024 sc1\n" \
  "s_nop 11\n" \
  "buffer_load_dwordx4 v[24:27], v101, s[0:3], s59 offen offset:2048 sc1\n" \
  "s_nop 11\n" \
  "buffer_load_dwordx4 v[28:31], v101, s[0:3], s59 offen offset:3072 sc1\n" \
  "s_waitcnt vmcnt(7)\n" \
  "v_smfmac_f32_16x16x64_bf16 v[32:35], v[0:3], a[0:7], v106\n" \
  "v_smfmac_f32_16x16x64_bf16 v[36:39], v[0:3], a[8:15], v106\n" \
  "v_smfmac_f32_16x16x64_bf16 v[40:43], v[0:3], a[16:23], v106\n" \
  "s_waitcnt vmcnt(6)\n" \
  "v_smfmac_f32_16x16x64_bf16 v[32:35], v[4:7], a[24:31], v106\n" \
  "v_smfmac_f32_16x16x64_bf16 v[36:39], v[4:7], a[32:39], v106\n" \
  "v_smfmac_f32_16x16x64_bf16 v[40:43], v[4:7], a[40:47], v106\n" \
  "s_waitcnt vmcnt(5)\n" \
  "v_smfmac_f32_16x16x64_bf16 v[32:35], v[8:11], a[48:55], v106\n" \
  "v_smfmac_f32_16x16x64_bf16 v[36:39], v[8:11], a[56:63], v106\n" \
  "v_smfmac_f32_16x16x64_bf16 v[40:43], v[8:11], a[64:71], v106\n" \
  "s_waitcnt vmcnt(4)\n" \
  "v_smfmac_f32_16x16x64_bf16 v[32:35], v[12:15], a[72:79], v106\n" \
  "v_mov_b32 v74, v76\n" \
  "v_smfmac_f32_16x16x64_bf16 v[36:39], v[12:15], a[80:87], v106\n" \
  "v_mov_b32 v74, v76\n" \
  "v_smfmac_f32_16x16x64_bf16 v[40:43], v[12:15], a[88:95], v106\n" \
  "v_mov_b32 v74, v76\n" \
  "s_waitcnt vmcnt(3)\n" \
  "v_smfmac_f32_16x16x64_bf16 v[32:35], v[16:19], a[96:103], v106\n" \
  "v_mov_b32 v74, v76\n" \
  "v_smfmac_f32_16x16x64_bf16 v[36:39], v[16:19], a[104:111], v106\n" \
  "v_mov_b32 v74, v76\n" \
  "v_smfmac_f32_16x16x64_bf16 v[40:43], v[16:19], a[112:119], v106\n" \
  "v_mov_b32 v74, v76\n" \
  "s_waitcnt vmcnt(2)\n" \
  "v_smfmac_f32_16x16x64_bf16 v[32:35], v[20:23], a[120:127], v106\n" \
  "v_mov_b32 v74, v76\n" \
  "v_smfmac_f32_16x16x64_bf16 v[36:39], v[20:23], a[128:135], v106\n" \
  "v_mov_b32 v74, v76\n" \
  "v_smfmac_f32_16x16x64_bf16 v[40:43], v[20:23], a[136:143], v106\n" \
  "v_mov_b32 v74, v76\n" \
  "s_waitcnt vmcnt(1)\n" \
  "v_smfmac_f32_16x16x64_bf16 v[32:35], v[24:27], a[144:151], v106\n" \
  "v_mov_b32 v74, v76\n" \
  "v_smfmac_f32_16x16x64_bf16 v[36:39], v[24:27], a[152:159], v106\n" \
  "v_mov_b32 v74, v76\n" \
  "v_smfmac_f32_16x16x64_bf16 v[40:43], v[24:27], a[160:167], v106\n" \
  "v_mov_b32 v74, v76\n" \
  "s_waitcnt vmcnt(0)\n" \
  "v_smfmac_f32_16x16x64_bf16 v[32:35], v[28:31], a[168:175], v106\n" \
  "v_mov_b32 v74, v76\n" \
  "v_smfmac_f32_16x16x64_bf16 v[36:39], v[28:31], a[176:183], v106\n" \
  "v_mov_b32 v74, v76\n" \
  "v_smfmac_f32_16x16x64_bf16 v[40:43], v[28:31], a[184:191], v106\n" \
  "v_mov_b32 v74, v76\n" \
  "v_add_f32 v44, v32, v33\n" \
  "v_add_f32 v46, v34, v35\n" \
  "v_cmp_u_f32 vcc, v44, v46\n" \
  "s_nop 0\n" \
  "v_add_f32 v45, v36, v37\n" \
  "v_add_f32 v47, v38, v39\n" \
  "ds_write2_b64 v102, v[44:45], v[46:47] offset1:120\n" \
  "v_add_f32 v48, v40, v41\n" \
  "v_add_f32 v49, v42, v43\n" \
  "ds_write2_b32 v103, v48, v49 offset1:240\n" \
  "s_cbranch_vccnz LBAD_%=\n" \
  "LOK_%=:\n" \
  "s_cmp_lg_u32 s13, 0\n" \
  "s_cbranch_scc1 LYO_%=\n" \
  "s_waitcnt lgkmcnt(0)\n" \
  "s_barrier\n" \
  "ds_read_b128 v[52:55], v104 offset:0\n" \
  "ds_read_b128 v[56:59], v104 offset:16\n" \
  "ds_read_b128 v[60:63], v104 offset:32\n" \
  "ds_read_b128 v[64:67], v104 offset:48\n" \
  "s_add_i32 s61, s56, 2\n" \
  "s_and_b32 s61, s61, 3\n" \
  "s_mul_i32 s61, s61, 393216\n" \
  "s_waitcnt lgkmcnt(3)\n" \
  "v_pk_add_f32 v[52:53], v[52:53], v[54:55]\n" \
  "s_waitcnt lgkmcnt(2)\n" \
  "v_pk_add_f32 v[56:57], v[56:57], v[58:59]\n" \
  "s_waitcnt lgkmcnt(1)\n" \
  "v_pk_add_f32 v[60:61], v[60:61], v[62:63]\n" \
  "s_waitcnt lgkmcnt(0)\n" \
  "v_pk_add_f32 v[64:65], v[64:65], v[66:67]\n" \
  "v_pk_add_f32 v[52:53], v[52:53], v[56:57]\n" \
  "v_pk_add_f32 v[60:61], v[60:61], v[64:65]\n" \
  "v_pk_add_f32 v[52:53], v[52:53], v[60:61]\n" \
  "v_cvt_pk_bf16_f32 v56, v52, v53\n" \
  "buffer_store_dword v56, v105, s[0:3], s59 offen sc1\n" \
  "buffer_store_dword v56, v109, s[0:3], s59 offen sc1\n" \
  "buffer_store_dword v56, v110, s[0:3], s59 offen sc1\n" \
  "buffer_store_dword v56, v111, s[0:3], s59 offen sc1\n" \
  "buffer_store_dword v76, v105, s[0:3], s61 offen sc1\n" \
  "buffer_store_dword v76, v109, s[0:3], s61 offen sc1\n" \
  "buffer_store_dword v76, v110, s[0:3], s61 offen sc1\n" \
  "buffer_store_dword v76, v111, s[0:3], s61 offen sc1\n" \
  "LFb_%=:\n" \
  "s_mov_b64 exec, 1\n" \
  "buffer_load_dword v72, off, s[0:3], s60 sc1\n" \
  "s_mov_b64 exec, -1\n" \
  "s_waitcnt vmcnt(0)\n" \
  "v_readfirstlane_b32 s57, v72\n" \
  "s_cmp_lg_u32 s57, s66\n" \
  "s_cbranch_scc1 LFbx_%=\n" \
  "s_cmp_eq_u32 s58, 0\n" \
  "s_cbranch_scc1 LFbx_%=\n" \
  "s_sub_u32 s58, s58, 1\n" \
  "s_branch LFb_%=\n" \
  "LFbx_%=:\n" \
  "s_bitcmp1_b32 s57, 1\n" \
  "s_cselect_b32 s61, s16, 0\n" \
  "s_add_i32 s57, s57, s61\n" \
  "s_add_i32 s57, s57, s14\n" \
  "s_add_i32 s57, s57, s15\n" \
  "LNX_%=:\n" \
  "s_add_i32 s56, s56, 1\n" \
  "s_cmp_lt_u32 s56, s18\n" \
  "s_cbranch_scc1 LSTEP_%=\n" \
  "s_branch LOUT_%=\n" \
  "LYO_%=:\n" \
  "s_waitcnt lgkmcnt(0)\n" \
  "s_barrier\n" \
  "s_branch LNX_%=\n" \
  "LBAD_%=:\n" \
  "v_xor_b32 v0, s66, v0\n" \
  "v_xor_b32 v1, s66, v1\n" \
  "v_xor_b32 v2, s66, v2\n" \
  "v_xor_b32 v3, s66, v3\n" \
  "v_xor_b32 v4, s66, v4\n" \
  "v_xor_b32 v5, s66, v5\n" \
  "v_xor_b32 v6, s66, v6\n" \
  "v_xor_b32 v7, s66, v7\n" \
  "v_xor_b32 v8, s66, v8\n" \
  "v_xor_b32 v9, s66, v9\n" \
  "v_xor_b32 v10, s66, v10\n" \
  "v_xor_b32 v11, s66, v11\n" \
  "v_xor_b32 v12, s66, v12\n" \
  "v_xor_b32 v13, s66, v13\n" \
  "v_xor_b32 v14, s66, v14\n" \
  "v_xor_b32 v15, s66, v15\n" \
  "v_xor_b32 v16, s66, v16\n" \
  "v_xor_b32 v17, s66, v17\n" \
  "v_xor_b32 v18, s66, v18\n" \
  "v_xor_b32 v19, s66, v19\n" \
  "v_xor_b32 v20, s66, v20\n" \
  "v_xor_b32 v21, s66, v21\n" \
  "v_xor_b32 v22, s66, v22\n" \
  "v_xor_b32 v23, s66, v23\n" \
  "v_xor_b32 v24, s66, v24\n" \
  "v_xor_b32 v25, s66, v25\n" \
  "v_xor_b32 v26, s66, v26\n" \
  "v_xor_b32 v27, s66, v27\n" \
  "v_xor_b32 v28, s66, v28\n" \
  "v_xor_b32 v29, s66, v29\n" \
  "v_xor_b32 v30, s66, v30\n" \
  "v_xor_b32 v31, s66, v31\n" \
  "v_min_u32 v0, v0, v1\n" \
  "v_min3_u32 v0, v0, v2, v3\n" \
  "v_min3_u32 v0, v0, v4, v5\n" \
  "v_min3_u32 v0, v0, v6, v7\n" \
  "v_min3_u32 v0, v0, v8, v9\n" \
  "v_min3_u32 v0, v0, v10, v11\n" \
  "v_min3_u32 v0, v0, v12, v13\n" \
  "v_min3_u32 v0, v0, v14, v15\n" \
  "v_min3_u32 v0, v0, v16, v17\n" \
  "v_min3_u32 v0, v0, v18, v19\n" \
  "v_min3_u32 v0, v0, v20, v21\n" \
  "v_min3_u32 v0, v0, v22, v23\n" \
  "v_min3_u32 v0, v0, v24, v25\n" \
  "v_min3_u32 v0, v0, v26, v27\n" \
  "v_min3_u32 v0, v0, v28, v29\n" \
  "v_min3_u32 v0, v0, v30, v31\n" \
  "v_cmp_eq_u32 vcc, 0, v0\n" \
  "s_cbranch_vccz LOK_%=\n" \
  "s_cmp_eq_u32 s58, 0\n" \
  "s_cbranch_scc1 LOK_%=\n" \
  "s_sub_u32 s58, s58, 1\n" \
  "v_mov_b64 v[32:33], 0\n" \
  "v_mov_b64 v[34:35], 0\n" \
  "v_mov_b64 v[36:37], 0\n" \
  "v_mov_b64 v[38:39], 0\n" \
  "v_mov_b64 v[40:41], 0\n" \
  "v_mov_b64 v[42:43], 0\n" \
  "s_add_i32 s65, s65, 1\n" \
  "LFc_%=:\n" \
  "s_mov_b64 exec, 1\n" \
  "buffer_load_dword v72, off, s[0:3], s60 sc1\n" \
  "s_mov_b64 exec, -1\n" \
  "s_waitcnt vmcnt(0)\n" \
  "v_readfirstlane_b32 s57, v72\n" \
  "s_cmp_lg_u32 s57, s66\n" \
  "s_cbranch_scc1 LFcx_%=\n" \
  "s_cmp_eq_u32 s58, 0\n" \
  "s_cbranch_scc1 LFcx_%=\n" \
  "s_sub_u32 s58, s58, 1\n" \
  "s_branch LFc_%=\n" \
  "LFcx_%=:\n" \
  "s_bitcmp1_b32 s57, 1\n" \
  "s_cselect_b32 s61, s16, 0\n" \
  "s_add_i32 s57, s57, s61\n" \
  "s_add_i32 s57, s57, s14\n" \
  "s_waitcnt lgkmcnt(0)\n" \
  "s_memrealtime s[72:73]\n" \
  "s_nop 2\n" \
  "s_memrealtime s[74:75]\n" \
  "s_nop 2\n" \
  "s_memrealtime s[76:77]\n" \
  "s_memtime vcc\n" \
  "s_nop 1\n" \
  "s_memrealtime s[78:79]\n" \
  "s_nop 2\n" \
  "s_memrealtime s[80:81]\n" \
  "s_nop 2\n" \
  "s_memrealtime s[82:83]\n" \
  "s_waitcnt lgkmcnt(0)\n" \
  "s_add_i32 s72, s72, s74\n" \
  "s_add_i32 s72, s72, s76\n" \
  "s_add_i32 s72, s72, s78\n" \
  "s_add_i32 s72, s72, s80\n" \
  "s_add_i32 s72, s72, s82\n" \
  "s_lshl_b32 s72, s72, 2\n" \
  "s_sub_i32 s67, s72, vcc_lo\n" \
  "s_sub_i32 s74, s57, s67\n" \
  "s_getpc_b64 s[76:77]\n" \
  "LPz_%=:\n" \
  "s_add_u32 s76, s76, LEz_%=-LPz_%=\n" \
  "s_addc_u32 s77, s77, 0\n" \
  "s_sub_i32 s74, s74, 128\n" \
  "LWz_%=:\n" \
  "s_memtime s[72:73]\n" \
  "s_waitcnt lgkmcnt(0)\n" \
  "s_sub_i32 s72, s74, s72\n" \
  "s_cmp_gt_u32 s72, 0x8000\n" \
  "s_cselect_b32 s72, 0, s72\n" \
  "s_cmp_gt_u32 s72, 255\n" \
  "s_cbranch_scc1 LWz_%=\n" \
  "s_lshr_b32 s72, s72, 3\n" \
  "s_lshl_b32 s72, s72, 2\n" \
  "s_sub_u32 s76, s76, s72\n" \
  "s_subb_u32 s77, s77, 0\n" \
  "s_setpc_b64 s[76:77]\n" \
  "s_nop 1\n" \
  "s_nop 1\n" \
  "s_nop 1\n" \
  "s_nop 1\n" \
  "s_nop 1\n" \
  "s_nop 1\n" \
  "s_nop 1\n" \
  "s_nop 1\n" \
  "s_nop 1\n" \
  "s_nop 1\n" \
  "s_nop 1\n" \
  "s_nop 1\n" \
  "s_nop 1\n" \
  "s_nop 1\n" \
  "s_nop 1\n" \
  "s_nop 1\n" \
  "s_nop 1\n" \
  "s_nop 1\n" \
  "s_nop 1\n" \
  "s_nop 1\n" \
  "s_nop 1\n" \
  "s_nop 1\n" \
  "s_nop 1\n" \
  "s_nop 1\n" \
  "s_nop 1\n" \
  "s_nop 1\n" \
  "s_nop 1\n" \
  "s_nop 1\n" \
  "s_nop 1\n" \
  "s_nop 1\n" \
  "s_nop 1\n" \
  "s_nop 1\n" \
  "LEz_%=:\n" \
  "s_branch LAGAIN_%=\n" \
  "LOUT_%=:\n" \
  "s_endpgm\n"

constexpr u32 L_SCAL = 146432;
DI void put1(u32& a, u4 v) { lds_st<u4>(a, v); a += 16; }
DI void put1(u32& a, u32 v) { lds_st<u32>(a, v); a += 4; }
DI void put1(u32& a, int v) { lds_st<u32>(a, (u32)v); a += 4; }
template <class... T> DI void put_scalars(int w, T... v) { u32 a = L_SCAL + w * 256; (put1(a, v), ...); WAIT_LGKM(0); }
#define RUN_ASM(TEXT) do { asm volatile(TEXT ::"s"(w * 256) : "v255", "a255", "s99", "memory"); __builtin_unreachable(); } while (0)
DI void role_x(int xi, int cp, const u16* Wq, const u16* Wk, const u16* Wv, char* ring, u32 pol, char* qkv, char* oT, char* flg, int L0, int L1, Tm tm) {
  const int tid = threadIdx.x, lane = tid & 63, w = __builtin_amdgcn_readfirstlane(tid >> 6);
  const int c = lane & 15, g = lane >> 4, h = xi >> 1, half = xi & 1;
  const u32 vlin = tid * 16, mb = R_M + cp * R_MCOPY;
  const int s9 = 2 * g + (c & 1);
  const u32 aQr = XL_Q + g * 128 + (c & 7) * 16;
  const u32 colq = (h * 128 + 32 * w + 2 * c) * 2, colv = (h * 128 + 64 * half + 16 * w + c) * 2;
  const u32 lA = (48 * (g >> 1) + 4 * (g & 1)) * 8192, lB = (16 * (g >> 1) + 4 * (g & 1)) * 8192;
  const u32 tb[21] = {
      vlin, rmap(mb + vlin, pol), rmap(mb + 4096 + vlin, pol), tid < 136 ? rmap(mb + 8192 + vlin, pol) : OOB,
      (u32)(s9 * 384 + (c >> 1) * 48), (u32)((24 + s9 / 3) * 384 + (c >> 1) * 48 + (s9 % 3) * 16),
      XL_Q + (4 * w + (c >> 2)) * 128 + 2 * g * 16 + (c & 3) * 4, aQr, c < 8 ? aQr + w * 512 : XL_Z, c < 8 ? XL_Z : aQr + w * 512,
      XL_K + (w >> 1) * 1024 + (2 * (w & 1) + (c >> 3)) * 256 + (4 * g + (c & 1)) * 16 + ((c >> 1) & 3) * 4,
      (u32)lane * 16, c < 8 ? rmap(R_O + (c >> 2) * 32768 + h * 1024 + half * 512 + w * 128 + (c & 3) * 32 + g * 8, pol) : OOB,
      (u32)(g * 512 + c * 16), (u32)(g * 1024 + c * 16), (lane & 1) ? 0xEEEEu : 0x4444u,
      lA + colq, lB + colq, lA + colv, lB + colv, (u32)(16 * (w & 1) + c - 4 * g)};
#pragma unroll
  for (int i = 0; i < 21; ++i) lds_st<u32>(XL_T + i * 1024 + tid * 4, tb[i]);
  if (tid < 4) lds_st<u32>(XL_Z + tid * 4, 0u);
  WAIT_LGKM(0);
  const u4 dR = mkdesc(ring, 2u << 20), dQ = mkdesc(qkv, 48u << 20), dO = mkdesc(oT, 16u << 20), dF = mkdesc(flg, F_LIM);
  const u4 dWq = mkdesc(Wq, 640 * 8192), dWk = mkdesc(Wk, 640 * 8192), dWv = mkdesc(Wv, 640 * 8192);
  put_scalars(w, (dR), (dQ), (dO), (dWq), (dWk), (dWv), (dF), (w * 256), (h * 24576), (16384 + half * 4096),
      ((h * 16 + half * 8 + (w >> 1) * 4) * 256 + (w & 1) * 131072), (tm.P), (__builtin_amdgcn_readfirstlane((int)(xi == 0 && w == 0))), (L0), (L1),
      (F_QKV + h * 256), (w), (F_DBG + (xi * 4 + w) * 2048), (F_OT + xi * 128), (2 * R_FLG + pol * 8192), (cp * 128), (tm.g), (4 + half), (half * 4 + w) * 3072);
  RUN_ASM(X_ASM0);

}

DI void role_y(int rg, int j, int cp, int oc, const u16* Wo, char* ring, u32 pol, char* flg, int L0, int L1, Tm tm) {
  const int tid = threadIdx.x, lane = tid & 63, w = __builtin_amdgcn_readfirstlane(tid >> 6);
  const int c = lane & 15, g = lane >> 4, mm = c & 7;
  const u32 row = (w * 1024 + 64 * (c >> 3) + 16 * (g >> 1) + 4 * (g & 1)) * 1280;
  const bool val = (g >> 1) == (c >> 3);
  const u32 part = (2 * w + (g >> 1)) * 8, r0 = 2 * (g & 1);
  const u32 og = rmap(R_O + oc * R_OCOPY + rg * 32768 + w * 8192 + (c >> 3) * 512 + g * 128 + (c & 7) * 16, pol);
  auto om = [&](int k) { return lane < 48 ? rmap(R_M + k * R_MCOPY + j * 384 + rg * 192 + lane * 4, pol) : OOB; };
  const u32 tb[12] = {og, og + 4096, val ? (r0 * 12 + mm) * 80 + part : 4096 + tid * 8, val ? (r0 * 12 + 8 + (mm >> 1)) * 80 + part + (mm & 1) * 4 : 8192 + tid * 4,
                     (u32)(lane < 48 ? lane : 0) * 80, om(0),
                     (lane & 1) ? 0xEEEEu : 0x4444u, row + (24 * j + 2 * mm) * 2, j < 26 ? row + (24 * j + 16 + mm) * 2 : OOB, om(1), om(2), om(3)};
#pragma unroll
  for (int i = 0; i < 12; ++i) lds_st<u32>(16384 + i * 1024 + tid * 4, tb[i]);
  WAIT_LGKM(0);
  const u4 dR = mkdesc(ring, 2u << 20), dW = mkdesc(Wo, 4096 * 1280), dF = mkdesc(flg, F_LIM);
  put_scalars(w, (dR), (dW), (dF), (w * 256), (w), (tm.A), (tm.P), (tm.g), (L0 * 63), (L1 * 63),
      (2 * R_FLG + pol * 8192 + cp * 128), (F_DBG + ((64 + rg * 27 + j) * 4 + w) * 2048));
  RUN_ASM(Y_ASM0);

}

#define G2_ASM \
  "v_mbcnt_lo_u32_b32 v0, -1, 0\n" \
  "v_mbcnt_hi_u32_b32 v0, -1, v0\n" \
  "v_lshlrev_b32 v0, 2, v0\n" \
  "v_add_u32 v0, %0, v0\n" \
  "v_add_u32 v0, 146432, v0\n" \
  "ds_read_b32 v0, v0\n" \
  "s_waitcnt lgkmcnt(0)\n" \
  "v_readlane_b32 s0, v0, 0\n" \
  "v_readlane_b32 s1, v0, 1\n" \
  "v_readlane_b32 s2, v0, 2\n" \
  "v_readlane_b32 s3, v0, 3\n" \
  "v_readlane_b32 s4, v0, 4\n" \
  "v_readlane_b32 s5, v0, 5\n" \
  "v_readlane_b32 s6, v0, 6\n" \
  "v_readlane_b32 s7, v0, 7\n" \
  "v_readlane_b32 s8, v0, 8\n" \
  "v_readlane_b32 s9, v0, 9\n" \
  "v_readlane_b32 s10, v0, 10\n" \
  "v_readlane_b32 s11, v0, 11\n" \
  "v_readlane_b32 s12, v0, 12\n" \
  "v_readlane_b32 s13, v0, 13\n" \
  "v_readlane_b32 s14, v0, 14\n" \
  "v_readlane_b32 s15, v0, 15\n" \
  "v_readlane_b32 s16, v0, 16\n" \
  "s_nop 4\n" \
  "v_mbcnt_lo_u32_b32 v0, -1, 0\n" \
  "v_mbcnt_hi_u32_b32 v0, -1, v0\n" \
  "v_lshlrev_b32 v0, 2, v0\n" \
  "v_add_u32 v0, s8, v0\n" \
  "v_add_u32 v0, 32768, v0\n" \
  "ds_read_b32 v116, v0 offset:0\n" \
  "ds_read_b32 v117, v0 offset:1024\n" \
  "ds_read_b32 v118, v0 offset:2048\n" \
  "ds_read_b32 v119, v0 offset:3072\n" \
  "ds_read_b32 v120, v0 offset:4096\n" \
  "ds_read_b32 v121, v0 offset:5120\n" \
  "ds_read_b32 v122, v0 offset:6144\n" \
  "ds_read_b32 v123, v0 offset:7168\n" \
  "ds_read_b32 v124, v0 offset:8192\n" \
  "ds_read_b32 v125, v0 offset:9216\n" \
  "ds_read_b32 v126, v0 offset:10240\n" \
  "s_waitcnt lgkmcnt(0)\n" \
  "s_barrier\n" \
  "s_mov_b32 s57, 1048576\n" \
  "s_mov_b32 s56, 0\n" \
  "s_mov_b32 s64, 0x3c000000\n" \
  "s_mov_b32 s62, 0\n" \
  "LLAYER_%=:\n" \
  "s_mov_b32 s67, s56\n" \
  "s_mov_b32 s66, s16\n" \
  "s_lshr_b32 s58, s56, 6\n" \
  "s_mul_i32 s65, s58, s15\n" \
  "s_add_i32 s65, s65, s12\n" \
  "s_add_i32 s60, s56, s9\n" \
  "s_add_i32 s60, s60, 1\n" \
  "v_mov_b32 v60, 0\n" \
  "v_cmp_gt_u32 vcc, s60, v60\n" \
  "s_cbranch_vccz LSk0x_%=\n" \
  "LSk0_%=:\n" \
  "buffer_load_dword v60, v122, s[4:7], 0 offen sc1\n" \
  "s_waitcnt vmcnt(0)\n" \
  "v_cmp_gt_u32 vcc, s60, v60\n" \
  "s_cbranch_vccz LSk0x_%=\n" \
  "s_cmp_eq_u32 s57, 0\n" \
  "s_cbranch_scc1 LSk0x_%=\n" \
  "s_sub_u32 s57, s57, 1\n" \
  "s_sleep 2\n" \
  "s_branch LSk0_%=\n" \
  "LSk0x_%=:\n" \
  "buffer_load_dwordx4 a[0:3], v118, s[0:3], s65 offen sc1\n" \
  "buffer_load_dwordx2 a[4:5], v119, s[0:3], s65 offen sc1\n" \
  "buffer_load_dwordx4 a[6:9], v120, s[0:3], s65 offen sc1\n" \
  "buffer_load_dwordx4 a[10:13], v120, s[0:3], s65 offen offset:768 sc1\n" \
  "s_add_i32 s65, s65, 12288\n" \
  "s_add_i32 s60, s60, 4\n" \
  "v_cmp_gt_u32 vcc, s60, v60\n" \
  "s_cbranch_vccz LSk1x_%=\n" \
  "LSk1_%=:\n" \
  "buffer_load_dword v60, v122, s[4:7], 0 offen sc1\n" \
  "s_waitcnt vmcnt(0)\n" \
  "v_cmp_gt_u32 vcc, s60, v60\n" \
  "s_cbranch_vccz LSk1x_%=\n" \
  "s_cmp_eq_u32 s57, 0\n" \
  "s_cbranch_scc1 LSk1x_%=\n" \
  "s_sub_u32 s57, s57, 1\n" \
  "s_sleep 2\n" \
  "s_branch LSk1_%=\n" \
  "LSk1x_%=:\n" \
  "buffer_load_dwordx4 a[14:17], v118, s[0:3], s65 offen sc1\n" \
  "buffer_load_dwordx2 a[18:19], v119, s[0:3], s65 offen sc1\n" \
  "buffer_load_dwordx4 a[20:23], v120, s[0:3], s65 offen sc1\n" \
  "buffer_load_dwordx4 a[24:27], v120, s[0:3], s65 offen offset:768 sc1\n" \
  "s_add_i32 s65, s65, 12288\n" \
  "s_add_i32 s60, s60, 4\n" \
  "v_cmp_gt_u32 vcc, s60, v60\n" \
  "s_cbranch_vccz LSk2x_%=\n" \
  "LSk2_%=:\n" \
  "buffer_load_dword v60, v122, s[4:7], 0 offen sc1\n" \
  "s_waitcnt vmcnt(0)\n" \
  "v_cmp_gt_u32 vcc, s60, v60\n" \
  "s_cbranch_vccz LSk2x_%=\n" \
  "s_cmp_eq_u32 s57, 0\n" \
  "s_cbranch_scc1 LSk2x_%=\n" \
  "s_sub_u32 s57, s57, 1\n" \
  "s_sleep 2\n" \
  "s_branch LSk2_%=\n" \
  "LSk2x_%=:\n" \
  "buffer_load_dwordx4 a[28:31], v118, s[0:3], s65 offen sc1\n" \
  "buffer_load_dwordx2 a[32:33], v119, s[0:3], s65 offen sc1\n" \
  "buffer_load_dwordx4 a[34:37], v120, s[0:3], s65 offen sc1\n" \
  "buffer_load_dwordx4 a[38:41], v120, s[0:3], s65 offen offset:768 sc1\n" \
  "s_add_i32 s65, s65, 12288\n" \
  "s_add_i32 s60, s60, 4\n" \
  "v_cmp_gt_u32 vcc, s60, v60\n" \
  "s_cbranch_vccz LSk3x_%=\n" \
  "LSk3_%=:\n" \
  "buffer_load_dword v60, v122, s[4:7], 0 offen sc1\n" \
  "s_waitcnt vmcnt(0)\n" \
  "v_cmp_gt_u32 vcc, s60, v60\n" \
  "s_cbranch_vccz LSk3x_%=\n" \
  "s_cmp_eq_u32 s57, 0\n" \
  "s_cbranch_scc1 LSk3x_%=\n" \
  "s_sub_u32 s57, s57, 1\n" \
  "s_sleep 2\n" \
  "s_branch LSk3_%=\n" \
  "LSk3x_%=:\n" \
  "buffer_load_dwordx4 a[42:45], v118, s[0:3], s65 offen sc1\n" \
  "buffer_load_dwordx2 a[46:47], v119, s[0:3], s65 offen sc1\n" \
  "buffer_load_dwordx4 a[48:51], v120, s[0:3], s65 offen sc1\n" \
  "buffer_load_dwordx4 a[52:55], v120, s[0:3], s65 offen offset:768 sc1\n" \
  "s_add_i32 s65, s65, 12288\n" \
  "s_add_i32 s60, s60, 4\n" \
  "v_cmp_gt_u32 vcc, s60, v60\n" \
  "s_cbranch_vccz LSk4x_%=\n" \
  "LSk4_%=:\n" \
  "buffer_load_dword v60, v122, s[4:7], 0 offen sc1\n" \
  "s_waitcnt vmcnt(0)\n" \
  "v_cmp_gt_u32 vcc, s60, v60\n" \
  "s_cbranch_vccz LSk4x_%=\n" \
  "s_cmp_eq_u32 s57, 0\n" \
  "s_cbranch_scc1 LSk4x_%=\n" \
  "s_sub_u32 s57, s57, 1\n" \
  "s_sleep 2\n" \
  "s_branch LSk4_%=\n" \
  "LSk4x_%=:\n" \
  "buffer_load_dwordx4 a[56:59], v118, s[0:3], s65 offen sc1\n" \
  "buffer_load_dwordx2 a[60:61], v119, s[0:3], s65 offen sc1\n" \
  "buffer_load_dwordx4 a[62:65], v120, s[0:3], s65 offen sc1\n" \
  "buffer_load_dwordx4 a[66:69], v120, s[0:3], s65 offen offset:768 sc1\n" \
  "s_add_i32 s65, s65, 12288\n" \
  "s_add_i32 s60, s60, 4\n" \
  "v_cmp_gt_u32 vcc, s60, v60\n" \
  "s_cbranch_vccz LSk5x_%=\n" \
  "LSk5_%=:\n" \
  "buffer_load_dword v60, v122, s[4:7], 0 offen sc1\n" \
  "s_waitcnt vmcnt(0)\n" \
  "v_cmp_gt_u32 vcc, s60, v60\n" \
  "s_cbranch_vccz LSk5x_%=\n" \
  "s_cmp_eq_u32 s57, 0\n" \
  "s_cbranch_scc1 LSk5x_%=\n" \
  "s_sub_u32 s57, s57, 1\n" \
  "s_sleep 2\n" \
  "s_branch LSk5_%=\n" \
  "LSk5x_%=:\n" \
  "buffer_load_dwordx4 a[70:73], v118, s[0:3], s65 offen sc1\n" \
  "buffer_load_dwordx2 a[74:75], v119, s[0:3], s65 offen sc1\n" \
  "buffer_load_dwordx4 a[76:79], v120, s[0:3], s65 offen sc1\n" \
  "buffer_load_dwordx4 a[80:83], v120, s[0:3], s65 offen offset:768 sc1\n" \
  "s_add_i32 s65, s65, 12288\n" \
  "s_add_i32 s60, s60, 4\n" \
  "v_cmp_gt_u32 vcc, s60, v60\n" \
  "s_cbranch_vccz LSk6x_%=\n" \
  "LSk6_%=:\n" \
  "buffer_load_dword v60, v122, s[4:7], 0 offen sc1\n" \
  "s_waitcnt vmcnt(0)\n" \
  "v_cmp_gt_u32 vcc, s60, v60\n" \
  "s_cbranch_vccz LSk6x_%=\n" \
  "s_cmp_eq_u32 s57, 0\n" \
  "s_cbranch_scc1 LSk6x_%=\n" \
  "s_sub_u32 s57, s57, 1\n" \
  "s_sleep 2\n" \
  "s_branch LSk6_%=\n" \
  "LSk6x_%=:\n" \
  "buffer_load_dwordx4 a[84:87], v118, s[0:3], s65 offen sc1\n" \
  "buffer_load_dwordx2 a[88:89], v119, s[0:3], s65 offen sc1\n" \
  "buffer_load_dwordx4 a[90:93], v120, s[0:3], s65 offen sc1\n" \
  "buffer_load_dwordx4 a[94:97], v120, s[0:3], s65 offen offset:768 sc1\n" \
  "s_add_i32 s65, s65, 12288\n" \
  "s_add_i32 s60, s60, 4\n" \
  "v_cmp_gt_u32 vcc, s60, v60\n" \
  "s_cbranch_vccz LSk7x_%=\n" \
  "LSk7_%=:\n" \
  "buffer_load_dword v60, v122, s[4:7], 0 offen sc1\n" \
  "s_waitcnt vmcnt(0)\n" \
  "v_cmp_gt_u32 vcc, s60, v60\n" \
  "s_cbranch_vccz LSk7x_%=\n" \
  "s_cmp_eq_u32 s57, 0\n" \
  "s_cbranch_scc1 LSk7x_%=\n" \
  "s_sub_u32 s57, s57, 1\n" \
  "s_sleep 2\n" \
  "s_branch LSk7_%=\n" \
  "LSk7x_%=:\n" \
  "buffer_load_dwordx4 a[98:101], v118, s[0:3], s65 offen sc1\n" \
  "buffer_load_dwordx2 a[102:103], v119, s[0:3], s65 offen sc1\n" \
  "buffer_load_dwordx4 a[104:107], v120, s[0:3], s65 offen sc1\n" \
  "buffer_load_dwordx4 a[108:111], v120, s[0:3], s65 offen offset:768 sc1\n" \
  "s_add_i32 s65, s65, 12288\n" \
  "s_add_i32 s60, s60, 4\n" \
  "v_cmp_gt_u32 vcc, s60, v60\n" \
  "s_cbranch_vccz LSk8x_%=\n" \
  "LSk8_%=:\n" \
  "buffer_load_dword v60, v122, s[4:7], 0 offen sc1\n" \
  "s_waitcnt vmcnt(0)\n" \
  "v_cmp_gt_u32 vcc, s60, v60\n" \
  "s_cbranch_vccz LSk8x_%=\n" \
  "s_cmp_eq_u32 s57, 0\n" \
  "s_cbranch_scc1 LSk8x_%=\n" \
  "s_sub_u32 s57, s57, 1\n" \
  "s_sleep 2\n" \
  "s_branch LSk8_%=\n" \
  "LSk8x_%=:\n" \
  "buffer_load_dwordx4 a[112:115], v118, s[0:3], s65 offen sc1\n" \
  "buffer_load_dwordx2 a[116:117], v119, s[0:3], s65 offen sc1\n" \
  "buffer_load_dwordx4 a[118:121], v120, s[0:3], s65 offen sc1\n" \
  "buffer_load_dwordx4 a[122:125], v120, s[0:3], s65 offen offset:768 sc1\n" \
  "s_add_i32 s65, s65, 12288\n" \
  "s_add_i32 s60, s60, 4\n" \
  "v_cmp_gt_u32 vcc, s60, v60\n" \
  "s_cbranch_vccz LSk9x_%=\n" \
  "LSk9_%=:\n" \
  "buffer_load_dword v60, v122, s[4:7], 0 offen sc1\n" \
  "s_waitcnt vmcnt(0)\n" \
  "v_cmp_gt_u32 vcc, s60, v60\n" \
  "s_cbranch_vccz LSk9x_%=\n" \
  "s_cmp_eq_u32 s57, 0\n" \
  "s_cbranch_scc1 LSk9x_%=\n" \
  "s_sub_u32 s57, s57, 1\n" \
  "s_sleep 2\n" \
  "s_branch LSk9_%=\n" \
  "LSk9x_%=:\n" \
  "buffer_load_dwordx4 a[126:129], v118, s[0:3], s65 offen sc1\n" \
  "buffer_load_dwordx2 a[130:131], v119, s[0:3], s65 offen sc1\n" \
  "buffer_load_dwordx4 a[132:135], v120, s[0:3], s65 offen sc1\n" \
  "buffer_load_dwordx4 a[136:139], v120, s[0:3], s65 offen offset:768 sc1\n" \
  "s_add_i32 s65, s65, 12288\n" \
  "s_add_i32 s60, s60, 4\n" \
  "v_cmp_gt_u32 vcc, s60, v60\n" \
  "s_cbranch_vccz LSk10x_%=\n" \
  "LSk10_%=:\n" \
  "buffer_load_dword v60, v122, s[4:7], 0 offen sc1\n" \
  "s_waitcnt vmcnt(0)\n" \
  "v_cmp_gt_u32 vcc, s60, v60\n" \
  "s_cbranch_vccz LSk10x_%=\n" \
  "s_cmp_eq_u32 s57, 0\n" \
  "s_cbranch_scc1 LSk10x_%=\n" \
  "s_sub_u32 s57, s57, 1\n" \
  "s_sleep 2\n" \
  "s_branch LSk10_%=\n" \
  "LSk10x_%=:\n" \
  "buffer_load_dwordx4 a[140:143], v118, s[0:3], s65 offen sc1\n" \
  "buffer_load_dwordx2 a[144:145], v119, s[0:3], s65 offen sc1\n" \
  "buffer_load_dwordx4 a[146:149], v120, s[0:3], s65 offen sc1\n" \
  "buffer_load_dwordx4 a[150:153], v120, s[0:3], s65 offen offset:768 sc1\n" \
  "s_add_i32 s65, s65, 12288\n" \
  "s_add_i32 s60, s60, 4\n" \
  "v_cmp_gt_u32 vcc, s60, v60\n" \
  "s_cbranch_vccz LSk11x_%=\n" \
  "LSk11_%=:\n" \
  "buffer_load_dword v60, v122, s[4:7], 0 offen sc1\n" \
  "s_waitcnt vmcnt(0)\n" \
  "v_cmp_gt_u32 vcc, s60, v60\n" \
  "s_cbranch_vccz LSk11x_%=\n" \
  "s_cmp_eq_u32 s57, 0\n" \
  "s_cbranch_scc1 LSk11x_%=\n" \
  "s_sub_u32 s57, s57, 1\n" \
  "s_sleep 2\n" \
  "s_branch LSk11_%=\n" \
  "LSk11x_%=:\n" \
  "buffer_load_dwordx4 a[154:157], v118, s[0:3], s65 offen sc1\n" \
  "buffer_load_dwordx2 a[158:159], v119, s[0:3], s65 offen sc1\n" \
  "buffer_load_dwordx4 a[160:163], v120, s[0:3], s65 offen sc1\n" \
  "buffer_load_dwordx4 a[164:167], v120, s[0:3], s65 offen offset:768 sc1\n" \
  "s_add_i32 s65, s65, 12288\n" \
  "s_add_i32 s60, s60, 4\n" \
  "s_waitcnt vmcnt(32)\n" \
  "v_cmp_gt_u32 vcc, s60, v60\n" \
  "s_cbranch_vccz LSk12x_%=\n" \
  "LSk12_%=:\n" \
  "buffer_load_dword v60, v122, s[4:7], 0 offen sc1\n" \
  "s_waitcnt vmcnt(0)\n" \
  "v_cmp_gt_u32 vcc, s60, v60\n" \
  "s_cbranch_vccz LSk12x_%=\n" \
  "s_cmp_eq_u32 s57, 0\n" \
  "s_cbranch_scc1 LSk12x_%=\n" \
  "s_sub_u32 s57, s57, 1\n" \
  "s_sleep 2\n" \
  "s_branch LSk12_%=\n" \
  "LSk12x_%=:\n" \
  "buffer_load_dwordx4 a[168:171], v118, s[0:3], s65 offen sc1\n" \
  "buffer_load_dwordx2 a[172:173], v119, s[0:3], s65 offen sc1\n" \
  "buffer_load_dwordx4 a[174:177], v120, s[0:3], s65 offen sc1\n" \
  "buffer_load_dwordx4 a[178:181], v120, s[0:3], s65 offen offset:768 sc1\n" \
  "s_add_i32 s65, s65, 12288\n" \
  "s_add_i32 s60, s60, 4\n" \
  "v_cmp_gt_u32 vcc, s60, v60\n" \
  "s_cbranch_vccz LSk13x_%=\n" \
  "LSk13_%=:\n" \
  "buffer_load_dword v60, v122, s[4:7], 0 offen sc1\n" \
  "s_waitcnt vmcnt(0)\n" \
  "v_cmp_gt_u32 vcc, s60, v60\n" \
  "s_cbranch_vccz LSk13x_%=\n" \
  "s_cmp_eq_u32 s57, 0\n" \
  "s_cbranch_scc1 LSk13x_%=\n" \
  "s_sub_u32 s57, s57, 1\n" \
  "s_sleep 2\n" \
  "s_branch LSk13_%=\n" \
  "LSk13x_%=:\n" \
  "buffer_load_dwordx4 a[182:185], v118, s[0:3], s65 offen sc1\n" \
  "buffer_load_dwordx2 a[186:187], v119, s[0:3], s65 offen sc1\n" \
  "buffer_load_dwordx4 a[188:191], v120, s[0:3], s65 offen sc1\n" \
  "buffer_load_dwordx4 a[192:195], v120, s[0:3], s65 offen offset:768 sc1\n" \
  "s_add_i32 s65, s65, 12288\n" \
  "s_add_i32 s60, s60, 4\n" \
  "v_cmp_gt_u32 vcc, s60, v60\n" \
  "s_cbranch_vccz LSk14x_%=\n" \
  "LSk14_%=:\n" \
  "buffer_load_dword v60, v122, s[4:7], 0 offen sc1\n" \
  "s_waitcnt vmcnt(0)\n" \
  "v_cmp_gt_u32 vcc, s60, v60\n" \
  "s_cbranch_vccz LSk14x_%=\n" \
  "s_cmp_eq_u32 s57, 0\n" \
  "s_cbranch_scc1 LSk14x_%=\n" \
  "s_sub_u32 s57, s57, 1\n" \
  "s_sleep 2\n" \
  "s_branch LSk14_%=\n" \
  "LSk14x_%=:\n" \
  "buffer_load_dwordx4 a[196:199], v118, s[0:3], s65 offen sc1\n" \
  "buffer_load_dwordx2 a[200:201], v119, s[0:3], s65 offen sc1\n" \
  "buffer_load_dwordx4 a[202:205], v120, s[0:3], s65 offen sc1\n" \
  "buffer_load_dwordx4 a[206:209], v120, s[0:3], s65 offen offset:768 sc1\n" \
  "s_add_i32 s65, s65, 12288\n" \
  "s_add_i32 s60, s60, 4\n" \
  "v_cmp_gt_u32 vcc, s60, v60\n" \
  "s_cbranch_vccz LSk15x_%=\n" \
  "LSk15_%=:\n" \
  "buffer_load_dword v60, v122, s[4:7], 0 offen sc1\n" \
  "s_waitcnt vmcnt(0)\n" \
  "v_cmp_gt_u32 vcc, s60, v60\n" \
  "s_cbranch_vccz LSk15x_%=\n" \
  "s_cmp_eq_u32 s57, 0\n" \
  "s_cbranch_scc1 LSk15x_%=\n" \
  "s_sub_u32 s57, s57, 1\n" \
  "s_sleep 2\n" \
  "s_branch LSk15_%=\n" \
  "LSk15x_%=:\n" \
  "buffer_load_dwordx4 a[210:213], v118, s[0:3], s65 offen sc1\n" \
  "buffer_load_dwordx2 a[214:215], v119, s[0:3], s65 offen sc1\n" \
  "buffer_load_dwordx4 a[216:219], v120, s[0:3], s65 offen sc1\n" \
  "buffer_load_dwordx4 a[220:223], v120, s[0:3], s65 offen offset:768 sc1\n" \
  "s_add_i32 s65, s65, 12288\n" \
  "s_add_i32 s60, s60, 4\n" \
  "s_mov_b32 s61, 0\n" \
  "LTOK_%=:\n" \
  "s_cmp_lg_u32 s61, 0\n" \
  "s_cbranch_scc1 LPRE_%=\n" \
  "s_waitcnt vmcnt(0)\n" \
  "s_cmp_lg_u32 s9, 0\n" \
  "s_cbranch_scc1 LNFa_%=\n" \
  "v_mov_b32 v61, s56\n" \
  "s_mov_b64 exec, 1\n" \
  "buffer_store_dword v61, off, s[4:7], s13 sc1\n" \
  "s_mov_b64 exec, -1\n" \
  "LNFa_%=:\n" \
  "s_lshr_b32 s60, s66, 5\n" \
  "s_add_i32 s60, s60, s67\n" \
  "s_add_i32 s60, s60, 1\n" \
  "LSq_%=:\n" \
  "buffer_load_dword v60, v121, s[4:7], 0 offen sc1\n" \
  "s_waitcnt vmcnt(0)\n" \
  "v_cmp_gt_u32 vcc, s60, v60\n" \
  "s_cbranch_vccz LSqx_%=\n" \
  "s_cmp_eq_u32 s57, 0\n" \
  "s_cbranch_scc1 LSqx_%=\n" \
  "s_sub_u32 s57, s57, 1\n" \
  "s_sleep 2\n" \
  "s_branch LSq_%=\n" \
  "LSqx_%=:\n" \
  "s_lshr_b32 s59, s67, 6\n" \
  "s_mul_i32 s59, s59, s15\n" \
  "s_mul_i32 s58, s66, 1280\n" \
  "s_add_i32 s59, s59, s58\n" \
  "s_add_i32 s59, s59, s10\n" \
  "buffer_load_dwordx4 v[54:57], v116, s[0:3], s59 offen sc1\n" \
  "buffer_load_dwordx2 v[58:59], v117, s[0:3], s59 offen sc1\n" \
  "s_waitcnt vmcnt(0)\n" \
  "LPRE_%=:\n" \
  "v_mov_b64 v[48:49], v[54:55]\n" \
  "v_mov_b64 v[50:51], v[56:57]\n" \
  "v_mov_b64 v[52:53], v[58:59]\n" \
  "s_add_i32 s60, s66, 35\n" \
  "s_lshr_b32 s59, s60, 5\n" \
  "s_add_i32 s59, s59, s67\n" \
  "v_cmp_ge_u32 vcc, s59, v60\n" \
  "s_mov_b32 s61, 0\n" \
  "s_mov_b32 s63, 0\n" \
  "s_cbranch_vccnz LNP_%=\n" \
  "s_cmp_ge_u32 s60, 2048\n" \
  "s_cbranch_scc1 LNP_%=\n" \
  "s_mov_b32 s61, 1\n" \
  "s_lshr_b32 s59, s67, 6\n" \
  "s_mul_i32 s59, s59, s15\n" \
  "s_mul_i32 s58, s60, 1280\n" \
  "s_add_i32 s59, s59, s58\n" \
  "s_add_i32 s59, s59, s10\n" \
  "buffer_load_dwordx4 v[54:57], v116, s[0:3], s59 offen sc1\n" \
  "buffer_load_dwordx2 v[58:59], v117, s[0:3], s59 offen sc1\n" \
  "LNP_%=:\n" \
  "buffer_load_dword v60, v121, s[4:7], 0 offen sc1\n" \
  "LRUN_%=:\n" \
  "v_mfma_f32_32x32x16_bf16 v[0:15], a[0:3], v[48:51], 0\n" \
  "v_mov_b64 v[32:33], 0\n" \
  "v_mov_b64 v[34:35], 0\n" \
  "v_mov_b64 v[36:37], 0\n" \
  "v_mov_b64 v[38:39], 0\n" \
  "v_mfma_f32_32x32x8_bf16 v[0:15], a[4:5], v[52:53], v[0:15]\n" \
  "v_mov_b64 v[40:41], 0\n" \
  "v_mov_b64 v[42:43], 0\n" \
  "v_mov_b64 v[44:45], 0\n" \
  "v_mov_b64 v[46:47], 0\n" \
  "s_nop 7\n" \
  "v_exp_f32 v0, v0\n" \
  "v_exp_f32 v1, v1\n" \
  "v_exp_f32 v2, v2\n" \
  "v_exp_f32 v3, v3\n" \
  "v_exp_f32 v4, v4\n" \
  "v_exp_f32 v5, v5\n" \
  "v_mfma_f32_32x32x16_bf16 v[16:31], a[14:17], v[48:51], 0\n" \
  "v_exp_f32 v6, v6\n" \
  "v_exp_f32 v7, v7\n" \
  "v_exp_f32 v8, v8\n" \
  "v_exp_f32 v9, v9\n" \
  "v_mfma_f32_32x32x8_bf16 v[16:31], a[18:19], v[52:53], v[16:31]\n" \
  "v_exp_f32 v10, v10\n" \
  "v_exp_f32 v11, v11\n" \
  "v_exp_f32 v12, v12\n" \
  "v_exp_f32 v13, v13\n" \
  "v_smfmac_f32_32x32x32_bf16 v[32:47], a[6:9], v[0:7], v126\n" \
  "v_exp_f32 v14, v14\n" \
  "v_exp_f32 v15, v15\n" \
  "v_exp_f32 v16, v16\n" \
  "v_exp_f32 v17, v17\n" \
  "v_smfmac_f32_32x32x32_bf16 v[32:47], a[10:13], v[8:15], v126\n" \
  "v_exp_f32 v18, v18\n" \
  "v_exp_f32 v19, v19\n" \
  "v_exp_f32 v20, v20\n" \
  "v_exp_f32 v21, v21\n" \
  "v_mfma_f32_32x32x16_bf16 v[0:15], a[28:31], v[48:51], 0\n" \
  "v_exp_f32 v22, v22\n" \
  "v_exp_f32 v23, v23\n" \
  "v_exp_f32 v24, v24\n" \
  "v_exp_f32 v25, v25\n" \
  "v_mfma_f32_32x32x8_bf16 v[0:15], a[32:33], v[52:53], v[0:15]\n" \
  "v_exp_f32 v26, v26\n" \
  "v_exp_f32 v27, v27\n" \
  "v_exp_f32 v28, v28\n" \
  "v_exp_f32 v29, v29\n" \
  "v_smfmac_f32_32x32x32_bf16 v[32:47], a[20:23], v[16:23], v126\n" \
  "v_exp_f32 v30, v30\n" \
  "v_exp_f32 v31, v31\n" \
  "v_exp_f32 v0, v0\n" \
  "v_exp_f32 v1, v1\n" \
  "v_smfmac_f32_32x32x32_bf16 v[32:47], a[24:27], v[24:31], v126\n" \
  "v_exp_f32 v2, v2\n" \
  "v_exp_f32 v3, v3\n" \
  "v_exp_f32 v4, v4\n" \
  "v_exp_f32 v5, v5\n" \
  "v_mfma_f32_32x32x16_bf16 v[16:31], a[42:45], v[48:51], 0\n" \
  "v_exp_f32 v6, v6\n" \
  "v_exp_f32 v7, v7\n" \
  "v_exp_f32 v8, v8\n" \
  "v_exp_f32 v9, v9\n" \
  "v_mfma_f32_32x32x8_bf16 v[16:31], a[46:47], v[52:53], v[16:31]\n" \
  "v_exp_f32 v10, v10\n" \
  "v_exp_f32 v11, v11\n" \
  "v_exp_f32 v12, v12\n" \
  "v_exp_f32 v13, v13\n" \
  "v_smfmac_f32_32x32x32_bf16 v[32:47], a[34:37], v[0:7], v126\n" \
  "v_exp_f32 v14, v14\n" \
  "v_exp_f32 v15, v15\n" \
  "v_exp_f32 v16, v16\n" \
  "v_exp_f32 v17, v17\n" \
  "v_smfmac_f32_32x32x32_bf16 v[32:47], a[38:41], v[8:15], v126\n" \
  "v_exp_f32 v18, v18\n" \
  "v_exp_f32 v19, v19\n" \
  "v_exp_f32 v20, v20\n" \
  "v_exp_f32 v21, v21\n" \
  "v_mfma_f32_32x32x16_bf16 v[0:15], a[56:59], v[48:51], 0\n" \
  "v_exp_f32 v22, v22\n" \
  "v_exp_f32 v23, v23\n" \
  "v_exp_f32 v24, v24\n" \
  "v_exp_f32 v25, v25\n" \
  "v_mfma_f32_32x32x8_bf16 v[0:15], a[60:61], v[52:53], v[0:15]\n" \
  "v_exp_f32 v26, v26\n" \
  "v_exp_f32 v27, v27\n" \
  "v_exp_f32 v28, v28\n" \
  "v_exp_f32 v29, v29\n" \
  "v_smfmac_f32_32x32x32_bf16 v[32:47], a[48:51], v[16:23], v126\n" \
  "v_exp_f32 v30, v30\n" \
  "v_exp_f32 v31, v31\n" \
  "v_exp_f32 v0, v0\n" \
  "v_exp_f32 v1, v1\n" \
  "v_smfmac_f32_32x32x32_bf16 v[32:47], a[52:55], v[24:31], v126\n" \
  "v_exp_f32 v2, v2\n" \
  "v_exp_f32 v3, v3\n" \
  "v_exp_f32 v4, v4\n" \
  "v_exp_f32 v5, v5\n" \
  "v_mfma_f32_32x32x16_bf16 v[16:31], a[70:73], v[48:51], 0\n" \
  "v_exp_f32 v6, v6\n" \
  "v_exp_f32 v7, v7\n" \
  "v_exp_f32 v8, v8\n" \
  "v_exp_f32 v9, v9\n" \
  "v_mfma_f32_32x32x8_bf16 v[16:31], a[74:75], v[52:53], v[16:31]\n" \
  "v_exp_f32 v10, v10\n" \
  "v_exp_f32 v11, v11\n" \
  "v_exp_f32 v12, v12\n" \
  "v_exp_f32 v13, v13\n" \
  "v_smfmac_f32_32x32x32_bf16 v[32:47], a[62:65], v[0:7], v126\n" \
  "v_exp_f32 v14, v14\n" \
  "v_exp_f32 v15, v15\n" \
  "v_exp_f32 v16, v16\n" \
  "v_exp_f32 v17, v17\n" \
  "v_smfmac_f32_32x32x32_bf16 v[32:47], a[66:69], v[8:15], v126\n" \
  "v_exp_f32 v18, v18\n" \
  "v_exp_f32 v19, v19\n" \
  "v_exp_f32 v20, v20\n" \
  "v_exp_f32 v21, v21\n" \
  "v_mfma_f32_32x32x16_bf16 v[0:15], a[84:87], v[48:51], 0\n" \
  "v_exp_f32 v22, v22\n" \
  "v_exp_f32 v23, v23\n" \
  "v_exp_f32 v24, v24\n" \
  "v_exp_f32 v25, v25\n" \
  "v_mfma_f32_32x32x8_bf16 v[0:15], a[88:89], v[52:53], v[0:15]\n" \
  "v_exp_f32 v26, v26\n" \
  "v_exp_f32 v27, v27\n" \
  "v_exp_f32 v28, v28\n" \
  "v_exp_f32 v29, v29\n" \
  "v_smfmac_f32_32x32x32_bf16 v[32:47], a[76:79], v[16:23], v126\n" \
  "v_exp_f32 v30, v30\n" \
  "v_exp_f32 v31, v31\n" \
  "s_waitcnt vmcnt(0)\n" \
  "s_cmp_lg_u32 s9, 0\n" \
  "s_cbranch_scc1 LNFb_%=\n" \
  "v_mov_b32 v61, s56\n" \
  "s_mov_b64 exec, 1\n" \
  "buffer_store_dword v61, off, s[4:7], s13 sc1\n" \
  "s_mov_b64 exec, -1\n" \
  "LNFb_%=:\n" \
  "v_exp_f32 v0, v0\n" \
  "v_exp_f32 v1, v1\n" \
  "v_smfmac_f32_32x32x32_bf16 v[32:47], a[80:83], v[24:31], v126\n" \
  "v_exp_f32 v2, v2\n" \
  "v_exp_f32 v3, v3\n" \
  "v_exp_f32 v4, v4\n" \
  "v_exp_f32 v5, v5\n" \
  "v_mfma_f32_32x32x16_bf16 v[16:31], a[98:101], v[48:51], 0\n" \
  "v_exp_f32 v6, v6\n" \
  "v_exp_f32 v7, v7\n" \
  "v_exp_f32 v8, v8\n" \
  "v_exp_f32 v9, v9\n" \
  "v_mfma_f32_32x32x8_bf16 v[16:31], a[102:103], v[52:53], v[16:31]\n" \
  "v_exp_f32 v10, v10\n" \
  "v_exp_f32 v11, v11\n" \
  "v_exp_f32 v12, v12\n" \
  "v_exp_f32 v13, v13\n" \
  "v_smfmac_f32_32x32x32_bf16 v[32:47], a[90:93], v[0:7], v126\n" \
  "v_exp_f32 v14, v14\n" \
  "v_exp_f32 v15, v15\n" \
  "v_exp_f32 v16, v16\n" \
  "v_exp_f32 v17, v17\n" \
  "v_smfmac_f32_32x32x32_bf16 v[32:47], a[94:97], v[8:15], v126\n" \
  "v_exp_f32 v18, v18\n" \
  "v_exp_f32 v19, v19\n" \
  "v_exp_f32 v20, v20\n" \
  "v_exp_f32 v21, v21\n" \
  "v_mfma_f32_32x32x16_bf16 v[0:15], a[112:115], v[48:51], 0\n" \
  "v_exp_f32 v22, v22\n" \
  "v_exp_f32 v23, v23\n" \
  "v_exp_f32 v24, v24\n" \
  "v_exp_f32 v25, v25\n" \
  "v_mfma_f32_32x32x8_bf16 v[0:15], a[116:117], v[52:53], v[0:15]\n" \
  "v_exp_f32 v26, v26\n" \
  "v_exp_f32 v27, v27\n" \
  "v_exp_f32 v28, v28\n" \
  "v_exp_f32 v29, v29\n" \
  "v_smfmac_f32_32x32x32_bf16 v[32:47], a[104:107], v[16:23], v126\n" \
  "v_exp_f32 v30, v30\n" \
  "v_exp_f32 v31, v31\n" \
  "v_exp_f32 v0, v0\n" \
  "v_exp_f32 v1, v1\n" \
  "v_smfmac_f32_32x32x32_bf16 v[32:47], a[108:111], v[24:31], v126\n" \
  "v_exp_f32 v2, v2\n" \
  "v_exp_f32 v3, v3\n" \
  "v_exp_f32 v4, v4\n" \
  "v_exp_f32 v5, v5\n" \
  "v_mfma_f32_32x32x16_bf16 v[16:31], a[126:129], v[48:51], 0\n" \
  "v_exp_f32 v6, v6\n" \
  "v_exp_f32 v7, v7\n" \
  "v_exp_f32 v8, v8\n" \
  "v_exp_f32 v9, v9\n" \
  "v_mfma_f32_32x32x8_bf16 v[16:31], a[130:131], v[52:53], v[16:31]\n" \
  "v_exp_f32 v10, v10\n" \
  "v_exp_f32 v11, v11\n" \
  "v_exp_f32 v12, v12\n" \
  "v_exp_f32 v13, v13\n" \
  "v_smfmac_f32_32x32x32_bf16 v[32:47], a[118:121], v[0:7], v126\n" \
  "v_exp_f32 v14, v14\n" \
  "v_exp_f32 v15, v15\n" \
  "v_exp_f32 v16, v16\n" \
  "v_exp_f32 v17, v17\n" \
  "v_smfmac_f32_32x32x32_bf16 v[32:47], a[122:125], v[8:15], v126\n" \
  "v_exp_f32 v18, v18\n" \
  "v_exp_f32 v19, v19\n" \
  "v_exp_f32 v20, v20\n" \
  "v_exp_f32 v21, v21\n" \
  "v_mfma_f32_32x32x16_bf16 v[0:15], a[140:143], v[48:51], 0\n" \
  "v_exp_f32 v22, v22\n" \
  "v_exp_f32 v23, v23\n" \
  "v_exp_f32 v24, v24\n" \
  "v_exp_f32 v25, v25\n" \
  "v_mfma_f32_32x32x8_bf16 v[0:15], a[144:145], v[52:53], v[0:15]\n" \
  "v_exp_f32 v26, v26\n" \
  "v_exp_f32 v27, v27\n" \
  "v_exp_f32 v28, v28\n" \
  "v_exp_f32 v29, v29\n" \
  "v_smfmac_f32_32x32x32_bf16 v[32:47], a[132:135], v[16:23], v126\n" \
  "v_exp_f32 v30, v30\n" \
  "v_exp_f32 v31, v31\n" \
  "v_exp_f32 v0, v0\n" \
  "v_exp_f32 v1, v1\n" \
  "v_smfmac_f32_32x32x32_bf16 v[32:47], a[136:139], v[24:31], v126\n" \
  "v_exp_f32 v2, v2\n" \
  "v_exp_f32 v3, v3\n" \
  "v_exp_f32 v4, v4\n" \
  "v_exp_f32 v5, v5\n" \
  "v_mfma_f32_32x32x16_bf16 v[16:31], a[154:157], v[48:51], 0\n" \
  "v_exp_f32 v6, v6\n" \
  "v_exp_f32 v7, v7\n" \
  "v_exp_f32 v8, v8\n" \
  "v_exp_f32 v9, v9\n" \
  "v_mfma_f32_32x32x8_bf16 v[16:31], a[158:159], v[52:53], v[16:31]\n" \
  "v_exp_f32 v10, v10\n" \
  "v_exp_f32 v11, v11\n" \
  "v_exp_f32 v12, v12\n" \
  "v_exp_f32 v13, v13\n" \
  "v_smfmac_f32_32x32x32_bf16 v[32:47], a[146:149], v[0:7], v126\n" \
  "v_exp_f32 v14, v14\n" \
  "v_exp_f32 v15, v15\n" \
  "v_exp_f32 v16, v16\n" \
  "v_exp_f32 v17, v17\n" \
  "v_smfmac_f32_32x32x32_bf16 v[32:47], a[150:153], v[8:15], v126\n" \
  "v_exp_f32 v18, v18\n" \
  "v_exp_f32 v19, v19\n" \
  "v_exp_f32 v20, v20\n" \
  "v_exp_f32 v21, v21\n" \
  "v_mfma_f32_32x32x16_bf16 v[0:15], a[168:171], v[48:51], 0\n" \
  "v_exp_f32 v22, v22\n" \
  "v_exp_f32 v23, v23\n" \
  "v_exp_f32 v24, v24\n" \
  "v_exp_f32 v25, v25\n" \
  "v_mfma_f32_32x32x8_bf16 v[0:15], a[172:173], v[52:53], v[0:15]\n" \
  "v_exp_f32 v26, v26\n" \
  "v_exp_f32 v27, v27\n" \
  "v_exp_f32 v28, v28\n" \
  "v_exp_f32 v29, v29\n" \
  "v_smfmac_f32_32x32x32_bf16 v[32:47], a[160:163], v[16:23], v126\n" \
  "v_exp_f32 v30, v30\n" \
  "v_exp_f32 v31, v31\n" \
  "v_exp_f32 v0, v0\n" \
  "v_exp_f32 v1, v1\n" \
  "v_smfmac_f32_32x32x32_bf16 v[32:47], a[164:167], v[24:31], v126\n" \
  "v_exp_f32 v2, v2\n" \
  "v_exp_f32 v3, v3\n" \
  "v_exp_f32 v4, v4\n" \
  "v_exp_f32 v5, v5\n" \
  "v_mfma_f32_32x32x16_bf16 v[16:31], a[182:185], v[48:51], 0\n" \
  "v_exp_f32 v6, v6\n" \
  "v_exp_f32 v7, v7\n" \
  "v_exp_f32 v8, v8\n" \
  "v_exp_f32 v9, v9\n" \
  "v_mfma_f32_32x32x8_bf16 v[16:31], a[186:187], v[52:53], v[16:31]\n" \
  "v_exp_f32 v10, v10\n" \
  "v_exp_f32 v11, v11\n" \
  "v_exp_f32 v12, v12\n" \
  "v_exp_f32 v13, v13\n" \
  "v_smfmac_f32_32x32x32_bf16 v[32:47], a[174:177], v[0:7], v126\n" \
  "v_exp_f32 v14, v14\n" \
  "v_exp_f32 v15, v15\n" \
  "v_exp_f32 v16, v16\n" \
  "v_exp_f32 v17, v17\n" \
  "v_smfmac_f32_32x32x32_bf16 v[32:47], a[178:181], v[8:15], v126\n" \
  "v_exp_f32 v18, v18\n" \
  "v_exp_f32 v19, v19\n" \
  "v_exp_f32 v20, v20\n" \
  "v_exp_f32 v21, v21\n" \
  "v_mfma_f32_32x32x16_bf16 v[0:15], a[196:199], v[48:51], 0\n" \
  "v_exp_f32 v22, v22\n" \
  "v_exp_f32 v23, v23\n" \
  "v_exp_f32 v24, v24\n" \
  "v_exp_f32 v25, v25\n" \
  "v_mfma_f32_32x32x8_bf16 v[0:15], a[200:201], v[52:53], v[0:15]\n" \
  "v_exp_f32 v26, v26\n" \
  "v_exp_f32 v27, v27\n" \
  "v_exp_f32 v28, v28\n" \
  "v_exp_f32 v29, v29\n" \
  "v_smfmac_f32_32x32x32_bf16 v[32:47], a[188:191], v[16:23], v126\n" \
  "v_exp_f32 v30, v30\n" \
  "v_exp_f32 v31, v31\n" \
  "v_exp_f32 v0, v0\n" \
  "v_exp_f32 v1, v1\n" \
  "v_smfmac_f32_32x32x32_bf16 v[32:47], a[192:195], v[24:31], v126\n" \
  "v_exp_f32 v2, v2\n" \
  "v_exp_f32 v3, v3\n" \
  "v_exp_f32 v4, v4\n" \
  "v_exp_f32 v5, v5\n" \
  "v_mfma_f32_32x32x16_bf16 v[16:31], a[210:213], v[48:51], 0\n" \
  "v_exp_f32 v6, v6\n" \
  "v_exp_f32 v7, v7\n" \
  "v_exp_f32 v8, v8\n" \
  "v_exp_f32 v9, v9\n" \
  "v_mfma_f32_32x32x8_bf16 v[16:31], a[214:215], v[52:53], v[16:31]\n" \
  "v_exp_f32 v10, v10\n" \
  "v_exp_f32 v11, v11\n" \
  "v_exp_f32 v12, v12\n" \
  "v_exp_f32 v13, v13\n" \
  "v_smfmac_f32_32x32x32_bf16 v[32:47], a[202:205], v[0:7], v126\n" \
  "v_exp_f32 v14, v14\n" \
  "v_exp_f32 v15, v15\n" \
  "v_exp_f32 v16, v16\n" \
  "v_exp_f32 v17, v17\n" \
  "v_smfmac_f32_32x32x32_bf16 v[32:47], a[206:209], v[8:15], v126\n" \
  "v_exp_f32 v18, v18\n" \
  "v_exp_f32 v19, v19\n" \
  "v_exp_f32 v20, v20\n" \
  "v_exp_f32 v21, v21\n" \
  "v_exp_f32 v22, v22\n" \
  "v_exp_f32 v23, v23\n" \
  "v_exp_f32 v24, v24\n" \
  "v_exp_f32 v25, v25\n" \
  "v_exp_f32 v26, v26\n" \
  "v_exp_f32 v27, v27\n" \
  "v_exp_f32 v28, v28\n" \
  "v_exp_f32 v29, v29\n" \
  "v_smfmac_f32_32x32x32_bf16 v[32:47], a[216:219], v[16:23], v126\n" \
  "v_exp_f32 v30, v30\n" \
  "v_exp_f32 v31, v31\n" \
  "s_nop 1\n" \
  "v_smfmac_f32_32x32x32_bf16 v[32:47], a[220:223], v[24:31], v126\n" \
  "s_lshl_b32 s58, s8, 4\n" \
  "s_add_i32 s58, s58, s62\n" \
  "v_add_u32 v61, s58, v125\n" \
  "v_add_u32 v62, s62, v125\n" \
  "s_nop 7\n" \
  "ds_write_b128 v61, v[32:35] offset:0\n" \
  "ds_write_b128 v61, v[36:39] offset:1024\n" \
  "ds_write_b128 v61, v[40:43] offset:2048\n" \
  "s_waitcnt lgkmcnt(0)\n" \
  "s_barrier\n" \
  "ds_read_b128 v[72:75], v62 offset:2048\n" \
  "ds_read_b128 v[84:87], v62 offset:6144\n" \
  "ds_read_b128 v[96:99], v62 offset:10240\n" \
  "ds_read_b128 v[108:111], v62 offset:14336\n" \
  "s_xor_b32 s62, s62, 16384\n" \
  "s_cmp_lg_u32 s9, 0\n" \
  "s_cbranch_scc1 LNR_%=\n" \
  "ds_read_b128 v[76:79], v62 offset:4096\n" \
  "ds_read_b128 v[80:83], v62 offset:5120\n" \
  "ds_read_b128 v[88:91], v62 offset:8192\n" \
  "ds_read_b128 v[92:95], v62 offset:9216\n" \
  "ds_read_b128 v[100:103], v62 offset:12288\n" \
  "ds_read_b128 v[104:107], v62 offset:13312\n" \
  "LNR_%=:\n" \
  "s_waitcnt lgkmcnt(0)\n" \
  "v_add_f32 v74, v74, v86\n" \
  "v_add_f32 v98, v98, v110\n" \
  "v_add_f32 v74, v74, v98\n" \
  "v_subrev_u32 v75, 0x21800000, v74\n" \
  "v_cmp_le_u32 vcc, s64, v75\n" \
  "s_cmp_lg_u32 s63, 0\n" \
  "s_cbranch_scc1 LGOOD_%=\n" \
  "s_cbranch_vccnz LMAX_%=\n" \
  "LGOOD_%=:\n" \
  "s_cmp_lg_u32 s9, 0\n" \
  "s_cbranch_scc1 LNS_%=\n" \
  "v_rcp_f32 v74, v74\n" \
  "v_pk_add_f32 v[32:33], v[32:33], v[76:77]\n" \
  "v_pk_add_f32 v[88:89], v[88:89], v[100:101]\n" \
  "v_pk_add_f32 v[34:35], v[34:35], v[78:79]\n" \
  "v_pk_add_f32 v[90:91], v[90:91], v[102:103]\n" \
  "v_pk_add_f32 v[36:37], v[36:37], v[80:81]\n" \
  "v_pk_add_f32 v[92:93], v[92:93], v[104:105]\n" \
  "v_pk_add_f32 v[38:39], v[38:39], v[82:83]\n" \
  "v_pk_add_f32 v[94:95], v[94:95], v[106:107]\n" \
  "v_pk_add_f32 v[40:41], v[40:41], v[84:85]\n" \
  "v_pk_add_f32 v[96:97], v[96:97], v[108:109]\n" \
  "v_pk_add_f32 v[32:33], v[32:33], v[88:89]\n" \
  "v_pk_add_f32 v[34:35], v[34:35], v[90:91]\n" \
  "v_pk_add_f32 v[36:37], v[36:37], v[92:93]\n" \
  "v_pk_add_f32 v[38:39], v[38:39], v[94:95]\n" \
  "v_pk_add_f32 v[40:41], v[40:41], v[96:97]\n" \
  "v_pk_mul_f32 v[32:33], v[32:33], v[74:75] op_sel_hi:[1,0]\n" \
  "v_pk_mul_f32 v[34:35], v[34:35], v[74:75] op_sel_hi:[1,0]\n" \
  "v_pk_mul_f32 v[36:37], v[36:37], v[74:75] op_sel_hi:[1,0]\n" \
  "v_pk_mul_f32 v[38:39], v[38:39], v[74:75] op_sel_hi:[1,0]\n" \
  "v_pk_mul_f32 v[40:41], v[40:41], v[74:75] op_sel_hi:[1,0]\n" \
  "v_cvt_pk_bf16_f32 v64, v32, v33\n" \
  "v_cvt_pk_bf16_f32 v65, v34, v35\n" \
  "v_cvt_pk_bf16_f32 v66, v36, v37\n" \
  "v_cvt_pk_bf16_f32 v67, v38, v39\n" \
  "v_cvt_pk_bf16_f32 v68, v40, v41\n" \
  "s_lshr_b32 s59, s67, 6\n" \
  "s_mul_i32 s59, s59, s15\n" \
  "s_mul_i32 s58, s66, 1280\n" \
  "s_add_i32 s59, s59, s58\n" \
  "s_add_i32 s59, s59, s11\n" \
  "buffer_store_dwordx4 v[64:67], v123, s[0:3], s59 offen sc1\n" \
  "buffer_store_dword v68, v124, s[0:3], s59 offen sc1\n" \
  "LNS_%=:\n" \
  "s_add_i32 s56, s56, 1\n" \
  "s_add_i32 s66, s66, 35\n" \
  "s_cmp_lt_u32 s66, 2048\n" \
  "s_cbranch_scc1 LTOK_%=\n" \
  "s_add_i32 s56, s67, 64\n" \
  "s_waitcnt vmcnt(0)\n" \
  "s_cmp_lg_u32 s9, 0\n" \
  "s_cbranch_scc1 LNFc_%=\n" \
  "v_mov_b32 v61, s56\n" \
  "s_mov_b64 exec, 1\n" \
  "buffer_store_dword v61, off, s[4:7], s13 sc1\n" \
  "s_mov_b64 exec, -1\n" \
  "LNFc_%=:\n" \
  "s_cmp_lt_u32 s56, s14\n" \
  "s_cbranch_scc1 LLAYER_%=\n" \
  "s_branch LOUT_%=\n" \
  "LMAX_%=:\n" \
  "v_mov_b32 v64, 0xff800000\n" \
  "v_mfma_f32_32x32x16_bf16 v[0:15], a[0:3], v[48:51], 0\n" \
  "v_mfma_f32_32x32x8_bf16 v[0:15], a[4:5], v[52:53], v[0:15]\n" \
  "s_nop 11\n" \
  "v_max3_f32 v64, v64, v0, v1\n" \
  "v_max3_f32 v64, v64, v2, v3\n" \
  "v_max3_f32 v64, v64, v4, v5\n" \
  "v_max3_f32 v64, v64, v6, v7\n" \
  "v_max3_f32 v64, v64, v8, v9\n" \
  "v_max3_f32 v64, v64, v10, v11\n" \
  "v_max3_f32 v64, v64, v12, v13\n" \
  "v_max3_f32 v64, v64, v14, v15\n" \
  "v_mfma_f32_32x32x16_bf16 v[16:31], a[14:17], v[48:51], 0\n" \
  "v_mfma_f32_32x32x8_bf16 v[16:31], a[18:19], v[52:53], v[16:31]\n" \
  "s_nop 11\n" \
  "v_max3_f32 v64, v64, v16, v17\n" \
  "v_max3_f32 v64, v64, v18, v19\n" \
  "v_max3_f32 v64, v64, v20, v21\n" \
  "v_max3_f32 v64, v64, v22, v23\n" \
  "v_max3_f32 v64, v64, v24, v25\n" \
  "v_max3_f32 v64, v64, v26, v27\n" \
  "v_max3_f32 v64, v64, v28, v29\n" \
  "v_max3_f32 v64, v64, v30, v31\n" \
  "v_mfma_f32_32x32x16_bf16 v[0:15], a[28:31], v[48:51], 0\n" \
  "v_mfma_f32_32x32x8_bf16 v[0:15], a[32:33], v[52:53], v[0:15]\n" \
  "s_nop 11\n" \
  "v_max3_f32 v64, v64, v0, v1\n" \
  "v_max3_f32 v64, v64, v2, v3\n" \
  "v_max3_f32 v64, v64, v4, v5\n" \
  "v_max3_f32 v64, v64, v6, v7\n" \
  "v_max3_f32 v64, v64, v8, v9\n" \
  "v_max3_f32 v64, v64, v10, v11\n" \
  "v_max3_f32 v64, v64, v12, v13\n" \
  "v_max3_f32 v64, v64, v14, v15\n" \
  "v_mfma_f32_32x32x16_bf16 v[16:31], a[42:45], v[48:51], 0\n" \
  "v_mfma_f32_32x32x8_bf16 v[16:31], a[46:47], v[52:53], v[16:31]\n" \
  "s_nop 11\n" \
  "v_max3_f32 v64, v64, v16, v17\n" \
  "v_max3_f32 v64, v64, v18, v19\n" \
  "v_max3_f32 v64, v64, v20, v21\n" \
  "v_max3_f32 v64, v64, v22, v23\n" \
  "v_max3_f32 v64, v64, v24, v25\n" \
  "v_max3_f32 v64, v64, v26, v27\n" \
  "v_max3_f32 v64, v64, v28, v29\n" \
  "v_max3_f32 v64, v64, v30, v31\n" \
  "v_mfma_f32_32x32x16_bf16 v[0:15], a[56:59], v[48:51], 0\n" \
  "v_mfma_f32_32x32x8_bf16 v[0:15], a[60:61], v[52:53], v[0:15]\n" \
  "s_nop 11\n" \
  "v_max3_f32 v64, v64, v0, v1\n" \
  "v_max3_f32 v64, v64, v2, v3\n" \
  "v_max3_f32 v64, v64, v4, v5\n" \
  "v_max3_f32 v64, v64, v6, v7\n" \
  "v_max3_f32 v64, v64, v8, v9\n" \
  "v_max3_f32 v64, v64, v10, v11\n" \
  "v_max3_f32 v64, v64, v12, v13\n" \
  "v_max3_f32 v64, v64, v14, v15\n" \
  "v_mfma_f32_32x32x16_bf16 v[16:31], a[70:73], v[48:51], 0\n" \
  "v_mfma_f32_32x32x8_bf16 v[16:31], a[74:75], v[52:53], v[16:31]\n" \
  "s_nop 11\n" \
  "v_max3_f32 v64, v64, v16, v17\n" \
  "v_max3_f32 v64, v64, v18, v19\n" \
  "v_max3_f32 v64, v64, v20, v21\n" \
  "v_max3_f32 v64, v64, v22, v23\n" \
  "v_max3_f32 v64, v64, v24, v25\n" \
  "v_max3_f32 v64, v64, v26, v27\n" \
  "v_max3_f32 v64, v64, v28, v29\n" \
  "v_max3_f32 v64, v64, v30, v31\n" \
  "v_mfma_f32_32x32x16_bf16 v[0:15], a[84:87], v[48:51], 0\n" \
  "v_mfma_f32_32x32x8_bf16 v[0:15], a[88:89], v[52:53], v[0:15]\n" \
  "s_nop 11\n" \
  "v_max3_f32 v64, v64, v0, v1\n" \
  "v_max3_f32 v64, v64, v2, v3\n" \
  "v_max3_f32 v64, v64, v4, v5\n" \
  "v_max3_f32 v64, v64, v6, v7\n" \
  "v_max3_f32 v64, v64, v8, v9\n" \
  "v_max3_f32 v64, v64, v10, v11\n" \
  "v_max3_f32 v64, v64, v12, v13\n" \
  "v_max3_f32 v64, v64, v14, v15\n" \
  "v_mfma_f32_32x32x16_bf16 v[16:31], a[98:101], v[48:51], 0\n" \
  "v_mfma_f32_32x32x8_bf16 v[16:31], a[102:103], v[52:53], v[16:31]\n" \
  "s_nop 11\n" \
  "v_max3_f32 v64, v64, v16, v17\n" \
  "v_max3_f32 v64, v64, v18, v19\n" \
  "v_max3_f32 v64, v64, v20, v21\n" \
  "v_max3_f32 v64, v64, v22, v23\n" \
  "v_max3_f32 v64, v64, v24, v25\n" \
  "v_max3_f32 v64, v64, v26, v27\n" \
  "v_max3_f32 v64, v64, v28, v29\n" \
  "v_max3_f32 v64, v64, v30, v31\n" \
  "v_mfma_f32_32x32x16_bf16 v[0:15], a[112:115], v[48:51], 0\n" \
  "v_mfma_f32_32x32x8_bf16 v[0:15], a[116:117], v[52:53], v[0:15]\n" \
  "s_nop 11\n" \
  "v_max3_f32 v64, v64, v0, v1\n" \
  "v_max3_f32 v64, v64, v2, v3\n" \
  "v_max3_f32 v64, v64, v4, v5\n" \
  "v_max3_f32 v64, v64, v6, v7\n" \
  "v_max3_f32 v64, v64, v8, v9\n" \
  "v_max3_f32 v64, v64, v10, v11\n" \
  "v_max3_f32 v64, v64, v12, v13\n" \
  "v_max3_f32 v64, v64, v14, v15\n" \
  "v_mfma_f32_32x32x16_bf16 v[16:31], a[126:129], v[48:51], 0\n" \
  "v_mfma_f32_32x32x8_bf16 v[16:31], a[130:131], v[52:53], v[16:31]\n" \
  "s_nop 11\n" \
  "v_max3_f32 v64, v64, v16, v17\n" \
  "v_max3_f32 v64, v64, v18, v19\n" \
  "v_max3_f32 v64, v64, v20, v21\n" \
  "v_max3_f32 v64, v64, v22, v23\n" \
  "v_max3_f32 v64, v64, v24, v25\n" \
  "v_max3_f32 v64, v64, v26, v27\n" \
  "v_max3_f32 v64, v64, v28, v29\n" \
  "v_max3_f32 v64, v64, v30, v31\n" \
  "v_mfma_f32_32x32x16_bf16 v[0:15], a[140:143], v[48:51], 0\n" \
  "v_mfma_f32_32x32x8_bf16 v[0:15], a[144:145], v[52:53], v[0:15]\n" \
  "s_nop 11\n" \
  "v_max3_f32 v64, v64, v0, v1\n" \
  "v_max3_f32 v64, v64, v2, v3\n" \
  "v_max3_f32 v64, v64, v4, v5\n" \
  "v_max3_f32 v64, v64, v6, v7\n" \
  "v_max3_f32 v64, v64, v8, v9\n" \
  "v_max3_f32 v64, v64, v10, v11\n" \
  "v_max3_f32 v64, v64, v12, v13\n" \
  "v_max3_f32 v64, v64, v14, v15\n" \
  "v_mfma_f32_32x32x16_bf16 v[16:31], a[154:157], v[48:51], 0\n" \
  "v_mfma_f32_32x32x8_bf16 v[16:31], a[158:159], v[52:53], v[16:31]\n" \
  "s_nop 11\n" \
  "v_max3_f32 v64, v64, v16, v17\n" \
  "v_max3_f32 v64, v64, v18, v19\n" \
  "v_max3_f32 v64, v64, v20, v21\n" \
  "v_max3_f32 v64, v64, v22, v23\n" \
  "v_max3_f32 v64, v64, v24, v25\n" \
  "v_max3_f32 v64, v64, v26, v27\n" \
  "v_max3_f32 v64, v64, v28, v29\n" \
  "v_max3_f32 v64, v64, v30, v31\n" \
  "v_mfma_f32_32x32x16_bf16 v[0:15], a[168:171], v[48:51], 0\n" \
  "v_mfma_f32_32x32x8_bf16 v[0:15], a[172:173], v[52:53], v[0:15]\n" \
  "s_nop 11\n" \
  "v_max3_f32 v64, v64, v0, v1\n" \
  "v_max3_f32 v64, v64, v2, v3\n" \
  "v_max3_f32 v64, v64, v4, v5\n" \
  "v_max3_f32 v64, v64, v6, v7\n" \
  "v_max3_f32 v64, v64, v8, v9\n" \
  "v_max3_f32 v64, v64, v10, v11\n" \
  "v_max3_f32 v64, v64, v12, v13\n" \
  "v_max3_f32 v64, v64, v14, v15\n" \
  "v_mfma_f32_32x32x16_bf16 v[16:31], a[182:185], v[48:51], 0\n" \
  "v_mfma_f32_32x32x8_bf16 v[16:31], a[186:187], v[52:53], v[16:31]\n" \
  "s_nop 11\n" \
  "v_max3_f32 v64, v64, v16, v17\n" \
  "v_max3_f32 v64, v64, v18, v19\n" \
  "v_max3_f32 v64, v64, v20, v21\n" \
  "v_max3_f32 v64, v64, v22, v23\n" \
  "v_max3_f32 v64, v64, v24, v25\n" \
  "v_max3_f32 v64, v64, v26, v27\n" \
  "v_max3_f32 v64, v64, v28, v29\n" \
  "v_max3_f32 v64, v64, v30, v31\n" \
  "v_mfma_f32_32x32x16_bf16 v[0:15], a[196:199], v[48:51], 0\n" \
  "v_mfma_f32_32x32x8_bf16 v[0:15], a[200:201], v[52:53], v[0:15]\n" \
  "s_nop 11\n" \
  "v_max3_f32 v64, v64, v0, v1\n" \
  "v_max3_f32 v64, v64, v2, v3\n" \
  "v_max3_f32 v64, v64, v4, v5\n" \
  "v_max3_f32 v64, v64, v6, v7\n" \
  "v_max3_f32 v64, v64, v8, v9\n" \
  "v_max3_f32 v64, v64, v10, v11\n" \
  "v_max3_f32 v64, v64, v12, v13\n" \
  "v_max3_f32 v64, v64, v14, v15\n" \
  "v_mfma_f32_32x32x16_bf16 v[16:31], a[210:213], v[48:51], 0\n" \
  "v_mfma_f32_32x32x8_bf16 v[16:31], a[214:215], v[52:53], v[16:31]\n" \
  "s_nop 11\n" \
  "v_max3_f32 v64, v64, v16, v17\n" \
  "v_max3_f32 v64, v64, v18, v19\n" \
  "v_max3_f32 v64, v64, v20, v21\n" \
  "v_max3_f32 v64, v64, v22, v23\n" \
  "v_max3_f32 v64, v64, v24, v25\n" \
  "v_max3_f32 v64, v64, v26, v27\n" \
  "v_max3_f32 v64, v64, v28, v29\n" \
  "v_max3_f32 v64, v64, v30, v31\n" \
  "v_mov_b32 v65, v64\n" \
  "s_nop 1\n" \
  "v_permlane32_swap_b32 v64, v65\n" \
  "v_max_f32 v64, v64, v65\n" \
  "ds_write_b32 v61, v64 offset:3072\n" \
  "s_waitcnt lgkmcnt(0)\n" \
  "s_barrier\n" \
  "ds_read_b32 v64, v62 offset:3072\n" \
  "ds_read_b32 v65, v62 offset:7168\n" \
  "ds_read_b32 v66, v62 offset:11264\n" \
  "ds_read_b32 v67, v62 offset:15360\n" \
  "s_waitcnt lgkmcnt(0)\n" \
  "v_max3_f32 v64, v64, v65, v66\n" \
  "v_max_f32 v64, v64, v67\n" \
  "v_sub_f32 v64, 0, v64\n" \
  "v_and_b32 v65, 0xffff0000, v64\n" \
  "v_sub_f32 v64, v64, v65\n" \
  "v_and_b32 v66, 0xffff0000, v64\n" \
  "v_sub_f32 v64, v64, v66\n" \
  "v_lshrrev_b32 v65, 16, v65\n" \
  "s_mov_b32 exec_lo, 0\n" \
  "v_or_b32 v52, v65, v66\n" \
  "v_lshrrev_b32 v53, 16, v64\n" \
  "s_mov_b64 exec, -1\n" \
  "s_mov_b32 s63, 1\n" \
  "s_nop 4\n" \
  "s_branch LRUN_%=\n" \
  "LOUT_%=:\n" \
  "s_cmp_lg_u32 s9, 0\n" \
  "s_cbranch_scc1 LO2_%=\n" \
  "LO2_%=:\n" \
  "s_endpgm\n"
#define G4QK_ASM \
  "v_mbcnt_lo_u32_b32 v0, -1, 0\n" \
  "v_mbcnt_hi_u32_b32 v0, -1, v0\n" \
  "v_lshlrev_b32 v0, 2, v0\n" \
  "v_add_u32 v0, %0, v0\n" \
  "v_add_u32 v0, 146432, v0\n" \
  "ds_read_b32 v0, v0\n" \
  "s_waitcnt lgkmcnt(0)\n" \
  "v_readlane_b32 s0, v0, 0\n" \
  "v_readlane_b32 s1, v0, 1\n" \
  "v_readlane_b32 s2, v0, 2\n" \
  "v_readlane_b32 s3, v0, 3\n" \
  "v_readlane_b32 s4, v0, 4\n" \
  "v_readlane_b32 s5, v0, 5\n" \
  "v_readlane_b32 s6, v0, 6\n" \
  "v_readlane_b32 s7, v0, 7\n" \
  "v_readlane_b32 s8, v0, 8\n" \
  "v_readlane_b32 s9, v0, 9\n" \
  "v_readlane_b32 s10, v0, 10\n" \
  "v_readlane_b32 s11, v0, 11\n" \
  "v_readlane_b32 s12, v0, 12\n" \
  "v_readlane_b32 s13, v0, 13\n" \
  "v_readlane_b32 s14, v0, 14\n" \
  "v_readlane_b32 s15, v0, 15\n" \
  "v_readlane_b32 s16, v0, 16\n" \
  "v_readlane_b32 s17, v0, 17\n" \
  "v_readlane_b32 s18, v0, 18\n" \
  "v_readlane_b32 s19, v0, 19\n" \
  "v_readlane_b32 s20, v0, 20\n" \
  "v_readlane_b32 s21, v0, 21\n" \
  "v_readlane_b32 s22, v0, 22\n" \
  "v_readlane_b32 s23, v0, 23\n" \
  "s_nop 4\n" \
  "v_mbcnt_lo_u32_b32 v0, -1, 0\n" \
  "v_mbcnt_hi_u32_b32 v0, -1, v0\n" \
  "v_lshlrev_b32 v0, 2, v0\n" \
  "v_add_u32 v0, s16, v0\n" \
  "v_add_u32 v0, 131072, v0\n" \
  "ds_read_b32 v110, v0 offset:0\n" \
  "ds_read_b32 v111, v0 offset:1024\n" \
  "ds_read_b32 v112, v0 offset:2048\n" \
  "ds_read_b32 v113, v0 offset:3072\n" \
  "ds_read_b32 v114, v0 offset:4096\n" \
  "ds_read_b32 v115, v0 offset:5120\n" \
  "ds_read_b32 v116, v0 offset:6144\n" \
  "ds_read_b32 v117, v0 offset:7168\n" \
  "s_waitcnt lgkmcnt(0)\n" \
  "s_barrier\n" \
  "s_lshl_b32 s59, s16, 7\n" \
  "s_add_i32 m0, s59, 0\n" \
  "s_mov_b32 s58, 0\n" \
  "buffer_load_dwordx4 v115, s[12:15], s58 offen lds\n" \
  "s_add_i32 m0, s59, 1024\n" \
  "s_mov_b32 s58, 65536\n" \
  "buffer_load_dwordx4 v115, s[12:15], s58 offen lds\n" \
  "s_add_i32 m0, s59, 2048\n" \
  "s_mov_b32 s58, 131072\n" \
  "buffer_load_dwordx4 v115, s[12:15], s58 offen lds\n" \
  "s_add_i32 m0, s59, 3072\n" \
  "s_mov_b32 s58, 196608\n" \
  "buffer_load_dwordx4 v115, s[12:15], s58 offen lds\n" \
  "s_add_i32 m0, s59, 4096\n" \
  "s_mov_b32 s58, 262144\n" \
  "buffer_load_dwordx4 v115, s[12:15], s58 offen lds\n" \
  "s_add_i32 m0, s59, 5120\n" \
  "s_mov_b32 s58, 327680\n" \
  "buffer_load_dwordx4 v115, s[12:15], s58 offen lds\n" \
  "s_add_i32 m0, s59, 6144\n" \
  "s_mov_b32 s58, 393216\n" \
  "buffer_load_dwordx4 v115, s[12:15], s58 offen lds\n" \
  "s_add_i32 m0, s59, 7168\n" \
  "s_mov_b32 s58, 458752\n" \
  "buffer_load_dwordx4 v115, s[12:15], s58 offen lds\n" \
  "s_add_i32 m0, s59, 8192\n" \
  "s_mov_b32 s58, 524288\n" \
  "buffer_load_dwordx4 v115, s[12:15], s58 offen lds\n" \
  "s_add_i32 m0, s59, 9216\n" \
  "s_mov_b32 s58, 589824\n" \
  "buffer_load_dwordx4 v115, s[12:15], s58 offen lds\n" \
  "s_add_i32 m0, s59, 10240\n" \
  "s_mov_b32 s58, 655360\n" \
  "buffer_load_dwordx4 v115, s[12:15], s58 offen lds\n" \
  "s_add_i32 m0, s59, 11264\n" \
  "s_mov_b32 s58, 720896\n" \
  "buffer_load_dwordx4 v115, s[12:15], s58 offen lds\n" \
  "s_add_i32 m0, s59, 12288\n" \
  "s_mov_b32 s58, 786432\n" \
  "buffer_load_dwordx4 v115, s[12:15], s58 offen lds\n" \
  "s_add_i32 m0, s59, 13312\n" \
  "s_mov_b32 s58, 851968\n" \
  "buffer_load_dwordx4 v115, s[12:15], s58 offen lds\n" \
  "s_add_i32 m0, s59, 14336\n" \
  "s_mov_b32 s58, 917504\n" \
  "buffer_load_dwordx4 v115, s[12:15], s58 offen lds\n" \
  "s_add_i32 m0, s59, 15360\n" \
  "s_mov_b32 s58, 983040\n" \
  "buffer_load_dwordx4 v115, s[12:15], s58 offen lds\n" \
  "s_add_i32 m0, s59, 16384\n" \
  "s_mov_b32 s58, 1048576\n" \
  "buffer_load_dwordx4 v115, s[12:15], s58 offen lds\n" \
  "s_add_i32 m0, s59, 17408\n" \
  "s_mov_b32 s58, 1114112\n" \
  "buffer_load_dwordx4 v115, s[12:15], s58 offen lds\n" \
  "s_add_i32 m0, s59, 18432\n" \
  "s_mov_b32 s58, 1179648\n" \
  "buffer_load_dwordx4 v115, s[12:15], s58 offen lds\n" \
  "s_add_i32 m0, s59, 19456\n" \
  "s_mov_b32 s58, 1245184\n" \
  "buffer_load_dwordx4 v115, s[12:15], s58 offen lds\n" \
  "s_add_i32 m0, s59, 20480\n" \
  "s_mov_b32 s58, 1310720\n" \
  "buffer_load_dwordx4 v115, s[12:15], s58 offen lds\n" \
  "s_add_i32 m0, s59, 21504\n" \
  "s_mov_b32 s58, 1376256\n" \
  "buffer_load_dwordx4 v115, s[12:15], s58 offen lds\n" \
  "s_add_i32 m0, s59, 22528\n" \
  "s_mov_b32 s58, 1441792\n" \
  "buffer_load_dwordx4 v115, s[12:15], s58 offen lds\n" \
  "s_add_i32 m0, s59, 23552\n" \
  "s_mov_b32 s58, 1507328\n" \
  "buffer_load_dwordx4 v115, s[12:15], s58 offen lds\n" \
  "s_add_i32 m0, s59, 24576\n" \
  "s_mov_b32 s58, 1572864\n" \
  "buffer_load_dwordx4 v115, s[12:15], s58 offen lds\n" \
  "s_add_i32 m0, s59, 25600\n" \
  "s_mov_b32 s58, 1638400\n" \
  "buffer_load_dwordx4 v115, s[12:15], s58 offen lds\n" \
  "s_add_i32 m0, s59, 26624\n" \
  "s_mov_b32 s58, 1703936\n" \
  "buffer_load_dwordx4 v115, s[12:15], s58 offen lds\n" \
  "s_add_i32 m0, s59, 27648\n" \
  "s_mov_b32 s58, 1769472\n" \
  "buffer_load_dwordx4 v115, s[12:15], s58 offen lds\n" \
  "s_add_i32 m0, s59, 28672\n" \
  "s_mov_b32 s58, 1835008\n" \
  "buffer_load_dwordx4 v115, s[12:15], s58 offen lds\n" \
  "s_add_i32 m0, s59, 29696\n" \
  "s_mov_b32 s58, 1900544\n" \
  "buffer_load_dwordx4 v115, s[12:15], s58 offen lds\n" \
  "s_add_i32 m0, s59, 30720\n" \
  "s_mov_b32 s58, 1966080\n" \
  "buffer_load_dwordx4 v115, s[12:15], s58 offen lds\n" \
  "s_add_i32 m0, s59, 31744\n" \
  "s_mov_b32 s58, 2031616\n" \
  "buffer_load_dwordx4 v115, s[12:15], s58 offen lds\n" \
  "s_waitcnt vmcnt(16)\n" \
  "ds_read_b64_tr_b16 a[0:1], v116 offset:0\n" \
  "ds_read_b64_tr_b16 a[2:3], v116 offset:512\n" \
  "ds_read_b64_tr_b16 a[4:5], v116 offset:64\n" \
  "ds_read_b64_tr_b16 a[6:7], v116 offset:576\n" \
  "ds_read_b64_tr_b16 a[8:9], v116 offset:2048\n" \
  "ds_read_b64_tr_b16 a[10:11], v116 offset:2560\n" \
  "ds_read_b64_tr_b16 a[12:13], v116 offset:2112\n" \
  "ds_read_b64_tr_b16 a[14:15], v116 offset:2624\n" \
  "ds_read_b64_tr_b16 a[16:17], v116 offset:4096\n" \
  "ds_read_b64_tr_b16 a[18:19], v116 offset:4608\n" \
  "ds_read_b64_tr_b16 a[20:21], v116 offset:4160\n" \
  "ds_read_b64_tr_b16 a[22:23], v116 offset:4672\n" \
  "ds_read_b64_tr_b16 a[24:25], v116 offset:6144\n" \
  "ds_read_b64_tr_b16 a[26:27], v116 offset:6656\n" \
  "ds_read_b64_tr_b16 a[28:29], v116 offset:6208\n" \
  "ds_read_b64_tr_b16 a[30:31], v116 offset:6720\n" \
  "ds_read_b64_tr_b16 a[32:33], v116 offset:8192\n" \
  "ds_read_b64_tr_b16 a[34:35], v116 offset:8704\n" \
  "ds_read_b64_tr_b16 a[36:37], v116 offset:8256\n" \
  "ds_read_b64_tr_b16 a[38:39], v116 offset:8768\n" \
  "ds_read_b64_tr_b16 a[40:41], v116 offset:10240\n" \
  "ds_read_b64_tr_b16 a[42:43], v116 offset:10752\n" \
  "ds_read_b64_tr_b16 a[44:45], v116 offset:10304\n" \
  "ds_read_b64_tr_b16 a[46:47], v116 offset:10816\n" \
  "ds_read_b64_tr_b16 a[48:49], v116 offset:12288\n" \
  "ds_read_b64_tr_b16 a[50:51], v116 offset:12800\n" \
  "ds_read_b64_tr_b16 a[52:53], v116 offset:12352\n" \
  "ds_read_b64_tr_b16 a[54:55], v116 offset:12864\n" \
  "ds_read_b64_tr_b16 a[56:57], v116 offset:14336\n" \
  "ds_read_b64_tr_b16 a[58:59], v116 offset:14848\n" \
  "ds_read_b64_tr_b16 a[60:61], v116 offset:14400\n" \
  "ds_read_b64_tr_b16 a[62:63], v116 offset:14912\n" \
  "s_waitcnt lgkmcnt(0)\n" \
  "s_add_i32 m0, s59, 0\n" \
  "s_mov_b32 s58, 2097152\n" \
  "buffer_load_dwordx4 v115, s[12:15], s58 offen lds\n" \
  "s_add_i32 m0, s59, 1024\n" \
  "s_mov_b32 s58, 2162688\n" \
  "buffer_load_dwordx4 v115, s[12:15], s58 offen lds\n" \
  "s_add_i32 m0, s59, 2048\n" \
  "s_mov_b32 s58, 2228224\n" \
  "buffer_load_dwordx4 v115, s[12:15], s58 offen lds\n" \
  "s_add_i32 m0, s59, 3072\n" \
  "s_mov_b32 s58, 2293760\n" \
  "buffer_load_dwordx4 v115, s[12:15], s58 offen lds\n" \
  "s_add_i32 m0, s59, 4096\n" \
  "s_mov_b32 s58, 2359296\n" \
  "buffer_load_dwordx4 v115, s[12:15], s58 offen lds\n" \
  "s_add_i32 m0, s59, 5120\n" \
  "s_mov_b32 s58, 2424832\n" \
  "buffer_load_dwordx4 v115, s[12:15], s58 offen lds\n" \
  "s_add_i32 m0, s59, 6144\n" \
  "s_mov_b32 s58, 2490368\n" \
  "buffer_load_dwordx4 v115, s[12:15], s58 offen lds\n" \
  "s_add_i32 m0, s59, 7168\n" \
  "s_mov_b32 s58, 2555904\n" \
  "buffer_load_dwordx4 v115, s[12:15], s58 offen lds\n" \
  "s_add_i32 m0, s59, 8192\n" \
  "s_mov_b32 s58, 2621440\n" \
  "buffer_load_dwordx4 v115, s[12:15], s58 offen lds\n" \
  "s_add_i32 m0, s59, 9216\n" \
  "s_mov_b32 s58, 2686976\n" \
  "buffer_load_dwordx4 v115, s[12:15], s58 offen lds\n" \
  "s_add_i32 m0, s59, 10240\n" \
  "s_mov_b32 s58, 2752512\n" \
  "buffer_load_dwordx4 v115, s[12:15], s58 offen lds\n" \
  "s_add_i32 m0, s59, 11264\n" \
  "s_mov_b32 s58, 2818048\n" \
  "buffer_load_dwordx4 v115, s[12:15], s58 offen lds\n" \
  "s_add_i32 m0, s59, 12288\n" \
  "s_mov_b32 s58, 2883584\n" \
  "buffer_load_dwordx4 v115, s[12:15], s58 offen lds\n" \
  "s_add_i32 m0, s59, 13312\n" \
  "s_mov_b32 s58, 2949120\n" \
  "buffer_load_dwordx4 v115, s[12:15], s58 offen lds\n" \
  "s_add_i32 m0, s59, 14336\n" \
  "s_mov_b32 s58, 3014656\n" \
  "buffer_load_dwordx4 v115, s[12:15], s58 offen lds\n" \
  "s_add_i32 m0, s59, 15360\n" \
  "s_mov_b32 s58, 3080192\n" \
  "buffer_load_dwordx4 v115, s[12:15], s58 offen lds\n" \
  "s_waitcnt vmcnt(16)\n" \
  "ds_read_b64_tr_b16 a[64:65], v116 offset:16384\n" \
  "ds_read_b64_tr_b16 a[66:67], v116 offset:16896\n" \
  "ds_read_b64_tr_b16 a[68:69], v116 offset:16448\n" \
  "ds_read_b64_tr_b16 a[70:71], v116 offset:16960\n" \
  "ds_read_b64_tr_b16 a[72:73], v116 offset:18432\n" \
  "ds_read_b64_tr_b16 a[74:75], v116 offset:18944\n" \
  "ds_read_b64_tr_b16 a[76:77], v116 offset:18496\n" \
  "ds_read_b64_tr_b16 a[78:79], v116 offset:19008\n" \
  "ds_read_b64_tr_b16 a[80:81], v116 offset:20480\n" \
  "ds_read_b64_tr_b16 a[82:83], v116 offset:20992\n" \
  "ds_read_b64_tr_b16 a[84:85], v116 offset:20544\n" \
  "ds_read_b64_tr_b16 a[86:87], v116 offset:21056\n" \
  "ds_read_b64_tr_b16 a[88:89], v116 offset:22528\n" \
  "ds_read_b64_tr_b16 a[90:91], v116 offset:23040\n" \
  "ds_read_b64_tr_b16 a[92:93], v116 offset:22592\n" \
  "ds_read_b64_tr_b16 a[94:95], v116 offset:23104\n" \
  "ds_read_b64_tr_b16 a[96:97], v116 offset:24576\n" \
  "ds_read_b64_tr_b16 a[98:99], v116 offset:25088\n" \
  "ds_read_b64_tr_b16 a[100:101], v116 offset:24640\n" \
  "ds_read_b64_tr_b16 a[102:103], v116 offset:25152\n" \
  "ds_read_b64_tr_b16 a[104:105], v116 offset:26624\n" \
  "ds_read_b64_tr_b16 a[106:107], v116 offset:27136\n" \
  "ds_read_b64_tr_b16 a[108:109], v116 offset:26688\n" \
  "ds_read_b64_tr_b16 a[110:111], v116 offset:27200\n" \
  "ds_read_b64_tr_b16 a[112:113], v116 offset:28672\n" \
  "ds_read_b64_tr_b16 a[114:115], v116 offset:29184\n" \
  "ds_read_b64_tr_b16 a[116:117], v116 offset:28736\n" \
  "ds_read_b64_tr_b16 a[118:119], v116 offset:29248\n" \
  "ds_read_b64_tr_b16 a[120:121], v116 offset:30720\n" \
  "ds_read_b64_tr_b16 a[122:123], v116 offset:31232\n" \
  "ds_read_b64_tr_b16 a[124:125], v116 offset:30784\n" \
  "ds_read_b64_tr_b16 a[126:127], v116 offset:31296\n" \
  "s_waitcnt lgkmcnt(0)\n" \
  "s_add_i32 m0, s59, 16384\n" \
  "s_mov_b32 s58, 3145728\n" \
  "buffer_load_dwordx4 v115, s[12:15], s58 offen lds\n" \
  "s_add_i32 m0, s59, 17408\n" \
  "s_mov_b32 s58, 3211264\n" \
  "buffer_load_dwordx4 v115, s[12:15], s58 offen lds\n" \
  "s_add_i32 m0, s59, 18432\n" \
  "s_mov_b32 s58, 3276800\n" \
  "buffer_load_dwordx4 v115, s[12:15], s58 offen lds\n" \
  "s_add_i32 m0, s59, 19456\n" \
  "s_mov_b32 s58, 3342336\n" \
  "buffer_load_dwordx4 v115, s[12:15], s58 offen lds\n" \
  "s_add_i32 m0, s59, 20480\n" \
  "s_mov_b32 s58, 3407872\n" \
  "buffer_load_dwordx4 v115, s[12:15], s58 offen lds\n" \
  "s_add_i32 m0, s59, 21504\n" \
  "s_mov_b32 s58, 3473408\n" \
  "buffer_load_dwordx4 v115, s[12:15], s58 offen lds\n" \
  "s_add_i32 m0, s59, 22528\n" \
  "s_mov_b32 s58, 3538944\n" \
  "buffer_load_dwordx4 v115, s[12:15], s58 offen lds\n" \
  "s_add_i32 m0, s59, 23552\n" \
  "s_mov_b32 s58, 3604480\n" \
  "buffer_load_dwordx4 v115, s[12:15], s58 offen lds\n" \
  "s_add_i32 m0, s59, 24576\n" \
  "s_mov_b32 s58, 3670016\n" \
  "buffer_load_dwordx4 v115, s[12:15], s58 offen lds\n" \
  "s_add_i32 m0, s59, 25600\n" \
  "s_mov_b32 s58, 3735552\n" \
  "buffer_load_dwordx4 v115, s[12:15], s58 offen lds\n" \
  "s_add_i32 m0, s59, 26624\n" \
  "s_mov_b32 s58, 3801088\n" \
  "buffer_load_dwordx4 v115, s[12:15], s58 offen lds\n" \
  "s_add_i32 m0, s59, 27648\n" \
  "s_mov_b32 s58, 3866624\n" \
  "buffer_load_dwordx4 v115, s[12:15], s58 offen lds\n" \
  "s_add_i32 m0, s59, 28672\n" \
  "s_mov_b32 s58, 3932160\n" \
  "buffer_load_dwordx4 v115, s[12:15], s58 offen lds\n" \
  "s_add_i32 m0, s59, 29696\n" \
  "s_mov_b32 s58, 3997696\n" \
  "buffer_load_dwordx4 v115, s[12:15], s58 offen lds\n" \
  "s_add_i32 m0, s59, 30720\n" \
  "s_mov_b32 s58, 4063232\n" \
  "buffer_load_dwordx4 v115, s[12:15], s58 offen lds\n" \
  "s_add_i32 m0, s59, 31744\n" \
  "s_mov_b32 s58, 4128768\n" \
  "buffer_load_dwordx4 v115, s[12:15], s58 offen lds\n" \
  "s_waitcnt vmcnt(16)\n" \
  "ds_read_b64_tr_b16 a[128:129], v116 offset:0\n" \
  "ds_read_b64_tr_b16 a[130:131], v116 offset:512\n" \
  "ds_read_b64_tr_b16 a[132:133], v116 offset:64\n" \
  "ds_read_b64_tr_b16 a[134:135], v116 offset:576\n" \
  "ds_read_b64_tr_b16 a[136:137], v116 offset:2048\n" \
  "ds_read_b64_tr_b16 a[138:139], v116 offset:2560\n" \
  "ds_read_b64_tr_b16 a[140:141], v116 offset:2112\n" \
  "ds_read_b64_tr_b16 a[142:143], v116 offset:2624\n" \
  "ds_read_b64_tr_b16 a[144:145], v116 offset:4096\n" \
  "ds_read_b64_tr_b16 a[146:147], v116 offset:4608\n" \
  "ds_read_b64_tr_b16 a[148:149], v116 offset:4160\n" \
  "ds_read_b64_tr_b16 a[150:151], v116 offset:4672\n" \
  "ds_read_b64_tr_b16 a[152:153], v116 offset:6144\n" \
  "ds_read_b64_tr_b16 a[154:155], v116 offset:6656\n" \
  "ds_read_b64_tr_b16 a[156:157], v116 offset:6208\n" \
  "ds_read_b64_tr_b16 a[158:159], v116 offset:6720\n" \
  "ds_read_b64_tr_b16 a[160:161], v116 offset:8192\n" \
  "ds_read_b64_tr_b16 a[162:163], v116 offset:8704\n" \
  "ds_read_b64_tr_b16 a[164:165], v116 offset:8256\n" \
  "ds_read_b64_tr_b16 a[166:167], v116 offset:8768\n" \
  "ds_read_b64_tr_b16 a[168:169], v116 offset:10240\n" \
  "ds_read_b64_tr_b16 a[170:171], v116 offset:10752\n" \
  "ds_read_b64_tr_b16 a[172:173], v116 offset:10304\n" \
  "ds_read_b64_tr_b16 a[174:175], v116 offset:10816\n" \
  "ds_read_b64_tr_b16 a[176:177], v116 offset:12288\n" \
  "ds_read_b64_tr_b16 a[178:179], v116 offset:12800\n" \
  "ds_read_b64_tr_b16 a[180:181], v116 offset:12352\n" \
  "ds_read_b64_tr_b16 a[182:183], v116 offset:12864\n" \
  "ds_read_b64_tr_b16 a[184:185], v116 offset:14336\n" \
  "ds_read_b64_tr_b16 a[186:187], v116 offset:14848\n" \
  "ds_read_b64_tr_b16 a[188:189], v116 offset:14400\n" \
  "ds_read_b64_tr_b16 a[190:191], v116 offset:14912\n" \
  "s_waitcnt lgkmcnt(0)\n" \
  "s_add_i32 m0, s59, 0\n" \
  "s_mov_b32 s58, 4194304\n" \
  "buffer_load_dwordx4 v115, s[12:15], s58 offen lds\n" \
  "s_add_i32 m0, s59, 1024\n" \
  "s_mov_b32 s58, 4259840\n" \
  "buffer_load_dwordx4 v115, s[12:15], s58 offen lds\n" \
  "s_add_i32 m0, s59, 2048\n" \
  "s_mov_b32 s58, 4325376\n" \
  "buffer_load_dwordx4 v115, s[12:15], s58 offen lds\n" \
  "s_add_i32 m0, s59, 3072\n" \
  "s_mov_b32 s58, 4390912\n" \
  "buffer_load_dwordx4 v115, s[12:15], s58 offen lds\n" \
  "s_add_i32 m0, s59, 4096\n" \
  "s_mov_b32 s58, 4456448\n" \
  "buffer_load_dwordx4 v115, s[12:15], s58 offen lds\n" \
  "s_add_i32 m0, s59, 5120\n" \
  "s_mov_b32 s58, 4521984\n" \
  "buffer_load_dwordx4 v115, s[12:15], s58 offen lds\n" \
  "s_add_i32 m0, s59, 6144\n" \
  "s_mov_b32 s58, 4587520\n" \
  "buffer_load_dwordx4 v115, s[12:15], s58 offen lds\n" \
  "s_add_i32 m0, s59, 7168\n" \
  "s_mov_b32 s58, 4653056\n" \
  "buffer_load_dwordx4 v115, s[12:15], s58 offen lds\n" \
  "s_add_i32 m0, s59, 8192\n" \
  "s_mov_b32 s58, 4718592\n" \
  "buffer_load_dwordx4 v115, s[12:15], s58 offen lds\n" \
  "s_add_i32 m0, s59, 9216\n" \
  "s_mov_b32 s58, 4784128\n" \
  "buffer_load_dwordx4 v115, s[12:15], s58 offen lds\n" \
  "s_add_i32 m0, s59, 10240\n" \
  "s_mov_b32 s58, 4849664\n" \
  "buffer_load_dwordx4 v115, s[12:15], s58 offen lds\n" \
  "s_add_i32 m0, s59, 11264\n" \
  "s_mov_b32 s58, 4915200\n" \
  "buffer_load_dwordx4 v115, s[12:15], s58 offen lds\n" \
  "s_add_i32 m0, s59, 12288\n" \
  "s_mov_b32 s58, 4980736\n" \
  "buffer_load_dwordx4 v115, s[12:15], s58 offen lds\n" \
  "s_add_i32 m0, s59, 13312\n" \
  "s_mov_b32 s58, 5046272\n" \
  "buffer_load_dwordx4 v115, s[12:15], s58 offen lds\n" \
  "s_add_i32 m0, s59, 14336\n" \
  "s_mov_b32 s58, 5111808\n" \
  "buffer_load_dwordx4 v115, s[12:15], s58 offen lds\n" \
  "s_add_i32 m0, s59, 15360\n" \
  "s_mov_b32 s58, 5177344\n" \
  "buffer_load_dwordx4 v115, s[12:15], s58 offen lds\n" \
  "s_waitcnt vmcnt(16)\n" \
  "ds_read_b64_tr_b16 a[192:193], v116 offset:16384\n" \
  "ds_read_b64_tr_b16 a[194:195], v116 offset:16896\n" \
  "ds_read_b64_tr_b16 a[196:197], v116 offset:16448\n" \
  "ds_read_b64_tr_b16 a[198:199], v116 offset:16960\n" \
  "ds_read_b64_tr_b16 a[200:201], v116 offset:18432\n" \
  "ds_read_b64_tr_b16 a[202:203], v116 offset:18944\n" \
  "ds_read_b64_tr_b16 a[204:205], v116 offset:18496\n" \
  "ds_read_b64_tr_b16 a[206:207], v116 offset:19008\n" \
  "ds_read_b64_tr_b16 a[208:209], v116 offset:20480\n" \
  "ds_read_b64_tr_b16 a[210:211], v116 offset:20992\n" \
  "ds_read_b64_tr_b16 a[212:213], v116 offset:20544\n" \
  "ds_read_b64_tr_b16 a[214:215], v116 offset:21056\n" \
  "ds_read_b64_tr_b16 a[216:217], v116 offset:22528\n" \
  "ds_read_b64_tr_b16 a[218:219], v116 offset:23040\n" \
  "ds_read_b64_tr_b16 a[220:221], v116 offset:22592\n" \
  "ds_read_b64_tr_b16 a[222:223], v116 offset:23104\n" \
  "ds_read_b64_tr_b16 a[224:225], v116 offset:24576\n" \
  "ds_read_b64_tr_b16 a[226:227], v116 offset:25088\n" \
  "ds_read_b64_tr_b16 a[228:229], v116 offset:24640\n" \
  "ds_read_b64_tr_b16 a[230:231], v116 offset:25152\n" \
  "ds_read_b64_tr_b16 a[232:233], v116 offset:26624\n" \
  "ds_read_b64_tr_b16 a[234:235], v116 offset:27136\n" \
  "ds_read_b64_tr_b16 a[236:237], v116 offset:26688\n" \
  "ds_read_b64_tr_b16 a[238:239], v116 offset:27200\n" \
  "ds_read_b64_tr_b16 a[240:241], v116 offset:28672\n" \
  "ds_read_b64_tr_b16 a[242:243], v116 offset:29184\n" \
  "ds_read_b64_tr_b16 a[244:245], v116 offset:28736\n" \
  "ds_read_b64_tr_b16 a[246:247], v116 offset:29248\n" \
  "ds_read_b64_tr_b16 a[248:249], v116 offset:30720\n" \
  "ds_read_b64_tr_b16 a[250:251], v116 offset:31232\n" \
  "ds_read_b64_tr_b16 a[252:253], v116 offset:30784\n" \
  "ds_read_b64_tr_b16 a[254:255], v116 offset:31296\n" \
  "s_waitcnt lgkmcnt(0)\n" \
  "s_waitcnt vmcnt(0)\n" \
  "ds_read_b64_tr_b16 v[192:193], v116 offset:0\n" \
  "ds_read_b64_tr_b16 v[194:195], v116 offset:512\n" \
  "ds_read_b64_tr_b16 v[196:197], v116 offset:64\n" \
  "ds_read_b64_tr_b16 v[198:199], v116 offset:576\n" \
  "ds_read_b64_tr_b16 v[200:201], v116 offset:2048\n" \
  "ds_read_b64_tr_b16 v[202:203], v116 offset:2560\n" \
  "ds_read_b64_tr_b16 v[204:205], v116 offset:2112\n" \
  "ds_read_b64_tr_b16 v[206:207], v116 offset:2624\n" \
  "ds_read_b64_tr_b16 v[208:209], v116 offset:4096\n" \
  "ds_read_b64_tr_b16 v[210:211], v116 offset:4608\n" \
  "ds_read_b64_tr_b16 v[212:213], v116 offset:4160\n" \
  "ds_read_b64_tr_b16 v[214:215], v116 offset:4672\n" \
  "ds_read_b64_tr_b16 v[216:217], v116 offset:6144\n" \
  "ds_read_b64_tr_b16 v[218:219], v116 offset:6656\n" \
  "ds_read_b64_tr_b16 v[220:221], v116 offset:6208\n" \
  "ds_read_b64_tr_b16 v[222:223], v116 offset:6720\n" \
  "ds_read_b64_tr_b16 v[224:225], v116 offset:8192\n" \
  "ds_read_b64_tr_b16 v[226:227], v116 offset:8704\n" \
  "ds_read_b64_tr_b16 v[228:229], v116 offset:8256\n" \
  "ds_read_b64_tr_b16 v[230:231], v116 offset:8768\n" \
  "ds_read_b64_tr_b16 v[232:233], v116 offset:10240\n" \
  "ds_read_b64_tr_b16 v[234:235], v116 offset:10752\n" \
  "ds_read_b64_tr_b16 v[236:237], v116 offset:10304\n" \
  "ds_read_b64_tr_b16 v[238:239], v116 offset:10816\n" \
  "ds_read_b64_tr_b16 v[240:241], v116 offset:12288\n" \
  "ds_read_b64_tr_b16 v[242:243], v116 offset:12800\n" \
  "ds_read_b64_tr_b16 v[244:245], v116 offset:12352\n" \
  "ds_read_b64_tr_b16 v[246:247], v116 offset:12864\n" \
  "ds_read_b64_tr_b16 v[248:249], v116 offset:14336\n" \
  "ds_read_b64_tr_b16 v[250:251], v116 offset:14848\n" \
  "ds_read_b64_tr_b16 v[252:253], v116 offset:14400\n" \
  "ds_read_b64_tr_b16 v[254:255], v116 offset:14912\n" \
  "s_waitcnt lgkmcnt(0)\n" \
  "v_mov_b32 v32, 0x3f803f80\n" \
  "v_mov_b32 v33, 0x3f803f80\n" \
  "v_mov_b32 v34, 0x3f803f80\n" \
  "v_mov_b32 v35, 0x3f803f80\n" \
  "s_nop 1\n" \
  "v_mfma_f32_32x32x16_bf16 v[0:15], v[32:35], a[0:3], 0\n" \
  "v_mfma_f32_32x32x16_bf16 v[16:31], v[32:35], a[4:7], 0\n" \
  "v_mfma_f32_32x32x16_bf16 v[0:15], v[32:35], a[8:11], v[0:15]\n" \
  "v_mfma_f32_32x32x16_bf16 v[16:31], v[32:35], a[12:15], v[16:31]\n" \
  "v_mfma_f32_32x32x16_bf16 v[0:15], v[32:35], a[16:19], v[0:15]\n" \
  "v_mfma_f32_32x32x16_bf16 v[16:31], v[32:35], a[20:23], v[16:31]\n" \
  "v_mfma_f32_32x32x16_bf16 v[0:15], v[32:35], a[24:27], v[0:15]\n" \
  "v_mfma_f32_32x32x16_bf16 v[16:31], v[32:35], a[28:31], v[16:31]\n" \
  "v_mfma_f32_32x32x16_bf16 v[0:15], v[32:35], a[32:35], v[0:15]\n" \
  "v_mfma_f32_32x32x16_bf16 v[16:31], v[32:35], a[36:39], v[16:31]\n" \
  "v_mfma_f32_32x32x16_bf16 v[0:15], v[32:35], a[40:43], v[0:15]\n" \
  "v_mfma_f32_32x32x16_bf16 v[16:31], v[32:35], a[44:47], v[16:31]\n" \
  "v_mfma_f32_32x32x16_bf16 v[0:15], v[32:35], a[48:51], v[0:15]\n" \
  "v_mfma_f32_32x32x16_bf16 v[16:31], v[32:35], a[52:55], v[16:31]\n" \
  "v_mfma_f32_32x32x16_bf16 v[0:15], v[32:35], a[56:59], v[0:15]\n" \
  "v_mfma_f32_32x32x16_bf16 v[16:31], v[32:35], a[60:63], v[16:31]\n" \
  "v_mfma_f32_32x32x16_bf16 v[0:15], v[32:35], a[64:67], v[0:15]\n" \
  "v_mfma_f32_32x32x16_bf16 v[16:31], v[32:35], a[68:71], v[16:31]\n" \
  "v_mfma_f32_32x32x16_bf16 v[0:15], v[32:35], a[72:75], v[0:15]\n" \
  "v_mfma_f32_32x32x16_bf16 v[16:31], v[32:35], a[76:79], v[16:31]\n" \
  "v_mfma_f32_32x32x16_bf16 v[0:15], v[32:35], a[80:83], v[0:15]\n" \
  "v_mfma_f32_32x32x16_bf16 v[16:31], v[32:35], a[84:87], v[16:31]\n" \
  "v_mfma_f32_32x32x16_bf16 v[0:15], v[32:35], a[88:91], v[0:15]\n" \
  "v_mfma_f32_32x32x16_bf16 v[16:31], v[32:35], a[92:95], v[16:31]\n" \
  "v_mfma_f32_32x32x16_bf16 v[0:15], v[32:35], a[96:99], v[0:15]\n" \
  "v_mfma_f32_32x32x16_bf16 v[16:31], v[32:35], a[100:103], v[16:31]\n" \
  "v_mfma_f32_32x32x16_bf16 v[0:15], v[32:35], a[104:107], v[0:15]\n" \
  "v_mfma_f32_32x32x16_bf16 v[16:31], v[32:35], a[108:111], v[16:31]\n" \
  "v_mfma_f32_32x32x16_bf16 v[0:15], v[32:35], a[112:115], v[0:15]\n" \
  "v_mfma_f32_32x32x16_bf16 v[16:31], v[32:35], a[116:119], v[16:31]\n" \
  "v_mfma_f32_32x32x16_bf16 v[0:15], v[32:35], a[120:123], v[0:15]\n" \
  "v_mfma_f32_32x32x16_bf16 v[16:31], v[32:35], a[124:127], v[16:31]\n" \
  "v_mfma_f32_32x32x16_bf16 v[0:15], v[32:35], a[128:131], v[0:15]\n" \
  "v_mfma_f32_32x32x16_bf16 v[16:31], v[32:35], a[132:135], v[16:31]\n" \
  "v_mfma_f32_32x32x16_bf16 v[0:15], v[32:35], a[136:139], v[0:15]\n" \
  "v_mfma_f32_32x32x16_bf16 v[16:31], v[32:35], a[140:143], v[16:31]\n" \
  "v_mfma_f32_32x32x16_bf16 v[0:15], v[32:35], a[144:147], v[0:15]\n" \
  "v_mfma_f32_32x32x16_bf16 v[16:31], v[32:35], a[148:151], v[16:31]\n" \
  "v_mfma_f32_32x32x16_bf16 v[0:15], v[32:35], a[152:155], v[0:15]\n" \
  "v_mfma_f32_32x32x16_bf16 v[16:31], v[32:35], a[156:159], v[16:31]\n" \
  "v_mfma_f32_32x32x16_bf16 v[0:15], v[32:35], a[160:163], v[0:15]\n" \
  "v_mfma_f32_32x32x16_bf16 v[16:31], v[32:35], a[164:167], v[16:31]\n" \
  "v_mfma_f32_32x32x16_bf16 v[0:15], v[32:35], a[168:171], v[0:15]\n" \
  "v_mfma_f32_32x32x16_bf16 v[16:31], v[32:35], a[172:175], v[16:31]\n" \
  "v_mfma_f32_32x32x16_bf16 v[0:15], v[32:35], a[176:179], v[0:15]\n" \
  "v_mfma_f32_32x32x16_bf16 v[16:31], v[32:35], a[180:183], v[16:31]\n" \
  "v_mfma_f32_32x32x16_bf16 v[0:15], v[32:35], a[184:187], v[0:15]\n" \
  "v_mfma_f32_32x32x16_bf16 v[16:31], v[32:35], a[188:191], v[16:31]\n" \
  "v_mfma_f32_32x32x16_bf16 v[0:15], v[32:35], a[192:195], v[0:15]\n" \
  "v_mfma_f32_32x32x16_bf16 v[16:31], v[32:35], a[196:199], v[16:31]\n" \
  "v_mfma_f32_32x32x16_bf16 v[0:15], v[32:35], a[200:203], v[0:15]\n" \
  "v_mfma_f32_32x32x16_bf16 v[16:31], v[32:35], a[204:207], v[16:31]\n" \
  "v_mfma_f32_32x32x16_bf16 v[0:15], v[32:35], a[208:211], v[0:15]\n" \
  "v_mfma_f32_32x32x16_bf16 v[16:31], v[32:35], a[212:215], v[16:31]\n" \
  "v_mfma_f32_32x32x16_bf16 v[0:15], v[32:35], a[216:219], v[0:15]\n" \
  "v_mfma_f32_32x32x16_bf16 v[16:31], v[32:35], a[220:223], v[16:31]\n" \
  "v_mfma_f32_32x32x16_bf16 v[0:15], v[32:35], a[224:227], v[0:15]\n" \
  "v_mfma_f32_32x32x16_bf16 v[16:31], v[32:35], a[228:231], v[16:31]\n" \
  "v_mfma_f32_32x32x16_bf16 v[0:15], v[32:35], a[232:235], v[0:15]\n" \
  "v_mfma_f32_32x32x16_bf16 v[16:31], v[32:35], a[236:239], v[16:31]\n" \
  "v_mfma_f32_32x32x16_bf16 v[0:15], v[32:35], a[240:243], v[0:15]\n" \
  "v_mfma_f32_32x32x16_bf16 v[16:31], v[32:35], a[244:247], v[16:31]\n" \
  "v_mfma_f32_32x32x16_bf16 v[0:15], v[32:35], a[248:251], v[0:15]\n" \
  "v_mfma_f32_32x32x16_bf16 v[16:31], v[32:35], a[252:255], v[16:31]\n" \
  "v_mfma_f32_32x32x16_bf16 v[0:15], v[32:35], v[192:195], v[0:15]\n" \
  "v_mfma_f32_32x32x16_bf16 v[16:31], v[32:35], v[196:199], v[16:31]\n" \
  "v_mfma_f32_32x32x16_bf16 v[0:15], v[32:35], v[200:203], v[0:15]\n" \
  "v_mfma_f32_32x32x16_bf16 v[16:31], v[32:35], v[204:207], v[16:31]\n" \
  "v_mfma_f32_32x32x16_bf16 v[0:15], v[32:35], v[208:211], v[0:15]\n" \
  "v_mfma_f32_32x32x16_bf16 v[16:31], v[32:35], v[212:215], v[16:31]\n" \
  "v_mfma_f32_32x32x16_bf16 v[0:15], v[32:35], v[216:219], v[0:15]\n" \
  "v_mfma_f32_32x32x16_bf16 v[16:31], v[32:35], v[220:223], v[16:31]\n" \
  "v_mfma_f32_32x32x16_bf16 v[0:15], v[32:35], v[224:227], v[0:15]\n" \
  "v_mfma_f32_32x32x16_bf16 v[16:31], v[32:35], v[228:231], v[16:31]\n" \
  "v_mfma_f32_32x32x16_bf16 v[0:15], v[32:35], v[232:235], v[0:15]\n" \
  "v_mfma_f32_32x32x16_bf16 v[16:31], v[32:35], v[236:239], v[16:31]\n" \
  "v_mfma_f32_32x32x16_bf16 v[0:15], v[32:35], v[240:243], v[0:15]\n" \
  "v_mfma_f32_32x32x16_bf16 v[16:31], v[32:35], v[244:247], v[16:31]\n" \
  "v_mfma_f32_32x32x16_bf16 v[0:15], v[32:35], v[248:251], v[0:15]\n" \
  "v_mfma_f32_32x32x16_bf16 v[16:31], v[32:35], v[252:255], v[16:31]\n" \
  "s_nop 4\n" \
  "v_cvt_pk_bf16_f32 v92, v0, 0\n" \
  "v_lshlrev_b32 v93, 16, v92\n" \
  "v_sub_f32 v94, v0, v93\n" \
  "v_cvt_pk_bf16_f32 v188, v93, v94\n" \
  "v_mov_b32 v189, v92\n" \
  "s_nop 1\n" \
  "v_cvt_pk_bf16_f32 v92, v16, 0\n" \
  "v_lshlrev_b32 v93, 16, v92\n" \
  "v_sub_f32 v94, v16, v93\n" \
  "v_cvt_pk_bf16_f32 v190, v93, v94\n" \
  "v_mov_b32 v191, v92\n" \
  "s_mov_b32 s57, 1048576\n" \
  "s_mov_b32 s56, 0\n" \
  "s_mov_b32 s61, 0\n" \
  "s_mov_b32 s62, 0\n" \
  "s_barrier\n" \
  "s_mov_b32 s66, -1\n" \
  "s_mov_b32 s67, 0\n" \
  "s_mov_b32 s65, 0\n" \
  "v_lshrrev_b32 v97, 2, v110\n" \
  "v_add_u32 v97, 0x14000, v97\n" \
  "LSEG_%=:\n" \
  "v_mov_b32 v101, v114\n" \
  "s_cmp_lg_u32 s61, 0\n" \
  "s_cbranch_scc1 LPRE_%=\n" \
  "v_mov_b32 v101, 0x7fff0000\n" \
  "s_waitcnt vmcnt(0)\n" \
  "v_mov_b32 v92, s56\n" \
  "s_mov_b64 exec, 1\n" \
  "buffer_store_dword v92, off, s[4:7], s20 sc1\n" \
  "s_mov_b64 exec, -1\n" \
  "s_add_i32 s60, s56, 1\n" \
  "LSx_%=:\n" \
  "buffer_load_dword v100, v113, s[4:7], 0 offen sc1\n" \
  "s_waitcnt vmcnt(0)\n" \
  "v_cmp_gt_u32 vcc, s60, v100\n" \
  "s_cbranch_vccz LSxx_%=\n" \
  "s_cmp_eq_u32 s57, 0\n" \
  "s_cbranch_scc1 LSxx_%=\n" \
  "s_sub_u32 s57, s57, 1\n" \
  "s_sleep 2\n" \
  "s_branch LSx_%=\n" \
  "LSxx_%=:\n" \
  "s_lshr_b32 s59, s56, 6\n" \
  "s_mul_i32 s59, s59, s22\n" \
  "s_and_b32 s58, s56, 63\n" \
  "s_mul_i32 s63, s58, 1280\n" \
  "s_add_i32 s63, s63, s59\n" \
  "s_add_i32 s63, s63, s18\n" \
  "s_mul_i32 s58, s58, 40960\n" \
  "s_add_i32 s59, s59, s58\n" \
  "s_add_i32 s59, s59, s17\n" \
  "s_lshl_b32 s58, s16, 2\n" \
  "s_add_i32 m0, s58, s62\n" \
  "buffer_load_dwordx2 v[64:65], v112, s[0:3], s63 offen offset:0 sc1\n" \
  "buffer_load_dwordx2 v[66:67], v112, s[0:3], s63 offen offset:256 sc1\n" \
  "buffer_load_dwordx2 v[68:69], v112, s[0:3], s63 offen offset:512 sc1\n" \
  "buffer_load_dwordx4 v110, s[0:3], s59 offen sc1 lds\n" \
  "s_add_i32 m0, m0, 4096\n" \
  "s_add_i32 s59, s59, 4096\n" \
  "buffer_load_dwordx4 v110, s[0:3], s59 offen sc1 lds\n" \
  "s_add_i32 m0, m0, 4096\n" \
  "s_add_i32 s59, s59, 4096\n" \
  "buffer_load_dwordx4 v110, s[0:3], s59 offen sc1 lds\n" \
  "s_add_i32 m0, m0, 4096\n" \
  "s_add_i32 s59, s59, 4096\n" \
  "buffer_load_dwordx4 v110, s[0:3], s59 offen sc1 lds\n" \
  "s_add_i32 m0, m0, 4096\n" \
  "s_add_i32 s59, s59, 4096\n" \
  "buffer_load_dwordx4 v110, s[0:3], s59 offen sc1 lds\n" \
  "s_add_i32 m0, m0, 4096\n" \
  "s_add_i32 s59, s59, 4096\n" \
  "buffer_load_dwordx4 v110, s[0:3], s59 offen sc1 lds\n" \
  "s_add_i32 m0, m0, 4096\n" \
  "s_add_i32 s59, s59, 4096\n" \
  "buffer_load_dwordx4 v110, s[0:3], s59 offen sc1 lds\n" \
  "s_add_i32 m0, m0, 4096\n" \
  "s_add_i32 s59, s59, 4096\n" \
  "buffer_load_dwordx4 v110, s[0:3], s59 offen sc1 lds\n" \
  "s_add_i32 m0, m0, 4096\n" \
  "s_add_i32 s59, s59, 4096\n" \
  "buffer_load_dwordx4 v110, s[0:3], s59 offen sc1 lds\n" \
  "s_add_i32 m0, m0, 4096\n" \
  "s_add_i32 s59, s59, 4096\n" \
  "buffer_load_dwordx4 v110, s[0:3], s59 offen sc1 lds\n" \
  "s_add_i32 m0, m0, 4096\n" \
  "s_add_i32 s59, s59, 4096\n" \
  "s_waitcnt vmcnt(10)\n" \
  "v_cndmask_b32 v68, 0, v68, s[66:67]\n" \
  "v_cndmask_b32 v69, 0, v69, s[66:67]\n" \
  "v_add_f32 v64, v64, v66\n" \
  "v_add_f32 v65, v65, v67\n" \
  "v_add_f32 v64, v64, v68\n" \
  "v_add_f32 v65, v65, v69\n" \
  "v_mov_b32 v66, v64\n" \
  "v_mov_b32 v67, v65\n" \
  "v_mov_b32 v68, 0x3acccccd\n" \
  "v_permlane32_swap_b32 v64, v66\n" \
  "v_permlane32_swap_b32 v65, v67\n" \
  "v_add_f32 v64, v64, v66\n" \
  "v_add_f32 v65, v65, v67\n" \
  "v_mul_f32 v64, 0xbacccccd, v64\n" \
  "v_mul_f32 v66, v64, v64\n" \
  "v_fma_f32 v65, v65, v68, -v66\n" \
  "v_max_f32 v65, 0, v65\n" \
  "v_add_f32 v65, 0x3727c5ac, v65\n" \
  "v_rsq_f32 v65, v65\n" \
  "v_cvt_pk_bf16_f32 v120, v64, v64\n" \
  "v_and_b32 v66, 0xffff0000, v120\n" \
  "v_sub_f32 v66, v64, v66\n" \
  "v_cvt_pk_bf16_f32 v121, v66, 0\n" \
  "v_mul_f32 v122, s23, v65\n" \
  "v_cndmask_b32 v120, 0, v120, s[66:67]\n" \
  "v_cndmask_b32 v121, 0, v121, s[66:67]\n" \
  "s_waitcnt vmcnt(0)\n" \
  "LPRE_%=:\n" \
  "s_waitcnt vmcnt(2)\n" \
  "s_barrier\n" \
  "v_add_u32 v96, s62, v111\n" \
  "ds_read_b128 v[32:35], v96 offset:0\n" \
  "ds_read_b128 v[36:39], v96 offset:1024\n" \
  "ds_read_b128 v[40:43], v96 offset:2048\n" \
  "ds_read_b128 v[44:47], v96 offset:3072\n" \
  "ds_read_b128 v[48:51], v96 offset:4096\n" \
  "ds_read_b128 v[52:55], v96 offset:5120\n" \
  "buffer_load_dword v100, v113, s[4:7], 0 offen sc1\n" \
  "v_mov_b64 v[72:73], v[120:121]\n" \
  "s_and_b32 s58, s56, 63\n" \
  "s_mul_i32 s58, s58, 786432\n" \
  "s_add_i32 s64, s58, s19\n" \
  "s_xor_b32 s62, s62, 40960\n" \
  "v_mfma_f32_32x32x8_bf16 v[0:15], v[188:189], v[72:73], 0\n" \
  "s_waitcnt lgkmcnt(5)\n" \
  "v_mfma_f32_32x32x16_bf16 v[0:15], a[0:3], v[32:35], v[0:15]\n" \
  "ds_read_b128 v[56:59], v96 offset:6144\n" \
  "s_waitcnt lgkmcnt(5)\n" \
  "v_mfma_f32_32x32x16_bf16 v[0:15], a[8:11], v[36:39], v[0:15]\n" \
  "ds_read_b128 v[60:63], v96 offset:7168\n" \
  "v_mul_f32 v16, v16, v74\n" \
  "v_mul_f32 v17, v17, v74\n" \
  "v_mul_f32 v18, v18, v74\n" \
  "v_mul_f32 v19, v19, v74\n" \
  "s_waitcnt lgkmcnt(5)\n" \
  "v_mfma_f32_32x32x16_bf16 v[0:15], a[16:19], v[40:43], v[0:15]\n" \
  "ds_read_b128 v[32:35], v96 offset:8192\n" \
  "v_mul_f32 v20, v20, v74\n" \
  "v_mul_f32 v21, v21, v74\n" \
  "v_mul_f32 v22, v22, v74\n" \
  "v_mul_f32 v23, v23, v74\n" \
  "s_waitcnt lgkmcnt(5)\n" \
  "v_mfma_f32_32x32x16_bf16 v[0:15], a[24:27], v[44:47], v[0:15]\n" \
  "ds_read_b128 v[36:39], v96 offset:9216\n" \
  "v_cvt_pk_bf16_f32 v102, v16, v17\n" \
  "v_cvt_pk_bf16_f32 v103, v18, v19\n" \
  "v_cvt_pk_bf16_f32 v104, v20, v21\n" \
  "v_cvt_pk_bf16_f32 v105, v22, v23\n" \
  "s_waitcnt lgkmcnt(5)\n" \
  "v_mfma_f32_32x32x16_bf16 v[0:15], a[32:35], v[48:51], v[0:15]\n" \
  "ds_read_b128 v[40:43], v96 offset:10240\n" \
  "buffer_store_dwordx4 v[102:105], v101, s[8:11], s65 offen offset:2048 sc1\n" \
  "v_mul_f32 v24, v24, v74\n" \
  "v_mul_f32 v25, v25, v74\n" \
  "v_mul_f32 v26, v26, v74\n" \
  "s_waitcnt lgkmcnt(5)\n" \
  "v_mfma_f32_32x32x16_bf16 v[0:15], a[40:43], v[52:55], v[0:15]\n" \
  "ds_read_b128 v[44:47], v96 offset:11264\n" \
  "v_mul_f32 v27, v27, v74\n" \
  "v_mul_f32 v28, v28, v74\n" \
  "v_mul_f32 v29, v29, v74\n" \
  "v_mul_f32 v30, v30, v74\n" \
  "s_waitcnt lgkmcnt(5)\n" \
  "v_mfma_f32_32x32x16_bf16 v[0:15], a[48:51], v[56:59], v[0:15]\n" \
  "ds_read_b128 v[48:51], v96 offset:12288\n" \
  "v_mul_f32 v31, v31, v74\n" \
  "v_cvt_pk_bf16_f32 v106, v24, v25\n" \
  "v_cvt_pk_bf16_f32 v107, v26, v27\n" \
  "v_cvt_pk_bf16_f32 v108, v28, v29\n" \
  "s_waitcnt lgkmcnt(5)\n" \
  "v_mfma_f32_32x32x16_bf16 v[0:15], a[56:59], v[60:63], v[0:15]\n" \
  "ds_read_b128 v[52:55], v96 offset:13312\n" \
  "v_cvt_pk_bf16_f32 v109, v30, v31\n" \
  "buffer_store_dwordx4 v[106:109], v101, s[8:11], s65 offen offset:3072 sc1\n" \
  "s_waitcnt lgkmcnt(5)\n" \
  "v_mfma_f32_32x32x16_bf16 v[0:15], a[64:67], v[32:35], v[0:15]\n" \
  "ds_read_b128 v[56:59], v96 offset:14336\n" \
  "s_waitcnt lgkmcnt(5)\n" \
  "v_mfma_f32_32x32x16_bf16 v[0:15], a[72:75], v[36:39], v[0:15]\n" \
  "ds_read_b128 v[60:63], v96 offset:15360\n" \
  "s_waitcnt lgkmcnt(5)\n" \
  "v_mfma_f32_32x32x16_bf16 v[0:15], a[80:83], v[40:43], v[0:15]\n" \
  "ds_read_b128 v[32:35], v96 offset:16384\n" \
  "s_waitcnt lgkmcnt(5)\n" \
  "v_mfma_f32_32x32x16_bf16 v[0:15], a[88:91], v[44:47], v[0:15]\n" \
  "ds_read_b128 v[36:39], v96 offset:17408\n" \
  "s_waitcnt lgkmcnt(5)\n" \
  "v_mfma_f32_32x32x16_bf16 v[0:15], a[96:99], v[48:51], v[0:15]\n" \
  "ds_read_b128 v[40:43], v96 offset:18432\n" \
  "v_mov_b32 v74, v122\n" \
  "s_waitcnt lgkmcnt(5)\n" \
  "v_mfma_f32_32x32x16_bf16 v[0:15], a[104:107], v[52:55], v[0:15]\n" \
  "ds_read_b128 v[44:47], v96 offset:19456\n" \
  "s_waitcnt lgkmcnt(5)\n" \
  "v_mfma_f32_32x32x16_bf16 v[0:15], a[112:115], v[56:59], v[0:15]\n" \
  "ds_read_b128 v[48:51], v96 offset:20480\n" \
  "s_waitcnt lgkmcnt(5)\n" \
  "v_mfma_f32_32x32x16_bf16 v[0:15], a[120:123], v[60:63], v[0:15]\n" \
  "ds_read_b128 v[52:55], v96 offset:21504\n" \
  "s_waitcnt lgkmcnt(5)\n" \
  "v_mfma_f32_32x32x16_bf16 v[0:15], a[128:131], v[32:35], v[0:15]\n" \
  "ds_read_b128 v[56:59], v96 offset:22528\n" \
  "s_waitcnt lgkmcnt(5)\n" \
  "v_mfma_f32_32x32x16_bf16 v[0:15], a[136:139], v[36:39], v[0:15]\n" \
  "ds_read_b128 v[60:63], v96 offset:23552\n" \
  "s_waitcnt lgkmcnt(5)\n" \
  "v_mfma_f32_32x32x16_bf16 v[0:15], a[144:147], v[40:43], v[0:15]\n" \
  "ds_read_b128 v[32:35], v96 offset:24576\n" \
  "s_waitcnt lgkmcnt(5)\n" \
  "v_mfma_f32_32x32x16_bf16 v[0:15], a[152:155], v[44:47], v[0:15]\n" \
  "ds_read_b128 v[36:39], v96 offset:25600\n" \
  "s_waitcnt lgkmcnt(5)\n" \
  "v_mfma_f32_32x32x16_bf16 v[0:15], a[160:163], v[48:51], v[0:15]\n" \
  "ds_read_b128 v[40:43], v96 offset:26624\n" \
  "s_waitcnt lgkmcnt(5)\n" \
  "v_mfma_f32_32x32x16_bf16 v[0:15], a[168:171], v[52:55], v[0:15]\n" \
  "ds_read_b128 v[44:47], v96 offset:27648\n" \
  "s_waitcnt lgkmcnt(5)\n" \
  "v_mfma_f32_32x32x16_bf16 v[0:15], a[176:179], v[56:59], v[0:15]\n" \
  "ds_read_b128 v[48:51], v96 offset:28672\n" \
  "s_waitcnt lgkmcnt(5)\n" \
  "v_mfma_f32_32x32x16_bf16 v[0:15], a[184:187], v[60:63], v[0:15]\n" \
  "ds_read_b128 v[52:55], v96 offset:29696\n" \
  "s_waitcnt lgkmcnt(5)\n" \
  "v_mfma_f32_32x32x16_bf16 v[0:15], a[192:195], v[32:35], v[0:15]\n" \
  "ds_read_b128 v[56:59], v96 offset:30720\n" \
  "s_waitcnt lgkmcnt(5)\n" \
  "v_mfma_f32_32x32x16_bf16 v[0:15], a[200:203], v[36:39], v[0:15]\n" \
  "ds_read_b128 v[60:63], v96 offset:31744\n" \
  "s_waitcnt lgkmcnt(5)\n" \
  "v_mfma_f32_32x32x16_bf16 v[0:15], a[208:211], v[40:43], v[0:15]\n" \
  "ds_read_b128 v[32:35], v96 offset:32768\n" \
  "s_waitcnt lgkmcnt(5)\n" \
  "v_mfma_f32_32x32x16_bf16 v[0:15], a[216:219], v[44:47], v[0:15]\n" \
  "ds_read_b128 v[36:39], v96 offset:33792\n" \
  "s_waitcnt lgkmcnt(5)\n" \
  "v_mfma_f32_32x32x16_bf16 v[0:15], a[224:227], v[48:51], v[0:15]\n" \
  "ds_read_b128 v[40:43], v96 offset:34816\n" \
  "s_waitcnt vmcnt(0)\n" \
  "v_mov_b32 v92, s56\n" \
  "s_mov_b64 exec, 1\n" \
  "buffer_store_dword v92, off, s[4:7], s20 sc1\n" \
  "s_mov_b64 exec, -1\n" \
  "s_add_i32 s60, s56, 2\n" \
  "v_cmp_gt_u32 vcc, s60, v100\n" \
  "s_waitcnt lgkmcnt(5)\n" \
  "v_mfma_f32_32x32x16_bf16 v[0:15], a[232:235], v[52:55], v[0:15]\n" \
  "ds_read_b128 v[44:47], v96 offset:35840\n" \
  "s_add_i32 s60, s56, 1\n" \
  "s_cmp_eq_u64 vcc, 0\n" \
  "s_cselect_b32 s61, 1, 0\n" \
  "s_cselect_b32 s58, 0, 0x7fff0000\n" \
  "v_or_b32 v93, s58, v110\n" \
  "v_or_b32 v94, s58, v112\n" \
  "s_waitcnt lgkmcnt(5)\n" \
  "v_mfma_f32_32x32x16_bf16 v[0:15], a[240:243], v[56:59], v[0:15]\n" \
  "ds_read_b128 v[48:51], v96 offset:36864\n" \
  "s_lshr_b32 s59, s60, 6\n" \
  "s_mul_i32 s59, s59, s22\n" \
  "s_and_b32 s58, s60, 63\n" \
  "s_mul_i32 s63, s58, 1280\n" \
  "s_add_i32 s63, s63, s59\n" \
  "s_add_i32 s63, s63, s18\n" \
  "s_mul_i32 s58, s58, 40960\n" \
  "s_add_i32 s59, s59, s58\n" \
  "s_add_i32 s59, s59, s17\n" \
  "s_lshl_b32 s58, s16, 2\n" \
  "s_add_i32 m0, s58, s62\n" \
  "s_waitcnt lgkmcnt(5)\n" \
  "v_mfma_f32_32x32x16_bf16 v[0:15], a[248:251], v[60:63], v[0:15]\n" \
  "ds_read_b128 v[52:55], v96 offset:37888\n" \
  "buffer_load_dwordx2 v[64:65], v94, s[0:3], s63 offen offset:0 sc1\n" \
  "s_waitcnt lgkmcnt(5)\n" \
  "v_mfma_f32_32x32x16_bf16 v[0:15], v[192:195], v[32:35], v[0:15]\n" \
  "ds_read_b128 v[56:59], v96 offset:38912\n" \
  "buffer_load_dwordx2 v[66:67], v94, s[0:3], s63 offen offset:256 sc1\n" \
  "s_waitcnt lgkmcnt(5)\n" \
  "v_mfma_f32_32x32x16_bf16 v[0:15], v[200:203], v[36:39], v[0:15]\n" \
  "ds_read_b128 v[60:63], v96 offset:39936\n" \
  "buffer_load_dwordx2 v[68:69], v94, s[0:3], s63 offen offset:512 sc1\n" \
  "s_waitcnt lgkmcnt(5)\n" \
  "v_mfma_f32_32x32x16_bf16 v[0:15], v[208:211], v[40:43], v[0:15]\n" \
  "ds_read_b128 v[32:35], v96 offset:0\n" \
  "buffer_load_dwordx4 v93, s[0:3], s59 offen sc1 lds\n" \
  "s_add_i32 m0, m0, 4096\n" \
  "s_add_i32 s59, s59, 4096\n" \
  "s_waitcnt lgkmcnt(5)\n" \
  "v_mfma_f32_32x32x16_bf16 v[0:15], v[216:219], v[44:47], v[0:15]\n" \
  "ds_read_b128 v[36:39], v96 offset:1024\n" \
  "s_waitcnt lgkmcnt(5)\n" \
  "v_mfma_f32_32x32x16_bf16 v[0:15], v[224:227], v[48:51], v[0:15]\n" \
  "ds_read_b128 v[40:43], v96 offset:2048\n" \
  "s_waitcnt lgkmcnt(5)\n" \
  "v_mfma_f32_32x32x16_bf16 v[0:15], v[232:235], v[52:55], v[0:15]\n" \
  "ds_read_b128 v[44:47], v96 offset:3072\n" \
  "buffer_load_dwordx4 v93, s[0:3], s59 offen sc1 lds\n" \
  "s_add_i32 m0, m0, 4096\n" \
  "s_add_i32 s59, s59, 4096\n" \
  "s_waitcnt lgkmcnt(5)\n" \
  "v_mfma_f32_32x32x16_bf16 v[0:15], v[240:243], v[56:59], v[0:15]\n" \
  "ds_read_b128 v[48:51], v96 offset:4096\n" \
  "s_waitcnt lgkmcnt(5)\n" \
  "v_mfma_f32_32x32x16_bf16 v[0:15], v[248:251], v[60:63], v[0:15]\n" \
  "ds_read_b128 v[52:55], v96 offset:5120\n" \
  "v_mfma_f32_32x32x8_bf16 v[16:31], v[190:191], v[72:73], 0\n" \
  "s_waitcnt lgkmcnt(5)\n" \
  "v_mfma_f32_32x32x16_bf16 v[16:31], a[4:7], v[32:35], v[16:31]\n" \
  "ds_read_b128 v[56:59], v96 offset:6144\n" \
  "buffer_load_dwordx4 v93, s[0:3], s59 offen sc1 lds\n" \
  "s_add_i32 m0, m0, 4096\n" \
  "s_add_i32 s59, s59, 4096\n" \
  "s_waitcnt lgkmcnt(5)\n" \
  "v_mfma_f32_32x32x16_bf16 v[16:31], a[12:15], v[36:39], v[16:31]\n" \
  "ds_read_b128 v[60:63], v96 offset:7168\n" \
  "s_waitcnt lgkmcnt(5)\n" \
  "v_mfma_f32_32x32x16_bf16 v[16:31], a[20:23], v[40:43], v[16:31]\n" \
  "ds_read_b128 v[32:35], v96 offset:8192\n" \
  "s_waitcnt lgkmcnt(5)\n" \
  "v_mfma_f32_32x32x16_bf16 v[16:31], a[28:31], v[44:47], v[16:31]\n" \
  "ds_read_b128 v[36:39], v96 offset:9216\n" \
  "buffer_load_dwordx4 v93, s[0:3], s59 offen sc1 lds\n" \
  "s_add_i32 m0, m0, 4096\n" \
  "s_add_i32 s59, s59, 4096\n" \
  "s_waitcnt lgkmcnt(5)\n" \
  "v_mfma_f32_32x32x16_bf16 v[16:31], a[36:39], v[48:51], v[16:31]\n" \
  "ds_read_b128 v[40:43], v96 offset:10240\n" \
  "s_waitcnt lgkmcnt(5)\n" \
  "v_mfma_f32_32x32x16_bf16 v[16:31], a[44:47], v[52:55], v[16:31]\n" \
  "ds_read_b128 v[44:47], v96 offset:11264\n" \
  "s_waitcnt lgkmcnt(5)\n" \
  "v_mfma_f32_32x32x16_bf16 v[16:31], a[52:55], v[56:59], v[16:31]\n" \
  "ds_read_b128 v[48:51], v96 offset:12288\n" \
  "buffer_load_dwordx4 v93, s[0:3], s59 offen sc1 lds\n" \
  "s_add_i32 m0, m0, 4096\n" \
  "s_add_i32 s59, s59, 4096\n" \
  "s_waitcnt lgkmcnt(5)\n" \
  "v_mfma_f32_32x32x16_bf16 v[16:31], a[60:63], v[60:63], v[16:31]\n" \
  "ds_read_b128 v[52:55], v96 offset:13312\n" \
  "s_waitcnt lgkmcnt(5)\n" \
  "v_mfma_f32_32x32x16_bf16 v[16:31], a[68:71], v[32:35], v[16:31]\n" \
  "ds_read_b128 v[56:59], v96 offset:14336\n" \
  "s_waitcnt lgkmcnt(5)\n" \
  "v_mfma_f32_32x32x16_bf16 v[16:31], a[76:79], v[36:39], v[16:31]\n" \
  "ds_read_b128 v[60:63], v96 offset:15360\n" \
  "buffer_load_dwordx4 v93, s[0:3], s59 offen sc1 lds\n" \
  "s_add_i32 m0, m0, 4096\n" \
  "s_add_i32 s59, s59, 4096\n" \
  "s_waitcnt lgkmcnt(5)\n" \
  "v_mfma_f32_32x32x16_bf16 v[16:31], a[84:87], v[40:43], v[16:31]\n" \
  "ds_read_b128 v[32:35], v96 offset:16384\n" \
  "s_waitcnt lgkmcnt(5)\n" \
  "v_mfma_f32_32x32x16_bf16 v[16:31], a[92:95], v[44:47], v[16:31]\n" \
  "ds_read_b128 v[36:39], v96 offset:17408\n" \
  "s_waitcnt lgkmcnt(5)\n" \
  "v_mfma_f32_32x32x16_bf16 v[16:31], a[100:103], v[48:51], v[16:31]\n" \
  "ds_read_b128 v[40:43], v96 offset:18432\n" \
  "buffer_load_dwordx4 v93, s[0:3], s59 offen sc1 lds\n" \
  "s_add_i32 m0, m0, 4096\n" \
  "s_add_i32 s59, s59, 4096\n" \
  "s_waitcnt lgkmcnt(5)\n" \
  "v_mfma_f32_32x32x16_bf16 v[16:31], a[108:111], v[52:55], v[16:31]\n" \
  "ds_read_b128 v[44:47], v96 offset:19456\n" \
  "s_waitcnt lgkmcnt(5)\n" \
  "v_mfma_f32_32x32x16_bf16 v[16:31], a[116:119], v[56:59], v[16:31]\n" \
  "ds_read_b128 v[48:51], v96 offset:20480\n" \
  "s_waitcnt lgkmcnt(5)\n" \
  "v_mfma_f32_32x32x16_bf16 v[16:31], a[124:127], v[60:63], v[16:31]\n" \
  "ds_read_b128 v[52:55], v96 offset:21504\n" \
  "buffer_load_dwordx4 v93, s[0:3], s59 offen sc1 lds\n" \
  "s_add_i32 m0, m0, 4096\n" \
  "s_add_i32 s59, s59, 4096\n" \
  "s_waitcnt lgkmcnt(5)\n" \
  "v_mfma_f32_32x32x16_bf16 v[16:31], a[132:135], v[32:35], v[16:31]\n" \
  "ds_read_b128 v[56:59], v96 offset:22528\n" \
  "s_waitcnt lgkmcnt(5)\n" \
  "v_mfma_f32_32x32x16_bf16 v[16:31], a[140:143], v[36:39], v[16:31]\n" \
  "ds_read_b128 v[60:63], v96 offset:23552\n" \
  "s_waitcnt lgkmcnt(5)\n" \
  "v_mfma_f32_32x32x16_bf16 v[16:31], a[148:151], v[40:43], v[16:31]\n" \
  "ds_read_b128 v[32:35], v96 offset:24576\n" \
  "buffer_load_dwordx4 v93, s[0:3], s59 offen sc1 lds\n" \
  "s_add_i32 m0, m0, 4096\n" \
  "s_add_i32 s59, s59, 4096\n" \
  "s_waitcnt lgkmcnt(5)\n" \
  "v_mfma_f32_32x32x16_bf16 v[16:31], a[156:159], v[44:47], v[16:31]\n" \
  "ds_read_b128 v[36:39], v96 offset:25600\n" \
  "s_waitcnt lgkmcnt(5)\n" \
  "v_mfma_f32_32x32x16_bf16 v[16:31], a[164:167], v[48:51], v[16:31]\n" \
  "ds_read_b128 v[40:43], v96 offset:26624\n" \
  "s_waitcnt lgkmcnt(5)\n" \
  "v_mfma_f32_32x32x16_bf16 v[16:31], a[172:175], v[52:55], v[16:31]\n" \
  "ds_read_b128 v[44:47], v96 offset:27648\n" \
  "buffer_load_dwordx4 v93, s[0:3], s59 offen sc1 lds\n" \
  "s_add_i32 m0, m0, 4096\n" \
  "s_add_i32 s59, s59, 4096\n" \
  "s_waitcnt lgkmcnt(5)\n" \
  "v_mfma_f32_32x32x16_bf16 v[16:31], a[180:183], v[56:59], v[16:31]\n" \
  "ds_read_b128 v[48:51], v96 offset:28672\n" \
  "v_mul_f32 v0, v0, v74\n" \
  "v_mul_f32 v1, v1, v74\n" \
  "v_mul_f32 v2, v2, v74\n" \
  "v_mul_f32 v3, v3, v74\n" \
  "s_waitcnt lgkmcnt(5)\n" \
  "v_mfma_f32_32x32x16_bf16 v[16:31], a[188:191], v[60:63], v[16:31]\n" \
  "ds_read_b128 v[52:55], v96 offset:29696\n" \
  "v_mul_f32 v4, v4, v74\n" \
  "v_mul_f32 v5, v5, v74\n" \
  "v_mul_f32 v6, v6, v74\n" \
  "v_mul_f32 v7, v7, v74\n" \
  "s_waitcnt lgkmcnt(5)\n" \
  "v_mfma_f32_32x32x16_bf16 v[16:31], a[196:199], v[32:35], v[16:31]\n" \
  "ds_read_b128 v[56:59], v96 offset:30720\n" \
  "v_cvt_pk_bf16_f32 v102, v0, v1\n" \
  "v_cvt_pk_bf16_f32 v103, v2, v3\n" \
  "v_cvt_pk_bf16_f32 v104, v4, v5\n" \
  "v_cvt_pk_bf16_f32 v105, v6, v7\n" \
  "s_waitcnt lgkmcnt(5)\n" \
  "v_mfma_f32_32x32x16_bf16 v[16:31], a[204:207], v[36:39], v[16:31]\n" \
  "ds_read_b128 v[60:63], v96 offset:31744\n" \
  "buffer_store_dwordx4 v[102:105], v114, s[8:11], s64 offen offset:0 sc1\n" \
  "v_mul_f32 v8, v8, v74\n" \
  "v_mul_f32 v9, v9, v74\n" \
  "v_mul_f32 v10, v10, v74\n" \
  "s_waitcnt lgkmcnt(5)\n" \
  "v_mfma_f32_32x32x16_bf16 v[16:31], a[212:215], v[40:43], v[16:31]\n" \
  "ds_read_b128 v[32:35], v96 offset:32768\n" \
  "v_mul_f32 v11, v11, v74\n" \
  "v_mul_f32 v12, v12, v74\n" \
  "v_mul_f32 v13, v13, v74\n" \
  "v_mul_f32 v14, v14, v74\n" \
  "s_waitcnt lgkmcnt(5)\n" \
  "v_mfma_f32_32x32x16_bf16 v[16:31], a[220:223], v[44:47], v[16:31]\n" \
  "ds_read_b128 v[36:39], v96 offset:33792\n" \
  "v_mul_f32 v15, v15, v74\n" \
  "v_cvt_pk_bf16_f32 v106, v8, v9\n" \
  "v_cvt_pk_bf16_f32 v107, v10, v11\n" \
  "v_cvt_pk_bf16_f32 v108, v12, v13\n" \
  "s_waitcnt lgkmcnt(5)\n" \
  "v_mfma_f32_32x32x16_bf16 v[16:31], a[228:231], v[48:51], v[16:31]\n" \
  "ds_read_b128 v[40:43], v96 offset:34816\n" \
  "v_cvt_pk_bf16_f32 v109, v14, v15\n" \
  "buffer_store_dwordx4 v[106:109], v114, s[8:11], s64 offen offset:1024 sc1\n" \
  "s_waitcnt lgkmcnt(5)\n" \
  "v_mfma_f32_32x32x16_bf16 v[16:31], a[236:239], v[52:55], v[16:31]\n" \
  "ds_read_b128 v[44:47], v96 offset:35840\n" \
  "s_waitcnt lgkmcnt(5)\n" \
  "v_mfma_f32_32x32x16_bf16 v[16:31], a[244:247], v[56:59], v[16:31]\n" \
  "ds_read_b128 v[48:51], v96 offset:36864\n" \
  "s_waitcnt vmcnt(12)\n" \
  "v_cndmask_b32 v68, 0, v68, s[66:67]\n" \
  "v_cndmask_b32 v69, 0, v69, s[66:67]\n" \
  "v_add_f32 v64, v64, v66\n" \
  "s_waitcnt lgkmcnt(5)\n" \
  "v_mfma_f32_32x32x16_bf16 v[16:31], a[252:255], v[60:63], v[16:31]\n" \
  "ds_read_b128 v[52:55], v96 offset:37888\n" \
  "v_add_f32 v65, v65, v67\n" \
  "v_add_f32 v64, v64, v68\n" \
  "v_add_f32 v65, v65, v69\n" \
  "v_mov_b32 v66, v64\n" \
  "s_waitcnt lgkmcnt(5)\n" \
  "v_mfma_f32_32x32x16_bf16 v[16:31], v[196:199], v[32:35], v[16:31]\n" \
  "ds_read_b128 v[56:59], v96 offset:38912\n" \
  "v_mov_b32 v67, v65\n" \
  "v_mov_b32 v68, 0x3acccccd\n" \
  "v_permlane32_swap_b32 v64, v66\n" \
  "v_permlane32_swap_b32 v65, v67\n" \
  "s_waitcnt lgkmcnt(5)\n" \
  "v_mfma_f32_32x32x16_bf16 v[16:31], v[204:207], v[36:39], v[16:31]\n" \
  "ds_read_b128 v[60:63], v96 offset:39936\n" \
  "v_add_f32 v64, v64, v66\n" \
  "v_add_f32 v65, v65, v67\n" \
  "v_mul_f32 v64, 0xbacccccd, v64\n" \
  "v_mul_f32 v66, v64, v64\n" \
  "s_waitcnt lgkmcnt(5)\n" \
  "v_mfma_f32_32x32x16_bf16 v[16:31], v[212:215], v[40:43], v[16:31]\n" \
  "v_fma_f32 v65, v65, v68, -v66\n" \
  "v_max_f32 v65, 0, v65\n" \
  "v_add_f32 v65, 0x3727c5ac, v65\n" \
  "v_rsq_f32 v65, v65\n" \
  "s_waitcnt lgkmcnt(4)\n" \
  "v_mfma_f32_32x32x16_bf16 v[16:31], v[220:223], v[44:47], v[16:31]\n" \
  "v_cvt_pk_bf16_f32 v120, v64, v64\n" \
  "v_and_b32 v66, 0xffff0000, v120\n" \
  "v_sub_f32 v66, v64, v66\n" \
  "v_cvt_pk_bf16_f32 v121, v66, 0\n" \
  "s_waitcnt lgkmcnt(3)\n" \
  "v_mfma_f32_32x32x16_bf16 v[16:31], v[228:231], v[48:51], v[16:31]\n" \
  "v_mul_f32 v122, s23, v65\n" \
  "v_cndmask_b32 v120, 0, v120, s[66:67]\n" \
  "v_cndmask_b32 v121, 0, v121, s[66:67]\n" \
  "s_waitcnt lgkmcnt(2)\n" \
  "v_mfma_f32_32x32x16_bf16 v[16:31], v[236:239], v[52:55], v[16:31]\n" \
  "s_waitcnt lgkmcnt(1)\n" \
  "v_mfma_f32_32x32x16_bf16 v[16:31], v[244:247], v[56:59], v[16:31]\n" \
  "s_waitcnt lgkmcnt(0)\n" \
  "v_mfma_f32_32x32x16_bf16 v[16:31], v[252:255], v[60:63], v[16:31]\n" \
  "s_mov_b32 s65, s64\n" \
  "s_add_i32 s56, s56, 1\n" \
  "s_cmp_lg_u32 s61, 0\n" \
  "s_cbranch_scc1 LSEG_%=\n" \
  "s_nop 7\n" \
  "v_mul_f32 v16, v16, v74\n" \
  "v_mul_f32 v17, v17, v74\n" \
  "v_mul_f32 v18, v18, v74\n" \
  "v_mul_f32 v19, v19, v74\n" \
  "v_mul_f32 v20, v20, v74\n" \
  "v_mul_f32 v21, v21, v74\n" \
  "v_mul_f32 v22, v22, v74\n" \
  "v_mul_f32 v23, v23, v74\n" \
  "v_cvt_pk_bf16_f32 v102, v16, v17\n" \
  "v_cvt_pk_bf16_f32 v103, v18, v19\n" \
  "v_cvt_pk_bf16_f32 v104, v20, v21\n" \
  "v_cvt_pk_bf16_f32 v105, v22, v23\n" \
  "buffer_store_dwordx4 v[102:105], v114, s[8:11], s64 offen offset:2048 sc1\n" \
  "v_mul_f32 v24, v24, v74\n" \
  "v_mul_f32 v25, v25, v74\n" \
  "v_mul_f32 v26, v26, v74\n" \
  "v_mul_f32 v27, v27, v74\n" \
  "v_mul_f32 v28, v28, v74\n" \
  "v_mul_f32 v29, v29, v74\n" \
  "v_mul_f32 v30, v30, v74\n" \
  "v_mul_f32 v31, v31, v74\n" \
  "v_cvt_pk_bf16_f32 v106, v24, v25\n" \
  "v_cvt_pk_bf16_f32 v107, v26, v27\n" \
  "v_cvt_pk_bf16_f32 v108, v28, v29\n" \
  "v_cvt_pk_bf16_f32 v109, v30, v31\n" \
  "buffer_store_dwordx4 v[106:109], v114, s[8:11], s64 offen offset:3072 sc1\n" \
  "s_cmp_lt_u32 s56, s21\n" \
  "s_cbranch_scc1 LSEG_%=\n" \
  "s_waitcnt vmcnt(0)\n" \
  "v_mov_b32 v92, s56\n" \
  "s_mov_b64 exec, 1\n" \
  "buffer_store_dword v92, off, s[4:7], s20 sc1\n" \
  "s_mov_b64 exec, -1\n" \
  "s_endpgm\n"
#define G4V_ASM \
  "v_mbcnt_lo_u32_b32 v0, -1, 0\n" \
  "v_mbcnt_hi_u32_b32 v0, -1, v0\n" \
  "v_lshlrev_b32 v0, 2, v0\n" \
  "v_add_u32 v0, %0, v0\n" \
  "v_add_u32 v0, 146432, v0\n" \
  "ds_read_b32 v0, v0\n" \
  "s_waitcnt lgkmcnt(0)\n" \
  "v_readlane_b32 s0, v0, 0\n" \
  "v_readlane_b32 s1, v0, 1\n" \
  "v_readlane_b32 s2, v0, 2\n" \
  "v_readlane_b32 s3, v0, 3\n" \
  "v_readlane_b32 s4, v0, 4\n" \
  "v_readlane_b32 s5, v0, 5\n" \
  "v_readlane_b32 s6, v0, 6\n" \
  "v_readlane_b32 s7, v0, 7\n" \
  "v_readlane_b32 s8, v0, 8\n" \
  "v_readlane_b32 s9, v0, 9\n" \
  "v_readlane_b32 s10, v0, 10\n" \
  "v_readlane_b32 s11, v0, 11\n" \
  "v_readlane_b32 s12, v0, 12\n" \
  "v_readlane_b32 s13, v0, 13\n" \
  "v_readlane_b32 s14, v0, 14\n" \
  "v_readlane_b32 s15, v0, 15\n" \
  "v_readlane_b32 s16, v0, 16\n" \
  "v_readlane_b32 s17, v0, 17\n" \
  "v_readlane_b32 s18, v0, 18\n" \
  "v_readlane_b32 s19, v0, 19\n" \
  "v_readlane_b32 s20, v0, 20\n" \
  "v_readlane_b32 s21, v0, 21\n" \
  "v_readlane_b32 s22, v0, 22\n" \
  "v_readlane_b32 s23, v0, 23\n" \
  "s_nop 4\n" \
  "v_mbcnt_lo_u32_b32 v0, -1, 0\n" \
  "v_mbcnt_hi_u32_b32 v0, -1, v0\n" \
  "v_lshlrev_b32 v0, 2, v0\n" \
  "v_add_u32 v0, s16, v0\n" \
  "v_add_u32 v0, 131072, v0\n" \
  "ds_read_b32 v110, v0 offset:0\n" \
  "ds_read_b32 v111, v0 offset:1024\n" \
  "ds_read_b32 v112, v0 offset:2048\n" \
  "ds_read_b32 v113, v0 offset:3072\n" \
  "ds_read_b32 v114, v0 offset:4096\n" \
  "ds_read_b32 v115, v0 offset:5120\n" \
  "ds_read_b32 v116, v0 offset:6144\n" \
  "ds_read_b32 v117, v0 offset:7168\n" \
  "s_waitcnt lgkmcnt(0)\n" \
  "s_barrier\n" \
  "s_lshl_b32 s59, s16, 7\n" \
  "s_add_i32 m0, s59, 0\n" \
  "s_mov_b32 s58, 0\n" \
  "buffer_load_dwordx4 v115, s[12:15], s58 offen lds\n" \
  "s_add_i32 m0, s59, 1024\n" \
  "s_mov_b32 s58, 65536\n" \
  "buffer_load_dwordx4 v115, s[12:15], s58 offen lds\n" \
  "s_add_i32 m0, s59, 2048\n" \
  "s_mov_b32 s58, 131072\n" \
  "buffer_load_dwordx4 v115, s[12:15], s58 offen lds\n" \
  "s_add_i32 m0, s59, 3072\n" \
  "s_mov_b32 s58, 196608\n" \
  "buffer_load_dwordx4 v115, s[12:15], s58 offen lds\n" \
  "s_add_i32 m0, s59, 4096\n" \
  "s_mov_b32 s58, 262144\n" \
  "buffer_load_dwordx4 v115, s[12:15], s58 offen lds\n" \
  "s_add_i32 m0, s59, 5120\n" \
  "s_mov_b32 s58, 327680\n" \
  "buffer_load_dwordx4 v115, s[12:15], s58 offen lds\n" \
  "s_add_i32 m0, s59, 6144\n" \
  "s_mov_b32 s58, 393216\n" \
  "buffer_load_dwordx4 v115, s[12:15], s58 offen lds\n" \
  "s_add_i32 m0, s59, 7168\n" \
  "s_mov_b32 s58, 458752\n" \
  "buffer_load_dwordx4 v115, s[12:15], s58 offen lds\n" \
  "s_add_i32 m0, s59, 8192\n" \
  "s_mov_b32 s58, 524288\n" \
  "buffer_load_dwordx4 v115, s[12:15], s58 offen lds\n" \
  "s_add_i32 m0, s59, 9216\n" \
  "s_mov_b32 s58, 589824\n" \
  "buffer_load_dwordx4 v115, s[12:15], s58 offen lds\n" \
  "s_add_i32 m0, s59, 10240\n" \
  "s_mov_b32 s58, 655360\n" \
  "buffer_load_dwordx4 v115, s[12:15], s58 offen lds\n" \
  "s_add_i32 m0, s59, 11264\n" \
  "s_mov_b32 s58, 720896\n" \
  "buffer_load_dwordx4 v115, s[12:15], s58 offen lds\n" \
  "s_add_i32 m0, s59, 12288\n" \
  "s_mov_b32 s58, 786432\n" \
  "buffer_load_dwordx4 v115, s[12:15], s58 offen lds\n" \
  "s_add_i32 m0, s59, 13312\n" \
  "s_mov_b32 s58, 851968\n" \
  "buffer_load_dwordx4 v115, s[12:15], s58 offen lds\n" \
  "s_add_i32 m0, s59, 14336\n" \
  "s_mov_b32 s58, 917504\n" \
  "buffer_load_dwordx4 v115, s[12:15], s58 offen lds\n" \
  "s_add_i32 m0, s59, 15360\n" \
  "s_mov_b32 s58, 983040\n" \
  "buffer_load_dwordx4 v115, s[12:15], s58 offen lds\n" \
  "s_add_i32 m0, s59, 16384\n" \
  "s_mov_b32 s58, 1048576\n" \
  "buffer_load_dwordx4 v115, s[12:15], s58 offen lds\n" \
  "s_add_i32 m0, s59, 17408\n" \
  "s_mov_b32 s58, 1114112\n" \
  "buffer_load_dwordx4 v115, s[12:15], s58 offen lds\n" \
  "s_add_i32 m0, s59, 18432\n" \
  "s_mov_b32 s58, 1179648\n" \
  "buffer_load_dwordx4 v115, s[12:15], s58 offen lds\n" \
  "s_add_i32 m0, s59, 19456\n" \
  "s_mov_b32 s58, 1245184\n" \
  "buffer_load_dwordx4 v115, s[12:15], s58 offen lds\n" \
  "s_add_i32 m0, s59, 20480\n" \
  "s_mov_b32 s58, 1310720\n" \
  "buffer_load_dwordx4 v115, s[12:15], s58 offen lds\n" \
  "s_add_i32 m0, s59, 21504\n" \
  "s_mov_b32 s58, 1376256\n" \
  "buffer_load_dwordx4 v115, s[12:15], s58 offen lds\n" \
  "s_add_i32 m0, s59, 22528\n" \
  "s_mov_b32 s58, 1441792\n" \
  "buffer_load_dwordx4 v115, s[12:15], s58 offen lds\n" \
  "s_add_i32 m0, s59, 23552\n" \
  "s_mov_b32 s58, 1507328\n" \
  "buffer_load_dwordx4 v115, s[12:15], s58 offen lds\n" \
  "s_add_i32 m0, s59, 24576\n" \
  "s_mov_b32 s58, 1572864\n" \
  "buffer_load_dwordx4 v115, s[12:15], s58 offen lds\n" \
  "s_add_i32 m0, s59, 25600\n" \
  "s_mov_b32 s58, 1638400\n" \
  "buffer_load_dwordx4 v115, s[12:15], s58 offen lds\n" \
  "s_add_i32 m0, s59, 26624\n" \
  "s_mov_b32 s58, 1703936\n" \
  "buffer_load_dwordx4 v115, s[12:15], s58 offen lds\n" \
  "s_add_i32 m0, s59, 27648\n" \
  "s_mov_b32 s58, 1769472\n" \
  "buffer_load_dwordx4 v115, s[12:15], s58 offen lds\n" \
  "s_add_i32 m0, s59, 28672\n" \
  "s_mov_b32 s58, 1835008\n" \
  "buffer_load_dwordx4 v115, s[12:15], s58 offen lds\n" \
  "s_add_i32 m0, s59, 29696\n" \
  "s_mov_b32 s58, 1900544\n" \
  "buffer_load_dwordx4 v115, s[12:15], s58 offen lds\n" \
  "s_add_i32 m0, s59, 30720\n" \
  "s_mov_b32 s58, 1966080\n" \
  "buffer_load_dwordx4 v115, s[12:15], s58 offen lds\n" \
  "s_add_i32 m0, s59, 31744\n" \
  "s_mov_b32 s58, 2031616\n" \
  "buffer_load_dwordx4 v115, s[12:15], s58 offen lds\n" \
  "s_waitcnt vmcnt(16)\n" \
  "ds_read_b64_tr_b16 a[0:1], v116 offset:0\n" \
  "ds_read_b64_tr_b16 a[2:3], v116 offset:512\n" \
  "ds_read_b64_tr_b16 a[4:5], v116 offset:64\n" \
  "ds_read_b64_tr_b16 a[6:7], v116 offset:576\n" \
  "ds_read_b64_tr_b16 a[8:9], v116 offset:2048\n" \
  "ds_read_b64_tr_b16 a[10:11], v116 offset:2560\n" \
  "ds_read_b64_tr_b16 a[12:13], v116 offset:2112\n" \
  "ds_read_b64_tr_b16 a[14:15], v116 offset:2624\n" \
  "ds_read_b64_tr_b16 a[16:17], v116 offset:4096\n" \
  "ds_read_b64_tr_b16 a[18:19], v116 offset:4608\n" \
  "ds_read_b64_tr_b16 a[20:21], v116 offset:4160\n" \
  "ds_read_b64_tr_b16 a[22:23], v116 offset:4672\n" \
  "ds_read_b64_tr_b16 a[24:25], v116 offset:6144\n" \
  "ds_read_b64_tr_b16 a[26:27], v116 offset:6656\n" \
  "ds_read_b64_tr_b16 a[28:29], v116 offset:6208\n" \
  "ds_read_b64_tr_b16 a[30:31], v116 offset:6720\n" \
  "ds_read_b64_tr_b16 a[32:33], v116 offset:8192\n" \
  "ds_read_b64_tr_b16 a[34:35], v116 offset:8704\n" \
  "ds_read_b64_tr_b16 a[36:37], v116 offset:8256\n" \
  "ds_read_b64_tr_b16 a[38:39], v116 offset:8768\n" \
  "ds_read_b64_tr_b16 a[40:41], v116 offset:10240\n" \
  "ds_read_b64_tr_b16 a[42:43], v116 offset:10752\n" \
  "ds_read_b64_tr_b16 a[44:45], v116 offset:10304\n" \
  "ds_read_b64_tr_b16 a[46:47], v116 offset:10816\n" \
  "ds_read_b64_tr_b16 a[48:49], v116 offset:12288\n" \
  "ds_read_b64_tr_b16 a[50:51], v116 offset:12800\n" \
  "ds_read_b64_tr_b16 a[52:53], v116 offset:12352\n" \
  "ds_read_b64_tr_b16 a[54:55], v116 offset:12864\n" \
  "ds_read_b64_tr_b16 a[56:57], v116 offset:14336\n" \
  "ds_read_b64_tr_b16 a[58:59], v116 offset:14848\n" \
  "ds_read_b64_tr_b16 a[60:61], v116 offset:14400\n" \
  "ds_read_b64_tr_b16 a[62:63], v116 offset:14912\n" \
  "s_waitcnt lgkmcnt(0)\n" \
  "s_add_i32 m0, s59, 0\n" \
  "s_mov_b32 s58, 2097152\n" \
  "buffer_load_dwordx4 v115, s[12:15], s58 offen lds\n" \
  "s_add_i32 m0, s59, 1024\n" \
  "s_mov_b32 s58, 2162688\n" \
  "buffer_load_dwordx4 v115, s[12:15], s58 offen lds\n" \
  "s_add_i32 m0, s59, 2048\n" \
  "s_mov_b32 s58, 2228224\n" \
  "buffer_load_dwordx4 v115, s[12:15], s58 offen lds\n" \
  "s_add_i32 m0, s59, 3072\n" \
  "s_mov_b32 s58, 2293760\n" \
  "buffer_load_dwordx4 v115, s[12:15], s58 offen lds\n" \
  "s_add_i32 m0, s59, 4096\n" \
  "s_mov_b32 s58, 2359296\n" \
  "buffer_load_dwordx4 v115, s[12:15], s58 offen lds\n" \
  "s_add_i32 m0, s59, 5120\n" \
  "s_mov_b32 s58, 2424832\n" \
  "buffer_load_dwordx4 v115, s[12:15], s58 offen lds\n" \
  "s_add_i32 m0, s59, 6144\n" \
  "s_mov_b32 s58, 2490368\n" \
  "buffer_load_dwordx4 v115, s[12:15], s58 offen lds\n" \
  "s_add_i32 m0, s59, 7168\n" \
  "s_mov_b32 s58, 2555904\n" \
  "buffer_load_dwordx4 v115, s[12:15], s58 offen lds\n" \
  "s_add_i32 m0, s59, 8192\n" \
  "s_mov_b32 s58, 2621440\n" \
  "buffer_load_dwordx4 v115, s[12:15], s58 offen lds\n" \
  "s_add_i32 m0, s59, 9216\n" \
  "s_mov_b32 s58, 2686976\n" \
  "buffer_load_dwordx4 v115, s[12:15], s58 offen lds\n" \
  "s_add_i32 m0, s59, 10240\n" \
  "s_mov_b32 s58, 2752512\n" \
  "buffer_load_dwordx4 v115, s[12:15], s58 offen lds\n" \
  "s_add_i32 m0, s59, 11264\n" \
  "s_mov_b32 s58, 2818048\n" \
  "buffer_load_dwordx4 v115, s[12:15], s58 offen lds\n" \
  "s_add_i32 m0, s59, 12288\n" \
  "s_mov_b32 s58, 2883584\n" \
  "buffer_load_dwordx4 v115, s[12:15], s58 offen lds\n" \
  "s_add_i32 m0, s59, 13312\n" \
  "s_mov_b32 s58, 2949120\n" \
  "buffer_load_dwordx4 v115, s[12:15], s58 offen lds\n" \
  "s_add_i32 m0, s59, 14336\n" \
  "s_mov_b32 s58, 3014656\n" \
  "buffer_load_dwordx4 v115, s[12:15], s58 offen lds\n" \
  "s_add_i32 m0, s59, 15360\n" \
  "s_mov_b32 s58, 3080192\n" \
  "buffer_load_dwordx4 v115, s[12:15], s58 offen lds\n" \
  "s_waitcnt vmcnt(16)\n" \
  "ds_read_b64_tr_b16 a[64:65], v116 offset:16384\n" \
  "ds_read_b64_tr_b16 a[66:67], v116 offset:16896\n" \
  "ds_read_b64_tr_b16 a[68:69], v116 offset:16448\n" \
  "ds_read_b64_tr_b16 a[70:71], v116 offset:16960\n" \
  "ds_read_b64_tr_b16 a[72:73], v116 offset:18432\n" \
  "ds_read_b64_tr_b16 a[74:75], v116 offset:18944\n" \
  "ds_read_b64_tr_b16 a[76:77], v116 offset:18496\n" \
  "ds_read_b64_tr_b16 a[78:79], v116 offset:19008\n" \
  "ds_read_b64_tr_b16 a[80:81], v116 offset:20480\n" \
  "ds_read_b64_tr_b16 a[82:83], v116 offset:20992\n" \
  "ds_read_b64_tr_b16 a[84:85], v116 offset:20544\n" \
  "ds_read_b64_tr_b16 a[86:87], v116 offset:21056\n" \
  "ds_read_b64_tr_b16 a[88:89], v116 offset:22528\n" \
  "ds_read_b64_tr_b16 a[90:91], v116 offset:23040\n" \
  "ds_read_b64_tr_b16 a[92:93], v116 offset:22592\n" \
  "ds_read_b64_tr_b16 a[94:95], v116 offset:23104\n" \
  "ds_read_b64_tr_b16 a[96:97], v116 offset:24576\n" \
  "ds_read_b64_tr_b16 a[98:99], v116 offset:25088\n" \
  "ds_read_b64_tr_b16 a[100:101], v116 offset:24640\n" \
  "ds_read_b64_tr_b16 a[102:103], v116 offset:25152\n" \
  "ds_read_b64_tr_b16 a[104:105], v116 offset:26624\n" \
  "ds_read_b64_tr_b16 a[106:107], v116 offset:27136\n" \
  "ds_read_b64_tr_b16 a[108:109], v116 offset:26688\n" \
  "ds_read_b64_tr_b16 a[110:111], v116 offset:27200\n" \
  "ds_read_b64_tr_b16 a[112:113], v116 offset:28672\n" \
  "ds_read_b64_tr_b16 a[114:115], v116 offset:29184\n" \
  "ds_read_b64_tr_b16 a[116:117], v116 offset:28736\n" \
  "ds_read_b64_tr_b16 a[118:119], v116 offset:29248\n" \
  "ds_read_b64_tr_b16 a[120:121], v116 offset:30720\n" \
  "ds_read_b64_tr_b16 a[122:123], v116 offset:31232\n" \
  "ds_read_b64_tr_b16 a[124:125], v116 offset:30784\n" \
  "ds_read_b64_tr_b16 a[126:127], v116 offset:31296\n" \
  "s_waitcnt lgkmcnt(0)\n" \
  "s_add_i32 m0, s59, 16384\n" \
  "s_mov_b32 s58, 3145728\n" \
  "buffer_load_dwordx4 v115, s[12:15], s58 offen lds\n" \
  "s_add_i32 m0, s59, 17408\n" \
  "s_mov_b32 s58, 3211264\n" \
  "buffer_load_dwordx4 v115, s[12:15], s58 offen lds\n" \
  "s_add_i32 m0, s59, 18432\n" \
  "s_mov_b32 s58, 3276800\n" \
  "buffer_load_dwordx4 v115, s[12:15], s58 offen lds\n" \
  "s_add_i32 m0, s59, 19456\n" \
  "s_mov_b32 s58, 3342336\n" \
  "buffer_load_dwordx4 v115, s[12:15], s58 offen lds\n" \
  "s_add_i32 m0, s59, 20480\n" \
  "s_mov_b32 s58, 3407872\n" \
  "buffer_load_dwordx4 v115, s[12:15], s58 offen lds\n" \
  "s_add_i32 m0, s59, 21504\n" \
  "s_mov_b32 s58, 3473408\n" \
  "buffer_load_dwordx4 v115, s[12:15], s58 offen lds\n" \
  "s_add_i32 m0, s59, 22528\n" \
  "s_mov_b32 s58, 3538944\n" \
  "buffer_load_dwordx4 v115, s[12:15], s58 offen lds\n" \
  "s_add_i32 m0, s59, 23552\n" \
  "s_mov_b32 s58, 3604480\n" \
  "buffer_load_dwordx4 v115, s[12:15], s58 offen lds\n" \
  "s_add_i32 m0, s59, 24576\n" \
  "s_mov_b32 s58, 3670016\n" \
  "buffer_load_dwordx4 v115, s[12:15], s58 offen lds\n" \
  "s_add_i32 m0, s59, 25600\n" \
  "s_mov_b32 s58, 3735552\n" \
  "buffer_load_dwordx4 v115, s[12:15], s58 offen lds\n" \
  "s_add_i32 m0, s59, 26624\n" \
  "s_mov_b32 s58, 3801088\n" \
  "buffer_load_dwordx4 v115, s[12:15], s58 offen lds\n" \
  "s_add_i32 m0, s59, 27648\n" \
  "s_mov_b32 s58, 3866624\n" \
  "buffer_load_dwordx4 v115, s[12:15], s58 offen lds\n" \
  "s_add_i32 m0, s59, 28672\n" \
  "s_mov_b32 s58, 3932160\n" \
  "buffer_load_dwordx4 v115, s[12:15], s58 offen lds\n" \
  "s_add_i32 m0, s59, 29696\n" \
  "s_mov_b32 s58, 3997696\n" \
  "buffer_load_dwordx4 v115, s[12:15], s58 offen lds\n" \
  "s_add_i32 m0, s59, 30720\n" \
  "s_mov_b32 s58, 4063232\n" \
  "buffer_load_dwordx4 v115, s[12:15], s58 offen lds\n" \
  "s_add_i32 m0, s59, 31744\n" \
  "s_mov_b32 s58, 4128768\n" \
  "buffer_load_dwordx4 v115, s[12:15], s58 offen lds\n" \
  "s_waitcnt vmcnt(16)\n" \
  "ds_read_b64_tr_b16 a[128:129], v116 offset:0\n" \
  "ds_read_b64_tr_b16 a[130:131], v116 offset:512\n" \
  "ds_read_b64_tr_b16 a[132:133], v116 offset:64\n" \
  "ds_read_b64_tr_b16 a[134:135], v116 offset:576\n" \
  "ds_read_b64_tr_b16 a[136:137], v116 offset:2048\n" \
  "ds_read_b64_tr_b16 a[138:139], v116 offset:2560\n" \
  "ds_read_b64_tr_b16 a[140:141], v116 offset:2112\n" \
  "ds_read_b64_tr_b16 a[142:143], v116 offset:2624\n" \
  "ds_read_b64_tr_b16 a[144:145], v116 offset:4096\n" \
  "ds_read_b64_tr_b16 a[146:147], v116 offset:4608\n" \
  "ds_read_b64_tr_b16 a[148:149], v116 offset:4160\n" \
  "ds_read_b64_tr_b16 a[150:151], v116 offset:4672\n" \
  "ds_read_b64_tr_b16 a[152:153], v116 offset:6144\n" \
  "ds_read_b64_tr_b16 a[154:155], v116 offset:6656\n" \
  "ds_read_b64_tr_b16 a[156:157], v116 offset:6208\n" \
  "ds_read_b64_tr_b16 a[158:159], v116 offset:6720\n" \
  "ds_read_b64_tr_b16 a[160:161], v116 offset:8192\n" \
  "ds_read_b64_tr_b16 a[162:163], v116 offset:8704\n" \
  "ds_read_b64_tr_b16 a[164:165], v116 offset:8256\n" \
  "ds_read_b64_tr_b16 a[166:167], v116 offset:8768\n" \
  "ds_read_b64_tr_b16 a[168:169], v116 offset:10240\n" \
  "ds_read_b64_tr_b16 a[170:171], v116 offset:10752\n" \
  "ds_read_b64_tr_b16 a[172:173], v116 offset:10304\n" \
  "ds_read_b64_tr_b16 a[174:175], v116 offset:10816\n" \
  "ds_read_b64_tr_b16 a[176:177], v116 offset:12288\n" \
  "ds_read_b64_tr_b16 a[178:179], v116 offset:12800\n" \
  "ds_read_b64_tr_b16 a[180:181], v116 offset:12352\n" \
  "ds_read_b64_tr_b16 a[182:183], v116 offset:12864\n" \
  "ds_read_b64_tr_b16 a[184:185], v116 offset:14336\n" \
  "ds_read_b64_tr_b16 a[186:187], v116 offset:14848\n" \
  "ds_read_b64_tr_b16 a[188:189], v116 offset:14400\n" \
  "ds_read_b64_tr_b16 a[190:191], v116 offset:14912\n" \
  "s_waitcnt lgkmcnt(0)\n" \
  "s_add_i32 m0, s59, 0\n" \
  "s_mov_b32 s58, 4194304\n" \
  "buffer_load_dwordx4 v115, s[12:15], s58 offen lds\n" \
  "s_add_i32 m0, s59, 1024\n" \
  "s_mov_b32 s58, 4259840\n" \
  "buffer_load_dwordx4 v115, s[12:15], s58 offen lds\n" \
  "s_add_i32 m0, s59, 2048\n" \
  "s_mov_b32 s58, 4325376\n" \
  "buffer_load_dwordx4 v115, s[12:15], s58 offen lds\n" \
  "s_add_i32 m0, s59, 3072\n" \
  "s_mov_b32 s58, 4390912\n" \
  "buffer_load_dwordx4 v115, s[12:15], s58 offen lds\n" \
  "s_add_i32 m0, s59, 4096\n" \
  "s_mov_b32 s58, 4456448\n" \
  "buffer_load_dwordx4 v115, s[12:15], s58 offen lds\n" \
  "s_add_i32 m0, s59, 5120\n" \
  "s_mov_b32 s58, 4521984\n" \
  "buffer_load_dwordx4 v115, s[12:15], s58 offen lds\n" \
  "s_add_i32 m0, s59, 6144\n" \
  "s_mov_b32 s58, 4587520\n" \
  "buffer_load_dwordx4 v115, s[12:15], s58 offen lds\n" \
  "s_add_i32 m0, s59, 7168\n" \
  "s_mov_b32 s58, 4653056\n" \
  "buffer_load_dwordx4 v115, s[12:15], s58 offen lds\n" \
  "s_add_i32 m0, s59, 8192\n" \
  "s_mov_b32 s58, 4718592\n" \
  "buffer_load_dwordx4 v115, s[12:15], s58 offen lds\n" \
  "s_add_i32 m0, s59, 9216\n" \
  "s_mov_b32 s58, 4784128\n" \
  "buffer_load_dwordx4 v115, s[12:15], s58 offen lds\n" \
  "s_add_i32 m0, s59, 10240\n" \
  "s_mov_b32 s58, 4849664\n" \
  "buffer_load_dwordx4 v115, s[12:15], s58 offen lds\n" \
  "s_add_i32 m0, s59, 11264\n" \
  "s_mov_b32 s58, 4915200\n" \
  "buffer_load_dwordx4 v115, s[12:15], s58 offen lds\n" \
  "s_add_i32 m0, s59, 12288\n" \
  "s_mov_b32 s58, 4980736\n" \
  "buffer_load_dwordx4 v115, s[12:15], s58 offen lds\n" \
  "s_add_i32 m0, s59, 13312\n" \
  "s_mov_b32 s58, 5046272\n" \
  "buffer_load_dwordx4 v115, s[12:15], s58 offen lds\n" \
  "s_add_i32 m0, s59, 14336\n" \
  "s_mov_b32 s58, 5111808\n" \
  "buffer_load_dwordx4 v115, s[12:15], s58 offen lds\n" \
  "s_add_i32 m0, s59, 15360\n" \
  "s_mov_b32 s58, 5177344\n" \
  "buffer_load_dwordx4 v115, s[12:15], s58 offen lds\n" \
  "s_waitcnt vmcnt(16)\n" \
  "ds_read_b64_tr_b16 a[192:193], v116 offset:16384\n" \
  "ds_read_b64_tr_b16 a[194:195], v116 offset:16896\n" \
  "ds_read_b64_tr_b16 a[196:197], v116 offset:16448\n" \
  "ds_read_b64_tr_b16 a[198:199], v116 offset:16960\n" \
  "ds_read_b64_tr_b16 a[200:201], v116 offset:18432\n" \
  "ds_read_b64_tr_b16 a[202:203], v116 offset:18944\n" \
  "ds_read_b64_tr_b16 a[204:205], v116 offset:18496\n" \
  "ds_read_b64_tr_b16 a[206:207], v116 offset:19008\n" \
  "ds_read_b64_tr_b16 a[208:209], v116 offset:20480\n" \
  "ds_read_b64_tr_b16 a[210:211], v116 offset:20992\n" \
  "ds_read_b64_tr_b16 a[212:213], v116 offset:20544\n" \
  "ds_read_b64_tr_b16 a[214:215], v116 offset:21056\n" \
  "ds_read_b64_tr_b16 a[216:217], v116 offset:22528\n" \
  "ds_read_b64_tr_b16 a[218:219], v116 offset:23040\n" \
  "ds_read_b64_tr_b16 a[220:221], v116 offset:22592\n" \
  "ds_read_b64_tr_b16 a[222:223], v116 offset:23104\n" \
  "ds_read_b64_tr_b16 a[224:225], v116 offset:24576\n" \
  "ds_read_b64_tr_b16 a[226:227], v116 offset:25088\n" \
  "ds_read_b64_tr_b16 a[228:229], v116 offset:24640\n" \
  "ds_read_b64_tr_b16 a[230:231], v116 offset:25152\n" \
  "ds_read_b64_tr_b16 a[232:233], v116 offset:26624\n" \
  "ds_read_b64_tr_b16 a[234:235], v116 offset:27136\n" \
  "ds_read_b64_tr_b16 a[236:237], v116 offset:26688\n" \
  "ds_read_b64_tr_b16 a[238:239], v116 offset:27200\n" \
  "ds_read_b64_tr_b16 a[240:241], v116 offset:28672\n" \
  "ds_read_b64_tr_b16 a[242:243], v116 offset:29184\n" \
  "ds_read_b64_tr_b16 a[244:245], v116 offset:28736\n" \
  "ds_read_b64_tr_b16 a[246:247], v116 offset:29248\n" \
  "ds_read_b64_tr_b16 a[248:249], v116 offset:30720\n" \
  "ds_read_b64_tr_b16 a[250:251], v116 offset:31232\n" \
  "ds_read_b64_tr_b16 a[252:253], v116 offset:30784\n" \
  "ds_read_b64_tr_b16 a[254:255], v116 offset:31296\n" \
  "s_waitcnt lgkmcnt(0)\n" \
  "s_waitcnt vmcnt(0)\n" \
  "ds_read_b64_tr_b16 v[192:193], v116 offset:0\n" \
  "ds_read_b64_tr_b16 v[194:195], v116 offset:512\n" \
  "ds_read_b64_tr_b16 v[196:197], v116 offset:64\n" \
  "ds_read_b64_tr_b16 v[198:199], v116 offset:576\n" \
  "ds_read_b64_tr_b16 v[200:201], v116 offset:2048\n" \
  "ds_read_b64_tr_b16 v[202:203], v116 offset:2560\n" \
  "ds_read_b64_tr_b16 v[204:205], v116 offset:2112\n" \
  "ds_read_b64_tr_b16 v[206:207], v116 offset:2624\n" \
  "ds_read_b64_tr_b16 v[208:209], v116 offset:4096\n" \
  "ds_read_b64_tr_b16 v[210:211], v116 offset:4608\n" \
  "ds_read_b64_tr_b16 v[212:213], v116 offset:4160\n" \
  "ds_read_b64_tr_b16 v[214:215], v116 offset:4672\n" \
  "ds_read_b64_tr_b16 v[216:217], v116 offset:6144\n" \
  "ds_read_b64_tr_b16 v[218:219], v116 offset:6656\n" \
  "ds_read_b64_tr_b16 v[220:221], v116 offset:6208\n" \
  "ds_read_b64_tr_b16 v[222:223], v116 offset:6720\n" \
  "ds_read_b64_tr_b16 v[224:225], v116 offset:8192\n" \
  "ds_read_b64_tr_b16 v[226:227], v116 offset:8704\n" \
  "ds_read_b64_tr_b16 v[228:229], v116 offset:8256\n" \
  "ds_read_b64_tr_b16 v[230:231], v116 offset:8768\n" \
  "ds_read_b64_tr_b16 v[232:233], v116 offset:10240\n" \
  "ds_read_b64_tr_b16 v[234:235], v116 offset:10752\n" \
  "ds_read_b64_tr_b16 v[236:237], v116 offset:10304\n" \
  "ds_read_b64_tr_b16 v[238:239], v116 offset:10816\n" \
  "ds_read_b64_tr_b16 v[240:241], v116 offset:12288\n" \
  "ds_read_b64_tr_b16 v[242:243], v116 offset:12800\n" \
  "ds_read_b64_tr_b16 v[244:245], v116 offset:12352\n" \
  "ds_read_b64_tr_b16 v[246:247], v116 offset:12864\n" \
  "ds_read_b64_tr_b16 v[248:249], v116 offset:14336\n" \
  "ds_read_b64_tr_b16 v[250:251], v116 offset:14848\n" \
  "ds_read_b64_tr_b16 v[252:253], v116 offset:14400\n" \
  "ds_read_b64_tr_b16 v[254:255], v116 offset:14912\n" \
  "s_waitcnt lgkmcnt(0)\n" \
  "v_mov_b32 v32, 0x3f803f80\n" \
  "v_mov_b32 v33, 0x3f803f80\n" \
  "v_mov_b32 v34, 0x3f803f80\n" \
  "v_mov_b32 v35, 0x3f803f80\n" \
  "s_nop 1\n" \
  "v_mfma_f32_32x32x16_bf16 v[0:15], v[32:35], a[0:3], 0\n" \
  "v_mfma_f32_32x32x16_bf16 v[16:31], v[32:35], a[4:7], 0\n" \
  "v_mfma_f32_32x32x16_bf16 v[0:15], v[32:35], a[8:11], v[0:15]\n" \
  "v_mfma_f32_32x32x16_bf16 v[16:31], v[32:35], a[12:15], v[16:31]\n" \
  "v_mfma_f32_32x32x16_bf16 v[0:15], v[32:35], a[16:19], v[0:15]\n" \
  "v_mfma_f32_32x32x16_bf16 v[16:31], v[32:35], a[20:23], v[16:31]\n" \
  "v_mfma_f32_32x32x16_bf16 v[0:15], v[32:35], a[24:27], v[0:15]\n" \
  "v_mfma_f32_32x32x16_bf16 v[16:31], v[32:35], a[28:31], v[16:31]\n" \
  "v_mfma_f32_32x32x16_bf16 v[0:15], v[32:35], a[32:35], v[0:15]\n" \
  "v_mfma_f32_32x32x16_bf16 v[16:31], v[32:35], a[36:39], v[16:31]\n" \
  "v_mfma_f32_32x32x16_bf16 v[0:15], v[32:35], a[40:43], v[0:15]\n" \
  "v_mfma_f32_32x32x16_bf16 v[16:31], v[32:35], a[44:47], v[16:31]\n" \
  "v_mfma_f32_32x32x16_bf16 v[0:15], v[32:35], a[48:51], v[0:15]\n" \
  "v_mfma_f32_32x32x16_bf16 v[16:31], v[32:35], a[52:55], v[16:31]\n" \
  "v_mfma_f32_32x32x16_bf16 v[0:15], v[32:35], a[56:59], v[0:15]\n" \
  "v_mfma_f32_32x32x16_bf16 v[16:31], v[32:35], a[60:63], v[16:31]\n" \
  "v_mfma_f32_32x32x16_bf16 v[0:15], v[32:35], a[64:67], v[0:15]\n" \
  "v_mfma_f32_32x32x16_bf16 v[16:31], v[32:35], a[68:71], v[16:31]\n" \
  "v_mfma_f32_32x32x16_bf16 v[0:15], v[32:35], a[72:75], v[0:15]\n" \
  "v_mfma_f32_32x32x16_bf16 v[16:31], v[32:35], a[76:79], v[16:31]\n" \
  "v_mfma_f32_32x32x16_bf16 v[0:15], v[32:35], a[80:83], v[0:15]\n" \
  "v_mfma_f32_32x32x16_bf16 v[16:31], v[32:35], a[84:87], v[16:31]\n" \
  "v_mfma_f32_32x32x16_bf16 v[0:15], v[32:35], a[88:91], v[0:15]\n" \
  "v_mfma_f32_32x32x16_bf16 v[16:31], v[32:35], a[92:95], v[16:31]\n" \
  "v_mfma_f32_32x32x16_bf16 v[0:15], v[32:35], a[96:99], v[0:15]\n" \
  "v_mfma_f32_32x32x16_bf16 v[16:31], v[32:35], a[100:103], v[16:31]\n" \
  "v_mfma_f32_32x32x16_bf16 v[0:15], v[32:35], a[104:107], v[0:15]\n" \
  "v_mfma_f32_32x32x16_bf16 v[16:31], v[32:35], a[108:111], v[16:31]\n" \
  "v_mfma_f32_32x32x16_bf16 v[0:15], v[32:35], a[112:115], v[0:15]\n" \
  "v_mfma_f32_32x32x16_bf16 v[16:31], v[32:35], a[116:119], v[16:31]\n" \
  "v_mfma_f32_32x32x16_bf16 v[0:15], v[32:35], a[120:123], v[0:15]\n" \
  "v_mfma_f32_32x32x16_bf16 v[16:31], v[32:35], a[124:127], v[16:31]\n" \
  "v_mfma_f32_32x32x16_bf16 v[0:15], v[32:35], a[128:131], v[0:15]\n" \
  "v_mfma_f32_32x32x16_bf16 v[16:31], v[32:35], a[132:135], v[16:31]\n" \
  "v_mfma_f32_32x32x16_bf16 v[0:15], v[32:35], a[136:139], v[0:15]\n" \
  "v_mfma_f32_32x32x16_bf16 v[16:31], v[32:35], a[140:143], v[16:31]\n" \
  "v_mfma_f32_32x32x16_bf16 v[0:15], v[32:35], a[144:147], v[0:15]\n" \
  "v_mfma_f32_32x32x16_bf16 v[16:31], v[32:35], a[148:151], v[16:31]\n" \
  "v_mfma_f32_32x32x16_bf16 v[0:15], v[32:35], a[152:155], v[0:15]\n" \
  "v_mfma_f32_32x32x16_bf16 v[16:31], v[32:35], a[156:159], v[16:31]\n" \
  "v_mfma_f32_32x32x16_bf16 v[0:15], v[32:35], a[160:163], v[0:15]\n" \
  "v_mfma_f32_32x32x16_bf16 v[16:31], v[32:35], a[164:167], v[16:31]\n" \
  "v_mfma_f32_32x32x16_bf16 v[0:15], v[32:35], a[168:171], v[0:15]\n" \
  "v_mfma_f32_32x32x16_bf16 v[16:31], v[32:35], a[172:175], v[16:31]\n" \
  "v_mfma_f32_32x32x16_bf16 v[0:15], v[32:35], a[176:179], v[0:15]\n" \
  "v_mfma_f32_32x32x16_bf16 v[16:31], v[32:35], a[180:183], v[16:31]\n" \
  "v_mfma_f32_32x32x16_bf16 v[0:15], v[32:35], a[184:187], v[0:15]\n" \
  "v_mfma_f32_32x32x16_bf16 v[16:31], v[32:35], a[188:191], v[16:31]\n" \
  "v_mfma_f32_32x32x16_bf16 v[0:15], v[32:35], a[192:195], v[0:15]\n" \
  "v_mfma_f32_32x32x16_bf16 v[16:31], v[32:35], a[196:199], v[16:31]\n" \
  "v_mfma_f32_32x32x16_bf16 v[0:15], v[32:35], a[200:203], v[0:15]\n" \
  "v_mfma_f32_32x32x16_bf16 v[16:31], v[32:35], a[204:207], v[16:31]\n" \
  "v_mfma_f32_32x32x16_bf16 v[0:15], v[32:35], a[208:211], v[0:15]\n" \
  "v_mfma_f32_32x32x16_bf16 v[16:31], v[32:35], a[212:215], v[16:31]\n" \
  "v_mfma_f32_32x32x16_bf16 v[0:15], v[32:35], a[216:219], v[0:15]\n" \
  "v_mfma_f32_32x32x16_bf16 v[16:31], v[32:35], a[220:223], v[16:31]\n" \
  "v_mfma_f32_32x32x16_bf16 v[0:15], v[32:35], a[224:227], v[0:15]\n" \
  "v_mfma_f32_32x32x16_bf16 v[16:31], v[32:35], a[228:231], v[16:31]\n" \
  "v_mfma_f32_32x32x16_bf16 v[0:15], v[32:35], a[232:235], v[0:15]\n" \
  "v_mfma_f32_32x32x16_bf16 v[16:31], v[32:35], a[236:239], v[16:31]\n" \
  "v_mfma_f32_32x32x16_bf16 v[0:15], v[32:35], a[240:243], v[0:15]\n" \
  "v_mfma_f32_32x32x16_bf16 v[16:31], v[32:35], a[244:247], v[16:31]\n" \
  "v_mfma_f32_32x32x16_bf16 v[0:15], v[32:35], a[248:251], v[0:15]\n" \
  "v_mfma_f32_32x32x16_bf16 v[16:31], v[32:35], a[252:255], v[16:31]\n" \
  "v_mfma_f32_32x32x16_bf16 v[0:15], v[32:35], v[192:195], v[0:15]\n" \
  "v_mfma_f32_32x32x16_bf16 v[16:31], v[32:35], v[196:199], v[16:31]\n" \
  "v_mfma_f32_32x32x16_bf16 v[0:15], v[32:35], v[200:203], v[0:15]\n" \
  "v_mfma_f32_32x32x16_bf16 v[16:31], v[32:35], v[204:207], v[16:31]\n" \
  "v_mfma_f32_32x32x16_bf16 v[0:15], v[32:35], v[208:211], v[0:15]\n" \
  "v_mfma_f32_32x32x16_bf16 v[16:31], v[32:35], v[212:215], v[16:31]\n" \
  "v_mfma_f32_32x32x16_bf16 v[0:15], v[32:35], v[216:219], v[0:15]\n" \
  "v_mfma_f32_32x32x16_bf16 v[16:31], v[32:35], v[220:223], v[16:31]\n" \
  "v_mfma_f32_32x32x16_bf16 v[0:15], v[32:35], v[224:227], v[0:15]\n" \
  "v_mfma_f32_32x32x16_bf16 v[16:31], v[32:35], v[228:231], v[16:31]\n" \
  "v_mfma_f32_32x32x16_bf16 v[0:15], v[32:35], v[232:235], v[0:15]\n" \
  "v_mfma_f32_32x32x16_bf16 v[16:31], v[32:35], v[236:239], v[16:31]\n" \
  "v_mfma_f32_32x32x16_bf16 v[0:15], v[32:35], v[240:243], v[0:15]\n" \
  "v_mfma_f32_32x32x16_bf16 v[16:31], v[32:35], v[244:247], v[16:31]\n" \
  "v_mfma_f32_32x32x16_bf16 v[0:15], v[32:35], v[248:251], v[0:15]\n" \
  "v_mfma_f32_32x32x16_bf16 v[16:31], v[32:35], v[252:255], v[16:31]\n" \
  "s_nop 4\n" \
  "v_cvt_pk_bf16_f32 v92, v0, 0\n" \
  "v_lshlrev_b32 v93, 16, v92\n" \
  "v_sub_f32 v94, v0, v93\n" \
  "v_cvt_pk_bf16_f32 v188, v93, v94\n" \
  "v_mov_b32 v189, v92\n" \
  "s_nop 1\n" \
  "v_cvt_pk_bf16_f32 v92, v16, 0\n" \
  "v_lshlrev_b32 v93, 16, v92\n" \
  "v_sub_f32 v94, v16, v93\n" \
  "v_cvt_pk_bf16_f32 v190, v93, v94\n" \
  "v_mov_b32 v191, v92\n" \
  "s_mov_b32 s57, 1048576\n" \
  "s_mov_b32 s56, 0\n" \
  "s_mov_b32 s61, 0\n" \
  "s_mov_b32 s62, 0\n" \
  "s_barrier\n" \
  "s_mov_b32 s66, -1\n" \
  "s_mov_b32 s67, 0\n" \
  "s_mov_b32 s65, 0\n" \
  "v_lshrrev_b32 v97, 2, v110\n" \
  "v_add_u32 v97, 0x14000, v97\n" \
  "LSEG_%=:\n" \
  "v_mov_b32 v101, v114\n" \
  "s_cmp_lg_u32 s61, 0\n" \
  "s_cbranch_scc1 LPRE_%=\n" \
  "v_mov_b32 v101, 0x7fff0000\n" \
  "s_waitcnt vmcnt(0)\n" \
  "v_mov_b32 v92, s56\n" \
  "s_mov_b64 exec, 1\n" \
  "buffer_store_dword v92, off, s[4:7], s20 sc1\n" \
  "s_mov_b64 exec, -1\n" \
  "s_add_i32 s60, s56, 1\n" \
  "LSx_%=:\n" \
  "buffer_load_dword v100, v113, s[4:7], 0 offen sc1\n" \
  "s_waitcnt vmcnt(0)\n" \
  "v_cmp_gt_u32 vcc, s60, v100\n" \
  "s_cbranch_vccz LSxx_%=\n" \
  "s_cmp_eq_u32 s57, 0\n" \
  "s_cbranch_scc1 LSxx_%=\n" \
  "s_sub_u32 s57, s57, 1\n" \
  "s_sleep 2\n" \
  "s_branch LSx_%=\n" \
  "LSxx_%=:\n" \
  "s_lshr_b32 s59, s56, 6\n" \
  "s_mul_i32 s59, s59, s22\n" \
  "s_and_b32 s58, s56, 63\n" \
  "s_mul_i32 s63, s58, 1280\n" \
  "s_add_i32 s63, s63, s59\n" \
  "s_add_i32 s63, s63, s18\n" \
  "s_mul_i32 s58, s58, 40960\n" \
  "s_add_i32 s59, s59, s58\n" \
  "s_add_i32 s59, s59, s17\n" \
  "s_lshl_b32 s58, s16, 2\n" \
  "s_add_i32 m0, s58, s62\n" \
  "buffer_load_dwordx2 v[64:65], v112, s[0:3], s63 offen offset:0 sc1\n" \
  "buffer_load_dwordx2 v[66:67], v112, s[0:3], s63 offen offset:256 sc1\n" \
  "buffer_load_dwordx2 v[68:69], v112, s[0:3], s63 offen offset:512 sc1\n" \
  "buffer_load_dwordx4 v110, s[0:3], s59 offen sc1 lds\n" \
  "s_add_i32 m0, m0, 4096\n" \
  "s_add_i32 s59, s59, 4096\n" \
  "buffer_load_dwordx4 v110, s[0:3], s59 offen sc1 lds\n" \
  "s_add_i32 m0, m0, 4096\n" \
  "s_add_i32 s59, s59, 4096\n" \
  "buffer_load_dwordx4 v110, s[0:3], s59 offen sc1 lds\n" \
  "s_add_i32 m0, m0, 4096\n" \
  "s_add_i32 s59, s59, 4096\n" \
  "buffer_load_dwordx4 v110, s[0:3], s59 offen sc1 lds\n" \
  "s_add_i32 m0, m0, 4096\n" \
  "s_add_i32 s59, s59, 4096\n" \
  "buffer_load_dwordx4 v110, s[0:3], s59 offen sc1 lds\n" \
  "s_add_i32 m0, m0, 4096\n" \
  "s_add_i32 s59, s59, 4096\n" \
  "buffer_load_dwordx4 v110, s[0:3], s59 offen sc1 lds\n" \
  "s_add_i32 m0, m0, 4096\n" \
  "s_add_i32 s59, s59, 4096\n" \
  "buffer_load_dwordx4 v110, s[0:3], s59 offen sc1 lds\n" \
  "s_add_i32 m0, m0, 4096\n" \
  "s_add_i32 s59, s59, 4096\n" \
  "buffer_load_dwordx4 v110, s[0:3], s59 offen sc1 lds\n" \
  "s_add_i32 m0, m0, 4096\n" \
  "s_add_i32 s59, s59, 4096\n" \
  "buffer_load_dwordx4 v110, s[0:3], s59 offen sc1 lds\n" \
  "s_add_i32 m0, m0, 4096\n" \
  "s_add_i32 s59, s59, 4096\n" \
  "buffer_load_dwordx4 v110, s[0:3], s59 offen sc1 lds\n" \
  "s_add_i32 m0, m0, 4096\n" \
  "s_add_i32 s59, s59, 4096\n" \
  "s_waitcnt vmcnt(10)\n" \
  "v_cndmask_b32 v68, 0, v68, s[66:67]\n" \
  "v_cndmask_b32 v69, 0, v69, s[66:67]\n" \
  "v_add_f32 v64, v64, v66\n" \
  "v_add_f32 v65, v65, v67\n" \
  "v_add_f32 v64, v64, v68\n" \
  "v_add_f32 v65, v65, v69\n" \
  "v_mov_b32 v66, v64\n" \
  "v_mov_b32 v67, v65\n" \
  "v_mov_b32 v68, 0x3acccccd\n" \
  "v_permlane32_swap_b32 v64, v66\n" \
  "v_permlane32_swap_b32 v65, v67\n" \
  "v_add_f32 v64, v64, v66\n" \
  "v_add_f32 v65, v65, v67\n" \
  "v_mul_f32 v64, 0xbacccccd, v64\n" \
  "v_mul_f32 v66, v64, v64\n" \
  "v_fma_f32 v65, v65, v68, -v66\n" \
  "v_max_f32 v65, 0, v65\n" \
  "v_add_f32 v65, 0x3727c5ac, v65\n" \
  "v_rsq_f32 v65, v65\n" \
  "v_cvt_pk_bf16_f32 v120, v64, v64\n" \
  "v_and_b32 v66, 0xffff0000, v120\n" \
  "v_sub_f32 v66, v64, v66\n" \
  "v_cvt_pk_bf16_f32 v121, v66, 0\n" \
  "v_mul_f32 v122, s23, v65\n" \
  "v_cndmask_b32 v120, 0, v120, s[66:67]\n" \
  "v_cndmask_b32 v121, 0, v121, s[66:67]\n" \
  "s_waitcnt vmcnt(0)\n" \
  "LPRE_%=:\n" \
  "s_waitcnt vmcnt(2)\n" \
  "s_barrier\n" \
  "v_add_u32 v96, s62, v111\n" \
  "ds_read_b128 v[32:35], v96 offset:0\n" \
  "ds_read_b128 v[36:39], v96 offset:1024\n" \
  "ds_read_b128 v[40:43], v96 offset:2048\n" \
  "ds_read_b128 v[44:47], v96 offset:3072\n" \
  "ds_read_b128 v[48:51], v96 offset:4096\n" \
  "ds_read_b128 v[52:55], v96 offset:5120\n" \
  "buffer_load_dword v100, v113, s[4:7], 0 offen sc1\n" \
  "v_mov_b64 v[72:73], v[120:121]\n" \
  "s_and_b32 s58, s56, 63\n" \
  "s_mul_i32 s58, s58, 786432\n" \
  "s_add_i32 s64, s58, s19\n" \
  "s_xor_b32 s62, s62, 40960\n" \
  "v_mfma_f32_32x32x8_bf16 v[0:15], v[72:73], v[188:189], 0\n" \
  "s_waitcnt lgkmcnt(5)\n" \
  "v_mfma_f32_32x32x16_bf16 v[0:15], v[32:35], a[0:3], v[0:15]\n" \
  "ds_read_b128 v[56:59], v96 offset:6144\n" \
  "s_waitcnt lgkmcnt(5)\n" \
  "v_mfma_f32_32x32x16_bf16 v[0:15], v[36:39], a[8:11], v[0:15]\n" \
  "ds_read_b128 v[60:63], v96 offset:7168\n" \
  "v_mul_f32 v16, v16, v76\n" \
  "v_mul_f32 v17, v17, v77\n" \
  "v_mul_f32 v18, v18, v78\n" \
  "v_mul_f32 v19, v19, v79\n" \
  "s_waitcnt lgkmcnt(5)\n" \
  "v_mfma_f32_32x32x16_bf16 v[0:15], v[40:43], a[16:19], v[0:15]\n" \
  "ds_read_b128 v[32:35], v96 offset:8192\n" \
  "v_mul_f32 v24, v24, v84\n" \
  "v_mul_f32 v25, v25, v85\n" \
  "v_mul_f32 v26, v26, v86\n" \
  "v_mul_f32 v27, v27, v87\n" \
  "s_waitcnt lgkmcnt(5)\n" \
  "v_mfma_f32_32x32x16_bf16 v[0:15], v[44:47], a[24:27], v[0:15]\n" \
  "ds_read_b128 v[36:39], v96 offset:9216\n" \
  "v_cvt_pk_bf16_f32 v102, v16, v17\n" \
  "v_cvt_pk_bf16_f32 v103, v18, v19\n" \
  "v_cvt_pk_bf16_f32 v104, v24, v25\n" \
  "v_cvt_pk_bf16_f32 v105, v26, v27\n" \
  "s_waitcnt lgkmcnt(5)\n" \
  "v_mfma_f32_32x32x16_bf16 v[0:15], v[48:51], a[32:35], v[0:15]\n" \
  "ds_read_b128 v[40:43], v96 offset:10240\n" \
  "buffer_store_dwordx4 v[102:105], v101, s[8:11], s65 offen offset:512 sc1\n" \
  "v_mul_f32 v20, v20, v80\n" \
  "v_mul_f32 v21, v21, v81\n" \
  "v_mul_f32 v22, v22, v82\n" \
  "s_waitcnt lgkmcnt(5)\n" \
  "v_mfma_f32_32x32x16_bf16 v[0:15], v[52:55], a[40:43], v[0:15]\n" \
  "ds_read_b128 v[44:47], v96 offset:11264\n" \
  "v_mul_f32 v23, v23, v83\n" \
  "v_mul_f32 v28, v28, v88\n" \
  "v_mul_f32 v29, v29, v89\n" \
  "v_mul_f32 v30, v30, v90\n" \
  "s_waitcnt lgkmcnt(5)\n" \
  "v_mfma_f32_32x32x16_bf16 v[0:15], v[56:59], a[48:51], v[0:15]\n" \
  "ds_read_b128 v[48:51], v96 offset:12288\n" \
  "v_mul_f32 v31, v31, v91\n" \
  "v_cvt_pk_bf16_f32 v106, v20, v21\n" \
  "v_cvt_pk_bf16_f32 v107, v22, v23\n" \
  "v_cvt_pk_bf16_f32 v108, v28, v29\n" \
  "s_waitcnt lgkmcnt(5)\n" \
  "v_mfma_f32_32x32x16_bf16 v[0:15], v[60:63], a[56:59], v[0:15]\n" \
  "ds_read_b128 v[52:55], v96 offset:13312\n" \
  "v_cvt_pk_bf16_f32 v109, v30, v31\n" \
  "buffer_store_dwordx4 v[106:109], v101, s[8:11], s65 offen offset:2560 sc1\n" \
  "s_waitcnt lgkmcnt(5)\n" \
  "v_mfma_f32_32x32x16_bf16 v[0:15], v[32:35], a[64:67], v[0:15]\n" \
  "ds_read_b128 v[56:59], v96 offset:14336\n" \
  "s_waitcnt lgkmcnt(5)\n" \
  "v_mfma_f32_32x32x16_bf16 v[0:15], v[36:39], a[72:75], v[0:15]\n" \
  "ds_read_b128 v[60:63], v96 offset:15360\n" \
  "s_waitcnt lgkmcnt(5)\n" \
  "v_mfma_f32_32x32x16_bf16 v[0:15], v[40:43], a[80:83], v[0:15]\n" \
  "ds_read_b128 v[32:35], v96 offset:16384\n" \
  "s_waitcnt lgkmcnt(5)\n" \
  "v_mfma_f32_32x32x16_bf16 v[0:15], v[44:47], a[88:91], v[0:15]\n" \
  "ds_read_b128 v[36:39], v96 offset:17408\n" \
  "s_waitcnt lgkmcnt(5)\n" \
  "v_mfma_f32_32x32x16_bf16 v[0:15], v[48:51], a[96:99], v[0:15]\n" \
  "ds_read_b128 v[40:43], v96 offset:18432\n" \
  "v_mov_b32 v74, v122\n" \
  "s_waitcnt lgkmcnt(5)\n" \
  "v_mfma_f32_32x32x16_bf16 v[0:15], v[52:55], a[104:107], v[0:15]\n" \
  "ds_read_b128 v[44:47], v96 offset:19456\n" \
  "ds_write_b32 v97, v122\n" \
  "s_waitcnt lgkmcnt(6)\n" \
  "v_mfma_f32_32x32x16_bf16 v[0:15], v[56:59], a[112:115], v[0:15]\n" \
  "ds_read_b128 v[48:51], v96 offset:20480\n" \
  "ds_read_b128 v[76:79], v117 offset:0\n" \
  "s_waitcnt lgkmcnt(7)\n" \
  "v_mfma_f32_32x32x16_bf16 v[0:15], v[60:63], a[120:123], v[0:15]\n" \
  "ds_read_b128 v[52:55], v96 offset:21504\n" \
  "ds_read_b128 v[80:83], v117 offset:32\n" \
  "s_waitcnt lgkmcnt(8)\n" \
  "v_mfma_f32_32x32x16_bf16 v[0:15], v[32:35], a[128:131], v[0:15]\n" \
  "ds_read_b128 v[56:59], v96 offset:22528\n" \
  "ds_read_b128 v[84:87], v117 offset:64\n" \
  "s_waitcnt lgkmcnt(9)\n" \
  "v_mfma_f32_32x32x16_bf16 v[0:15], v[36:39], a[136:139], v[0:15]\n" \
  "ds_read_b128 v[60:63], v96 offset:23552\n" \
  "ds_read_b128 v[88:91], v117 offset:96\n" \
  "s_waitcnt lgkmcnt(10)\n" \
  "v_mfma_f32_32x32x16_bf16 v[0:15], v[40:43], a[144:147], v[0:15]\n" \
  "ds_read_b128 v[32:35], v96 offset:24576\n" \
  "s_waitcnt lgkmcnt(10)\n" \
  "v_mfma_f32_32x32x16_bf16 v[0:15], v[44:47], a[152:155], v[0:15]\n" \
  "ds_read_b128 v[36:39], v96 offset:25600\n" \
  "s_waitcnt lgkmcnt(9)\n" \
  "v_mfma_f32_32x32x16_bf16 v[0:15], v[48:51], a[160:163], v[0:15]\n" \
  "ds_read_b128 v[40:43], v96 offset:26624\n" \
  "s_waitcnt lgkmcnt(8)\n" \
  "v_mfma_f32_32x32x16_bf16 v[0:15], v[52:55], a[168:171], v[0:15]\n" \
  "ds_read_b128 v[44:47], v96 offset:27648\n" \
  "s_waitcnt lgkmcnt(7)\n" \
  "v_mfma_f32_32x32x16_bf16 v[0:15], v[56:59], a[176:179], v[0:15]\n" \
  "ds_read_b128 v[48:51], v96 offset:28672\n" \
  "s_waitcnt lgkmcnt(6)\n" \
  "v_mfma_f32_32x32x16_bf16 v[0:15], v[60:63], a[184:187], v[0:15]\n" \
  "ds_read_b128 v[52:55], v96 offset:29696\n" \
  "s_waitcnt lgkmcnt(5)\n" \
  "v_mfma_f32_32x32x16_bf16 v[0:15], v[32:35], a[192:195], v[0:15]\n" \
  "ds_read_b128 v[56:59], v96 offset:30720\n" \
  "s_waitcnt lgkmcnt(5)\n" \
  "v_mfma_f32_32x32x16_bf16 v[0:15], v[36:39], a[200:203], v[0:15]\n" \
  "ds_read_b128 v[60:63], v96 offset:31744\n" \
  "s_waitcnt lgkmcnt(5)\n" \
  "v_mfma_f32_32x32x16_bf16 v[0:15], v[40:43], a[208:211], v[0:15]\n" \
  "ds_read_b128 v[32:35], v96 offset:32768\n" \
  "s_waitcnt lgkmcnt(5)\n" \
  "v_mfma_f32_32x32x16_bf16 v[0:15], v[44:47], a[216:219], v[0:15]\n" \
  "ds_read_b128 v[36:39], v96 offset:33792\n" \
  "s_waitcnt lgkmcnt(5)\n" \
  "v_mfma_f32_32x32x16_bf16 v[0:15], v[48:51], a[224:227], v[0:15]\n" \
  "ds_read_b128 v[40:43], v96 offset:34816\n" \
  "s_waitcnt vmcnt(0)\n" \
  "v_mov_b32 v92, s56\n" \
  "s_mov_b64 exec, 1\n" \
  "buffer_store_dword v92, off, s[4:7], s20 sc1\n" \
  "s_mov_b64 exec, -1\n" \
  "s_add_i32 s60, s56, 2\n" \
  "v_cmp_gt_u32 vcc, s60, v100\n" \
  "s_waitcnt lgkmcnt(5)\n" \
  "v_mfma_f32_32x32x16_bf16 v[0:15], v[52:55], a[232:235], v[0:15]\n" \
  "ds_read_b128 v[44:47], v96 offset:35840\n" \
  "s_add_i32 s60, s56, 1\n" \
  "s_cmp_eq_u64 vcc, 0\n" \
  "s_cselect_b32 s61, 1, 0\n" \
  "s_cselect_b32 s58, 0, 0x7fff0000\n" \
  "v_or_b32 v93, s58, v110\n" \
  "v_or_b32 v94, s58, v112\n" \
  "s_waitcnt lgkmcnt(5)\n" \
  "v_mfma_f32_32x32x16_bf16 v[0:15], v[56:59], a[240:243], v[0:15]\n" \
  "ds_read_b128 v[48:51], v96 offset:36864\n" \
  "s_lshr_b32 s59, s60, 6\n" \
  "s_mul_i32 s59, s59, s22\n" \
  "s_and_b32 s58, s60, 63\n" \
  "s_mul_i32 s63, s58, 1280\n" \
  "s_add_i32 s63, s63, s59\n" \
  "s_add_i32 s63, s63, s18\n" \
  "s_mul_i32 s58, s58, 40960\n" \
  "s_add_i32 s59, s59, s58\n" \
  "s_add_i32 s59, s59, s17\n" \
  "s_lshl_b32 s58, s16, 2\n" \
  "s_add_i32 m0, s58, s62\n" \
  "s_waitcnt lgkmcnt(5)\n" \
  "v_mfma_f32_32x32x16_bf16 v[0:15], v[60:63], a[248:251], v[0:15]\n" \
  "ds_read_b128 v[52:55], v96 offset:37888\n" \
  "buffer_load_dwordx2 v[64:65], v94, s[0:3], s63 offen offset:0 sc1\n" \
  "s_waitcnt lgkmcnt(5)\n" \
  "v_mfma_f32_32x32x16_bf16 v[0:15], v[32:35], v[192:195], v[0:15]\n" \
  "ds_read_b128 v[56:59], v96 offset:38912\n" \
  "buffer_load_dwordx2 v[66:67], v94, s[0:3], s63 offen offset:256 sc1\n" \
  "s_waitcnt lgkmcnt(5)\n" \
  "v_mfma_f32_32x32x16_bf16 v[0:15], v[36:39], v[200:203], v[0:15]\n" \
  "ds_read_b128 v[60:63], v96 offset:39936\n" \
  "buffer_load_dwordx2 v[68:69], v94, s[0:3], s63 offen offset:512 sc1\n" \
  "s_waitcnt lgkmcnt(5)\n" \
  "v_mfma_f32_32x32x16_bf16 v[0:15], v[40:43], v[208:211], v[0:15]\n" \
  "ds_read_b128 v[32:35], v96 offset:0\n" \
  "buffer_load_dwordx4 v93, s[0:3], s59 offen sc1 lds\n" \
  "s_add_i32 m0, m0, 4096\n" \
  "s_add_i32 s59, s59, 4096\n" \
  "s_waitcnt lgkmcnt(5)\n" \
  "v_mfma_f32_32x32x16_bf16 v[0:15], v[44:47], v[216:219], v[0:15]\n" \
  "ds_read_b128 v[36:39], v96 offset:1024\n" \
  "s_waitcnt lgkmcnt(5)\n" \
  "v_mfma_f32_32x32x16_bf16 v[0:15], v[48:51], v[224:227], v[0:15]\n" \
  "ds_read_b128 v[40:43], v96 offset:2048\n" \
  "s_waitcnt lgkmcnt(5)\n" \
  "v_mfma_f32_32x32x16_bf16 v[0:15], v[52:55], v[232:235], v[0:15]\n" \
  "ds_read_b128 v[44:47], v96 offset:3072\n" \
  "buffer_load_dwordx4 v93, s[0:3], s59 offen sc1 lds\n" \
  "s_add_i32 m0, m0, 4096\n" \
  "s_add_i32 s59, s59, 4096\n" \
  "s_waitcnt lgkmcnt(5)\n" \
  "v_mfma_f32_32x32x16_bf16 v[0:15], v[56:59], v[240:243], v[0:15]\n" \
  "ds_read_b128 v[48:51], v96 offset:4096\n" \
  "s_waitcnt lgkmcnt(5)\n" \
  "v_mfma_f32_32x32x16_bf16 v[0:15], v[60:63], v[248:251], v[0:15]\n" \
  "ds_read_b128 v[52:55], v96 offset:5120\n" \
  "v_mfma_f32_32x32x8_bf16 v[16:31], v[72:73], v[190:191], 0\n" \
  "s_waitcnt lgkmcnt(5)\n" \
  "v_mfma_f32_32x32x16_bf16 v[16:31], v[32:35], a[4:7], v[16:31]\n" \
  "ds_read_b128 v[56:59], v96 offset:6144\n" \
  "buffer_load_dwordx4 v93, s[0:3], s59 offen sc1 lds\n" \
  "s_add_i32 m0, m0, 4096\n" \
  "s_add_i32 s59, s59, 4096\n" \
  "s_waitcnt lgkmcnt(5)\n" \
  "v_mfma_f32_32x32x16_bf16 v[16:31], v[36:39], a[12:15], v[16:31]\n" \
  "ds_read_b128 v[60:63], v96 offset:7168\n" \
  "s_waitcnt lgkmcnt(5)\n" \
  "v_mfma_f32_32x32x16_bf16 v[16:31], v[40:43], a[20:23], v[16:31]\n" \
  "ds_read_b128 v[32:35], v96 offset:8192\n" \
  "s_waitcnt lgkmcnt(5)\n" \
  "v_mfma_f32_32x32x16_bf16 v[16:31], v[44:47], a[28:31], v[16:31]\n" \
  "ds_read_b128 v[36:39], v96 offset:9216\n" \
  "buffer_load_dwordx4 v93, s[0:3], s59 offen sc1 lds\n" \
  "s_add_i32 m0, m0, 4096\n" \
  "s_add_i32 s59, s59, 4096\n" \
  "s_waitcnt lgkmcnt(5)\n" \
  "v_mfma_f32_32x32x16_bf16 v[16:31], v[48:51], a[36:39], v[16:31]\n" \
  "ds_read_b128 v[40:43], v96 offset:10240\n" \
  "s_waitcnt lgkmcnt(5)\n" \
  "v_mfma_f32_32x32x16_bf16 v[16:31], v[52:55], a[44:47], v[16:31]\n" \
  "ds_read_b128 v[44:47], v96 offset:11264\n" \
  "s_waitcnt lgkmcnt(5)\n" \
  "v_mfma_f32_32x32x16_bf16 v[16:31], v[56:59], a[52:55], v[16:31]\n" \
  "ds_read_b128 v[48:51], v96 offset:12288\n" \
  "buffer_load_dwordx4 v93, s[0:3], s59 offen sc1 lds\n" \
  "s_add_i32 m0, m0, 4096\n" \
  "s_add_i32 s59, s59, 4096\n" \
  "s_waitcnt lgkmcnt(5)\n" \
  "v_mfma_f32_32x32x16_bf16 v[16:31], v[60:63], a[60:63], v[16:31]\n" \
  "ds_read_b128 v[52:55], v96 offset:13312\n" \
  "s_waitcnt lgkmcnt(5)\n" \
  "v_mfma_f32_32x32x16_bf16 v[16:31], v[32:35], a[68:71], v[16:31]\n" \
  "ds_read_b128 v[56:59], v96 offset:14336\n" \
  "s_waitcnt lgkmcnt(5)\n" \
  "v_mfma_f32_32x32x16_bf16 v[16:31], v[36:39], a[76:79], v[16:31]\n" \
  "ds_read_b128 v[60:63], v96 offset:15360\n" \
  "buffer_load_dwordx4 v93, s[0:3], s59 offen sc1 lds\n" \
  "s_add_i32 m0, m0, 4096\n" \
  "s_add_i32 s59, s59, 4096\n" \
  "s_waitcnt lgkmcnt(5)\n" \
  "v_mfma_f32_32x32x16_bf16 v[16:31], v[40:43], a[84:87], v[16:31]\n" \
  "ds_read_b128 v[32:35], v96 offset:16384\n" \
  "s_waitcnt lgkmcnt(5)\n" \
  "v_mfma_f32_32x32x16_bf16 v[16:31], v[44:47], a[92:95], v[16:31]\n" \
  "ds_read_b128 v[36:39], v96 offset:17408\n" \
  "s_waitcnt lgkmcnt(5)\n" \
  "v_mfma_f32_32x32x16_bf16 v[16:31], v[48:51], a[100:103], v[16:31]\n" \
  "ds_read_b128 v[40:43], v96 offset:18432\n" \
  "buffer_load_dwordx4 v93, s[0:3], s59 offen sc1 lds\n" \
  "s_add_i32 m0, m0, 4096\n" \
  "s_add_i32 s59, s59, 4096\n" \
  "s_waitcnt lgkmcnt(5)\n" \
  "v_mfma_f32_32x32x16_bf16 v[16:31], v[52:55], a[108:111], v[16:31]\n" \
  "ds_read_b128 v[44:47], v96 offset:19456\n" \
  "s_waitcnt lgkmcnt(5)\n" \
  "v_mfma_f32_32x32x16_bf16 v[16:31], v[56:59], a[116:119], v[16:31]\n" \
  "ds_read_b128 v[48:51], v96 offset:20480\n" \
  "s_waitcnt lgkmcnt(5)\n" \
  "v_mfma_f32_32x32x16_bf16 v[16:31], v[60:63], a[124:127], v[16:31]\n" \
  "ds_read_b128 v[52:55], v96 offset:21504\n" \
  "buffer_load_dwordx4 v93, s[0:3], s59 offen sc1 lds\n" \
  "s_add_i32 m0, m0, 4096\n" \
  "s_add_i32 s59, s59, 4096\n" \
  "s_waitcnt lgkmcnt(5)\n" \
  "v_mfma_f32_32x32x16_bf16 v[16:31], v[32:35], a[132:135], v[16:31]\n" \
  "ds_read_b128 v[56:59], v96 offset:22528\n" \
  "s_waitcnt lgkmcnt(5)\n" \
  "v_mfma_f32_32x32x16_bf16 v[16:31], v[36:39], a[140:143], v[16:31]\n" \
  "ds_read_b128 v[60:63], v96 offset:23552\n" \
  "s_waitcnt lgkmcnt(5)\n" \
  "v_mfma_f32_32x32x16_bf16 v[16:31], v[40:43], a[148:151], v[16:31]\n" \
  "ds_read_b128 v[32:35], v96 offset:24576\n" \
  "buffer_load_dwordx4 v93, s[0:3], s59 offen sc1 lds\n" \
  "s_add_i32 m0, m0, 4096\n" \
  "s_add_i32 s59, s59, 4096\n" \
  "s_waitcnt lgkmcnt(5)\n" \
  "v_mfma_f32_32x32x16_bf16 v[16:31], v[44:47], a[156:159], v[16:31]\n" \
  "ds_read_b128 v[36:39], v96 offset:25600\n" \
  "s_waitcnt lgkmcnt(5)\n" \
  "v_mfma_f32_32x32x16_bf16 v[16:31], v[48:51], a[164:167], v[16:31]\n" \
  "ds_read_b128 v[40:43], v96 offset:26624\n" \
  "s_waitcnt lgkmcnt(5)\n" \
  "v_mfma_f32_32x32x16_bf16 v[16:31], v[52:55], a[172:175], v[16:31]\n" \
  "ds_read_b128 v[44:47], v96 offset:27648\n" \
  "buffer_load_dwordx4 v93, s[0:3], s59 offen sc1 lds\n" \
  "s_add_i32 m0, m0, 4096\n" \
  "s_add_i32 s59, s59, 4096\n" \
  "s_waitcnt lgkmcnt(5)\n" \
  "v_mfma_f32_32x32x16_bf16 v[16:31], v[56:59], a[180:183], v[16:31]\n" \
  "ds_read_b128 v[48:51], v96 offset:28672\n" \
  "v_mul_f32 v0, v0, v76\n" \
  "v_mul_f32 v1, v1, v77\n" \
  "v_mul_f32 v2, v2, v78\n" \
  "v_mul_f32 v3, v3, v79\n" \
  "s_waitcnt lgkmcnt(5)\n" \
  "v_mfma_f32_32x32x16_bf16 v[16:31], v[60:63], a[188:191], v[16:31]\n" \
  "ds_read_b128 v[52:55], v96 offset:29696\n" \
  "v_mul_f32 v8, v8, v84\n" \
  "v_mul_f32 v9, v9, v85\n" \
  "v_mul_f32 v10, v10, v86\n" \
  "v_mul_f32 v11, v11, v87\n" \
  "s_waitcnt lgkmcnt(5)\n" \
  "v_mfma_f32_32x32x16_bf16 v[16:31], v[32:35], a[196:199], v[16:31]\n" \
  "ds_read_b128 v[56:59], v96 offset:30720\n" \
  "v_cvt_pk_bf16_f32 v102, v0, v1\n" \
  "v_cvt_pk_bf16_f32 v103, v2, v3\n" \
  "v_cvt_pk_bf16_f32 v104, v8, v9\n" \
  "v_cvt_pk_bf16_f32 v105, v10, v11\n" \
  "s_waitcnt lgkmcnt(5)\n" \
  "v_mfma_f32_32x32x16_bf16 v[16:31], v[36:39], a[204:207], v[16:31]\n" \
  "ds_read_b128 v[60:63], v96 offset:31744\n" \
  "buffer_store_dwordx4 v[102:105], v114, s[8:11], s64 offen offset:0 sc1\n" \
  "v_mul_f32 v4, v4, v80\n" \
  "v_mul_f32 v5, v5, v81\n" \
  "v_mul_f32 v6, v6, v82\n" \
  "s_waitcnt lgkmcnt(5)\n" \
  "v_mfma_f32_32x32x16_bf16 v[16:31], v[40:43], a[212:215], v[16:31]\n" \
  "ds_read_b128 v[32:35], v96 offset:32768\n" \
  "v_mul_f32 v7, v7, v83\n" \
  "v_mul_f32 v12, v12, v88\n" \
  "v_mul_f32 v13, v13, v89\n" \
  "v_mul_f32 v14, v14, v90\n" \
  "s_waitcnt lgkmcnt(5)\n" \
  "v_mfma_f32_32x32x16_bf16 v[16:31], v[44:47], a[220:223], v[16:31]\n" \
  "ds_read_b128 v[36:39], v96 offset:33792\n" \
  "v_mul_f32 v15, v15, v91\n" \
  "v_cvt_pk_bf16_f32 v106, v4, v5\n" \
  "v_cvt_pk_bf16_f32 v107, v6, v7\n" \
  "v_cvt_pk_bf16_f32 v108, v12, v13\n" \
  "s_waitcnt lgkmcnt(5)\n" \
  "v_mfma_f32_32x32x16_bf16 v[16:31], v[48:51], a[228:231], v[16:31]\n" \
  "ds_read_b128 v[40:43], v96 offset:34816\n" \
  "v_cvt_pk_bf16_f32 v109, v14, v15\n" \
  "buffer_store_dwordx4 v[106:109], v114, s[8:11], s64 offen offset:2048 sc1\n" \
  "s_waitcnt lgkmcnt(5)\n" \
  "v_mfma_f32_32x32x16_bf16 v[16:31], v[52:55], a[236:239], v[16:31]\n" \
  "ds_read_b128 v[44:47], v96 offset:35840\n" \
  "s_waitcnt lgkmcnt(5)\n" \
  "v_mfma_f32_32x32x16_bf16 v[16:31], v[56:59], a[244:247], v[16:31]\n" \
  "ds_read_b128 v[48:51], v96 offset:36864\n" \
  "s_waitcnt vmcnt(12)\n" \
  "v_cndmask_b32 v68, 0, v68, s[66:67]\n" \
  "v_cndmask_b32 v69, 0, v69, s[66:67]\n" \
  "v_add_f32 v64, v64, v66\n" \
  "s_waitcnt lgkmcnt(5)\n" \
  "v_mfma_f32_32x32x16_bf16 v[16:31], v[60:63], a[252:255], v[16:31]\n" \
  "ds_read_b128 v[52:55], v96 offset:37888\n" \
  "v_add_f32 v65, v65, v67\n" \
  "v_add_f32 v64, v64, v68\n" \
  "v_add_f32 v65, v65, v69\n" \
  "v_mov_b32 v66, v64\n" \
  "s_waitcnt lgkmcnt(5)\n" \
  "v_mfma_f32_32x32x16_bf16 v[16:31], v[32:35], v[196:199], v[16:31]\n" \
  "ds_read_b128 v[56:59], v96 offset:38912\n" \
  "v_mov_b32 v67, v65\n" \
  "v_mov_b32 v68, 0x3acccccd\n" \
  "v_permlane32_swap_b32 v64, v66\n" \
  "v_permlane32_swap_b32 v65, v67\n" \
  "s_waitcnt lgkmcnt(5)\n" \
  "v_mfma_f32_32x32x16_bf16 v[16:31], v[36:39], v[204:207], v[16:31]\n" \
  "ds_read_b128 v[60:63], v96 offset:39936\n" \
  "v_add_f32 v64, v64, v66\n" \
  "v_add_f32 v65, v65, v67\n" \
  "v_mul_f32 v64, 0xbacccccd, v64\n" \
  "v_mul_f32 v66, v64, v64\n" \
  "s_waitcnt lgkmcnt(5)\n" \
  "v_mfma_f32_32x32x16_bf16 v[16:31], v[40:43], v[212:215], v[16:31]\n" \
  "v_fma_f32 v65, v65, v68, -v66\n" \
  "v_max_f32 v65, 0, v65\n" \
  "v_add_f32 v65, 0x3727c5ac, v65\n" \
  "v_rsq_f32 v65, v65\n" \
  "s_waitcnt lgkmcnt(4)\n" \
  "v_mfma_f32_32x32x16_bf16 v[16:31], v[44:47], v[220:223], v[16:31]\n" \
  "v_cvt_pk_bf16_f32 v120, v64, v64\n" \
  "v_and_b32 v66, 0xffff0000, v120\n" \
  "v_sub_f32 v66, v64, v66\n" \
  "v_cvt_pk_bf16_f32 v121, v66, 0\n" \
  "s_waitcnt lgkmcnt(3)\n" \
  "v_mfma_f32_32x32x16_bf16 v[16:31], v[48:51], v[228:231], v[16:31]\n" \
  "v_mul_f32 v122, s23, v65\n" \
  "v_cndmask_b32 v120, 0, v120, s[66:67]\n" \
  "v_cndmask_b32 v121, 0, v121, s[66:67]\n" \
  "s_waitcnt lgkmcnt(2)\n" \
  "v_mfma_f32_32x32x16_bf16 v[16:31], v[52:55], v[236:239], v[16:31]\n" \
  "s_waitcnt lgkmcnt(1)\n" \
  "v_mfma_f32_32x32x16_bf16 v[16:31], v[56:59], v[244:247], v[16:31]\n" \
  "s_waitcnt lgkmcnt(0)\n" \
  "v_mfma_f32_32x32x16_bf16 v[16:31], v[60:63], v[252:255], v[16:31]\n" \
  "s_mov_b32 s65, s64\n" \
  "s_add_i32 s56, s56, 1\n" \
  "s_cmp_lg_u32 s61, 0\n" \
  "s_cbranch_scc1 LSEG_%=\n" \
  "s_nop 7\n" \
  "v_mul_f32 v16, v16, v76\n" \
  "v_mul_f32 v17, v17, v77\n" \
  "v_mul_f32 v18, v18, v78\n" \
  "v_mul_f32 v19, v19, v79\n" \
  "v_mul_f32 v24, v24, v84\n" \
  "v_mul_f32 v25, v25, v85\n" \
  "v_mul_f32 v26, v26, v86\n" \
  "v_mul_f32 v27, v27, v87\n" \
  "v_cvt_pk_bf16_f32 v102, v16, v17\n" \
  "v_cvt_pk_bf16_f32 v103, v18, v19\n" \
  "v_cvt_pk_bf16_f32 v104, v24, v25\n" \
  "v_cvt_pk_bf16_f32 v105, v26, v27\n" \
  "buffer_store_dwordx4 v[102:105], v114, s[8:11], s64 offen offset:512 sc1\n" \
  "v_mul_f32 v20, v20, v80\n" \
  "v_mul_f32 v21, v21, v81\n" \
  "v_mul_f32 v22, v22, v82\n" \
  "v_mul_f32 v23, v23, v83\n" \
  "v_mul_f32 v28, v28, v88\n" \
  "v_mul_f32 v29, v29, v89\n" \
  "v_mul_f32 v30, v30, v90\n" \
  "v_mul_f32 v31, v31, v91\n" \
  "v_cvt_pk_bf16_f32 v106, v20, v21\n" \
  "v_cvt_pk_bf16_f32 v107, v22, v23\n" \
  "v_cvt_pk_bf16_f32 v108, v28, v29\n" \
  "v_cvt_pk_bf16_f32 v109, v30, v31\n" \
  "buffer_store_dwordx4 v[106:109], v114, s[8:11], s64 offen offset:2560 sc1\n" \
  "s_cmp_lt_u32 s56, s21\n" \
  "s_cbranch_scc1 LSEG_%=\n" \
  "s_waitcnt vmcnt(0)\n" \
  "v_mov_b32 v92, s56\n" \
  "s_mov_b64 exec, 1\n" \
  "buffer_store_dword v92, off, s[4:7], s20 sc1\n" \
  "s_mov_b64 exec, -1\n" \
  "s_endpgm\n"
#define G5_ASM \
  "v_mbcnt_lo_u32_b32 v0, -1, 0\n" \
  "v_mbcnt_hi_u32_b32 v0, -1, v0\n" \
  "v_lshlrev_b32 v0, 2, v0\n" \
  "v_add_u32 v0, %0, v0\n" \
  "v_add_u32 v0, 146432, v0\n" \
  "ds_read_b32 v0, v0\n" \
  "s_waitcnt lgkmcnt(0)\n" \
  "v_readlane_b32 s0, v0, 0\n" \
  "v_readlane_b32 s1, v0, 1\n" \
  "v_readlane_b32 s2, v0, 2\n" \
  "v_readlane_b32 s3, v0, 3\n" \
  "v_readlane_b32 s4, v0, 4\n" \
  "v_readlane_b32 s5, v0, 5\n" \
  "v_readlane_b32 s6, v0, 6\n" \
  "v_readlane_b32 s7, v0, 7\n" \
  "v_readlane_b32 s8, v0, 8\n" \
  "v_readlane_b32 s9, v0, 9\n" \
  "v_readlane_b32 s10, v0, 10\n" \
  "v_readlane_b32 s11, v0, 11\n" \
  "v_readlane_b32 s12, v0, 12\n" \
  "v_readlane_b32 s13, v0, 13\n" \
  "v_readlane_b32 s14, v0, 14\n" \
  "v_readlane_b32 s15, v0, 15\n" \
  "v_readlane_b32 s16, v0, 16\n" \
  "v_readlane_b32 s17, v0, 17\n" \
  "v_readlane_b32 s18, v0, 18\n" \
  "v_readlane_b32 s19, v0, 19\n" \
  "v_readlane_b32 s20, v0, 20\n" \
  "v_readlane_b32 s21, v0, 21\n" \
  "v_readlane_b32 s22, v0, 22\n" \
  "v_readlane_b32 s23, v0, 23\n" \
  "v_readlane_b32 s24, v0, 24\n" \
  "v_readlane_b32 s25, v0, 25\n" \
  "v_readlane_b32 s26, v0, 26\n" \
  "s_nop 4\n" \
  "v_mbcnt_lo_u32_b32 v0, -1, 0\n" \
  "v_mbcnt_hi_u32_b32 v0, -1, v0\n" \
  "v_lshlrev_b32 v0, 2, v0\n" \
  "v_add_u32 v0, s20, v0\n" \
  "v_add_u32 v0, 131072, v0\n" \
  "ds_read_b32 v84, v0 offset:0\n" \
  "ds_read_b32 v85, v0 offset:1024\n" \
  "ds_read_b32 v86, v0 offset:2048\n" \
  "ds_read_b32 v87, v0 offset:3072\n" \
  "ds_read_b32 v88, v0 offset:4096\n" \
  "ds_read_b32 v89, v0 offset:5120\n" \
  "ds_read_b32 v90, v0 offset:6144\n" \
  "ds_read_b32 v91, v0 offset:7168\n" \
  "ds_read_b32 v92, v0 offset:8192\n" \
  "s_waitcnt lgkmcnt(0)\n" \
  "s_barrier\n" \
  "s_lshl_b32 s59, s20, 7\n" \
  "s_add_i32 m0, s59, 0\n" \
  "v_add_u32 v82, 0, v88\n" \
  "buffer_load_dwordx4 v82, s[12:15], 0 offen lds\n" \
  "s_add_i32 m0, s59, 1024\n" \
  "v_add_u32 v82, 10240, v88\n" \
  "buffer_load_dwordx4 v82, s[12:15], 0 offen lds\n" \
  "s_add_i32 m0, s59, 2048\n" \
  "v_add_u32 v82, 20480, v88\n" \
  "buffer_load_dwordx4 v82, s[12:15], 0 offen lds\n" \
  "s_add_i32 m0, s59, 3072\n" \
  "v_add_u32 v82, 30720, v88\n" \
  "buffer_load_dwordx4 v82, s[12:15], 0 offen lds\n" \
  "s_add_i32 m0, s59, 4096\n" \
  "v_add_u32 v82, 40960, v88\n" \
  "buffer_load_dwordx4 v82, s[12:15], 0 offen lds\n" \
  "s_add_i32 m0, s59, 5120\n" \
  "v_add_u32 v82, 51200, v88\n" \
  "buffer_load_dwordx4 v82, s[12:15], 0 offen lds\n" \
  "s_add_i32 m0, s59, 6144\n" \
  "v_add_u32 v82, 61440, v88\n" \
  "buffer_load_dwordx4 v82, s[12:15], 0 offen lds\n" \
  "s_add_i32 m0, s59, 7168\n" \
  "v_add_u32 v82, 71680, v88\n" \
  "buffer_load_dwordx4 v82, s[12:15], 0 offen lds\n" \
  "s_add_i32 m0, s59, 8192\n" \
  "v_add_u32 v82, 81920, v88\n" \
  "buffer_load_dwordx4 v82, s[12:15], 0 offen lds\n" \
  "s_add_i32 m0, s59, 9216\n" \
  "v_add_u32 v82, 92160, v88\n" \
  "buffer_load_dwordx4 v82, s[12:15], 0 offen lds\n" \
  "s_add_i32 m0, s59, 10240\n" \
  "v_add_u32 v82, 102400, v88\n" \
  "buffer_load_dwordx4 v82, s[12:15], 0 offen lds\n" \
  "s_add_i32 m0, s59, 11264\n" \
  "v_add_u32 v82, 112640, v88\n" \
  "buffer_load_dwordx4 v82, s[12:15], 0 offen lds\n" \
  "s_add_i32 m0, s59, 12288\n" \
  "v_add_u32 v82, 122880, v88\n" \
  "buffer_load_dwordx4 v82, s[12:15], 0 offen lds\n" \
  "s_add_i32 m0, s59, 13312\n" \
  "v_add_u32 v82, 133120, v88\n" \
  "buffer_load_dwordx4 v82, s[12:15], 0 offen lds\n" \
  "s_add_i32 m0, s59, 14336\n" \
  "v_add_u32 v82, 143360, v88\n" \
  "buffer_load_dwordx4 v82, s[12:15], 0 offen lds\n" \
  "s_add_i32 m0, s59, 15360\n" \
  "v_add_u32 v82, 153600, v88\n" \
  "buffer_load_dwordx4 v82, s[12:15], 0 offen lds\n" \
  "s_add_i32 m0, s59, 16384\n" \
  "v_add_u32 v82, 163840, v88\n" \
  "buffer_load_dwordx4 v82, s[12:15], 0 offen lds\n" \
  "s_add_i32 m0, s59, 17408\n" \
  "v_add_u32 v82, 174080, v88\n" \
  "buffer_load_dwordx4 v82, s[12:15], 0 offen lds\n" \
  "s_add_i32 m0, s59, 18432\n" \
  "v_add_u32 v82, 184320, v88\n" \
  "buffer_load_dwordx4 v82, s[12:15], 0 offen lds\n" \
  "s_add_i32 m0, s59, 19456\n" \
  "v_add_u32 v82, 194560, v88\n" \
  "buffer_load_dwordx4 v82, s[12:15], 0 offen lds\n" \
  "s_add_i32 m0, s59, 20480\n" \
  "v_add_u32 v82, 204800, v88\n" \
  "buffer_load_dwordx4 v82, s[12:15], 0 offen lds\n" \
  "s_add_i32 m0, s59, 21504\n" \
  "v_add_u32 v82, 215040, v88\n" \
  "buffer_load_dwordx4 v82, s[12:15], 0 offen lds\n" \
  "s_add_i32 m0, s59, 22528\n" \
  "v_add_u32 v82, 225280, v88\n" \
  "buffer_load_dwordx4 v82, s[12:15], 0 offen lds\n" \
  "s_add_i32 m0, s59, 23552\n" \
  "v_add_u32 v82, 235520, v88\n" \
  "buffer_load_dwordx4 v82, s[12:15], 0 offen lds\n" \
  "s_add_i32 m0, s59, 24576\n" \
  "v_add_u32 v82, 245760, v88\n" \
  "buffer_load_dwordx4 v82, s[12:15], 0 offen lds\n" \
  "s_add_i32 m0, s59, 25600\n" \
  "v_add_u32 v82, 256000, v88\n" \
  "buffer_load_dwordx4 v82, s[12:15], 0 offen lds\n" \
  "s_add_i32 m0, s59, 26624\n" \
  "v_add_u32 v82, 266240, v88\n" \
  "buffer_load_dwordx4 v82, s[12:15], 0 offen lds\n" \
  "s_add_i32 m0, s59, 27648\n" \
  "v_add_u32 v82, 276480, v88\n" \
  "buffer_load_dwordx4 v82, s[12:15], 0 offen lds\n" \
  "s_add_i32 m0, s59, 28672\n" \
  "v_add_u32 v82, 286720, v88\n" \
  "buffer_load_dwordx4 v82, s[12:15], 0 offen lds\n" \
  "s_add_i32 m0, s59, 29696\n" \
  "v_add_u32 v82, 296960, v88\n" \
  "buffer_load_dwordx4 v82, s[12:15], 0 offen lds\n" \
  "s_add_i32 m0, s59, 30720\n" \
  "v_add_u32 v82, 307200, v88\n" \
  "buffer_load_dwordx4 v82, s[12:15], 0 offen lds\n" \
  "s_add_i32 m0, s59, 31744\n" \
  "v_add_u32 v82, 317440, v88\n" \
  "buffer_load_dwordx4 v82, s[12:15], 0 offen lds\n" \
  "s_waitcnt vmcnt(16)\n" \
  "ds_read_b64_tr_b16 a[0:1], v89 offset:0\n" \
  "ds_read_b64_tr_b16 a[2:3], v89 offset:2048\n" \
  "ds_read_b64_tr_b16 a[4:5], v89 offset:32\n" \
  "ds_read_b64_tr_b16 a[6:7], v89 offset:2080\n" \
  "ds_read_b64_tr_b16 a[8:9], v89 offset:64\n" \
  "ds_read_b64_tr_b16 a[10:11], v89 offset:2112\n" \
  "ds_read_b64_tr_b16 a[12:13], v89 offset:4096\n" \
  "ds_read_b64_tr_b16 a[14:15], v89 offset:6144\n" \
  "ds_read_b64_tr_b16 a[16:17], v89 offset:4128\n" \
  "ds_read_b64_tr_b16 a[18:19], v89 offset:6176\n" \
  "ds_read_b64_tr_b16 a[20:21], v89 offset:4160\n" \
  "ds_read_b64_tr_b16 a[22:23], v89 offset:6208\n" \
  "ds_read_b64_tr_b16 a[24:25], v89 offset:8192\n" \
  "ds_read_b64_tr_b16 a[26:27], v89 offset:10240\n" \
  "ds_read_b64_tr_b16 a[28:29], v89 offset:8224\n" \
  "ds_read_b64_tr_b16 a[30:31], v89 offset:10272\n" \
  "ds_read_b64_tr_b16 a[32:33], v89 offset:8256\n" \
  "ds_read_b64_tr_b16 a[34:35], v89 offset:10304\n" \
  "ds_read_b64_tr_b16 a[36:37], v89 offset:12288\n" \
  "ds_read_b64_tr_b16 a[38:39], v89 offset:14336\n" \
  "ds_read_b64_tr_b16 a[40:41], v89 offset:12320\n" \
  "ds_read_b64_tr_b16 a[42:43], v89 offset:14368\n" \
  "ds_read_b64_tr_b16 a[44:45], v89 offset:12352\n" \
  "ds_read_b64_tr_b16 a[46:47], v89 offset:14400\n" \
  "s_waitcnt lgkmcnt(0)\n" \
  "s_add_i32 m0, s59, 0\n" \
  "v_add_u32 v82, 327680, v88\n" \
  "buffer_load_dwordx4 v82, s[12:15], 0 offen lds\n" \
  "s_add_i32 m0, s59, 1024\n" \
  "v_add_u32 v82, 337920, v88\n" \
  "buffer_load_dwordx4 v82, s[12:15], 0 offen lds\n" \
  "s_add_i32 m0, s59, 2048\n" \
  "v_add_u32 v82, 348160, v88\n" \
  "buffer_load_dwordx4 v82, s[12:15], 0 offen lds\n" \
  "s_add_i32 m0, s59, 3072\n" \
  "v_add_u32 v82, 358400, v88\n" \
  "buffer_load_dwordx4 v82, s[12:15], 0 offen lds\n" \
  "s_add_i32 m0, s59, 4096\n" \
  "v_add_u32 v82, 368640, v88\n" \
  "buffer_load_dwordx4 v82, s[12:15], 0 offen lds\n" \
  "s_add_i32 m0, s59, 5120\n" \
  "v_add_u32 v82, 378880, v88\n" \
  "buffer_load_dwordx4 v82, s[12:15], 0 offen lds\n" \
  "s_add_i32 m0, s59, 6144\n" \
  "v_add_u32 v82, 389120, v88\n" \
  "buffer_load_dwordx4 v82, s[12:15], 0 offen lds\n" \
  "s_add_i32 m0, s59, 7168\n" \
  "v_add_u32 v82, 399360, v88\n" \
  "buffer_load_dwordx4 v82, s[12:15], 0 offen lds\n" \
  "s_add_i32 m0, s59, 8192\n" \
  "v_add_u32 v82, 409600, v88\n" \
  "buffer_load_dwordx4 v82, s[12:15], 0 offen lds\n" \
  "s_add_i32 m0, s59, 9216\n" \
  "v_add_u32 v82, 419840, v88\n" \
  "buffer_load_dwordx4 v82, s[12:15], 0 offen lds\n" \
  "s_add_i32 m0, s59, 10240\n" \
  "v_add_u32 v82, 430080, v88\n" \
  "buffer_load_dwordx4 v82, s[12:15], 0 offen lds\n" \
  "s_add_i32 m0, s59, 11264\n" \
  "v_add_u32 v82, 440320, v88\n" \
  "buffer_load_dwordx4 v82, s[12:15], 0 offen lds\n" \
  "s_add_i32 m0, s59, 12288\n" \
  "v_add_u32 v82, 450560, v88\n" \
  "buffer_load_dwordx4 v82, s[12:15], 0 offen lds\n" \
  "s_add_i32 m0, s59, 13312\n" \
  "v_add_u32 v82, 460800, v88\n" \
  "buffer_load_dwordx4 v82, s[12:15], 0 offen lds\n" \
  "s_add_i32 m0, s59, 14336\n" \
  "v_add_u32 v82, 471040, v88\n" \
  "buffer_load_dwordx4 v82, s[12:15], 0 offen lds\n" \
  "s_add_i32 m0, s59, 15360\n" \
  "v_add_u32 v82, 481280, v88\n" \
  "buffer_load_dwordx4 v82, s[12:15], 0 offen lds\n" \
  "s_waitcnt vmcnt(16)\n" \
  "ds_read_b64_tr_b16 a[48:49], v89 offset:16384\n" \
  "ds_read_b64_tr_b16 a[50:51], v89 offset:18432\n" \
  "ds_read_b64_tr_b16 a[52:53], v89 offset:16416\n" \
  "ds_read_b64_tr_b16 a[54:55], v89 offset:18464\n" \
  "ds_read_b64_tr_b16 a[56:57], v89 offset:16448\n" \
  "ds_read_b64_tr_b16 a[58:59], v89 offset:18496\n" \
  "ds_read_b64_tr_b16 a[60:61], v89 offset:20480\n" \
  "ds_read_b64_tr_b16 a[62:63], v89 offset:22528\n" \
  "ds_read_b64_tr_b16 a[64:65], v89 offset:20512\n" \
  "ds_read_b64_tr_b16 a[66:67], v89 offset:22560\n" \
  "ds_read_b64_tr_b16 a[68:69], v89 offset:20544\n" \
  "ds_read_b64_tr_b16 a[70:71], v89 offset:22592\n" \
  "ds_read_b64_tr_b16 a[72:73], v89 offset:24576\n" \
  "ds_read_b64_tr_b16 a[74:75], v89 offset:26624\n" \
  "ds_read_b64_tr_b16 a[76:77], v89 offset:24608\n" \
  "ds_read_b64_tr_b16 a[78:79], v89 offset:26656\n" \
  "ds_read_b64_tr_b16 a[80:81], v89 offset:24640\n" \
  "ds_read_b64_tr_b16 a[82:83], v89 offset:26688\n" \
  "ds_read_b64_tr_b16 a[84:85], v89 offset:28672\n" \
  "ds_read_b64_tr_b16 a[86:87], v89 offset:30720\n" \
  "ds_read_b64_tr_b16 a[88:89], v89 offset:28704\n" \
  "ds_read_b64_tr_b16 a[90:91], v89 offset:30752\n" \
  "ds_read_b64_tr_b16 a[92:93], v89 offset:28736\n" \
  "ds_read_b64_tr_b16 a[94:95], v89 offset:30784\n" \
  "s_waitcnt lgkmcnt(0)\n" \
  "s_add_i32 m0, s59, 16384\n" \
  "v_add_u32 v82, 491520, v88\n" \
  "buffer_load_dwordx4 v82, s[12:15], 0 offen lds\n" \
  "s_add_i32 m0, s59, 17408\n" \
  "v_add_u32 v82, 501760, v88\n" \
  "buffer_load_dwordx4 v82, s[12:15], 0 offen lds\n" \
  "s_add_i32 m0, s59, 18432\n" \
  "v_add_u32 v82, 512000, v88\n" \
  "buffer_load_dwordx4 v82, s[12:15], 0 offen lds\n" \
  "s_add_i32 m0, s59, 19456\n" \
  "v_add_u32 v82, 522240, v88\n" \
  "buffer_load_dwordx4 v82, s[12:15], 0 offen lds\n" \
  "s_add_i32 m0, s59, 20480\n" \
  "v_add_u32 v82, 532480, v88\n" \
  "buffer_load_dwordx4 v82, s[12:15], 0 offen lds\n" \
  "s_add_i32 m0, s59, 21504\n" \
  "v_add_u32 v82, 542720, v88\n" \
  "buffer_load_dwordx4 v82, s[12:15], 0 offen lds\n" \
  "s_add_i32 m0, s59, 22528\n" \
  "v_add_u32 v82, 552960, v88\n" \
  "buffer_load_dwordx4 v82, s[12:15], 0 offen lds\n" \
  "s_add_i32 m0, s59, 23552\n" \
  "v_add_u32 v82, 563200, v88\n" \
  "buffer_load_dwordx4 v82, s[12:15], 0 offen lds\n" \
  "s_add_i32 m0, s59, 24576\n" \
  "v_add_u32 v82, 573440, v88\n" \
  "buffer_load_dwordx4 v82, s[12:15], 0 offen lds\n" \
  "s_add_i32 m0, s59, 25600\n" \
  "v_add_u32 v82, 583680, v88\n" \
  "buffer_load_dwordx4 v82, s[12:15], 0 offen lds\n" \
  "s_add_i32 m0, s59, 26624\n" \
  "v_add_u32 v82, 593920, v88\n" \
  "buffer_load_dwordx4 v82, s[12:15], 0 offen lds\n" \
  "s_add_i32 m0, s59, 27648\n" \
  "v_add_u32 v82, 604160, v88\n" \
  "buffer_load_dwordx4 v82, s[12:15], 0 offen lds\n" \
  "s_add_i32 m0, s59, 28672\n" \
  "v_add_u32 v82, 614400, v88\n" \
  "buffer_load_dwordx4 v82, s[12:15], 0 offen lds\n" \
  "s_add_i32 m0, s59, 29696\n" \
  "v_add_u32 v82, 624640, v88\n" \
  "buffer_load_dwordx4 v82, s[12:15], 0 offen lds\n" \
  "s_add_i32 m0, s59, 30720\n" \
  "v_add_u32 v82, 634880, v88\n" \
  "buffer_load_dwordx4 v82, s[12:15], 0 offen lds\n" \
  "s_add_i32 m0, s59, 31744\n" \
  "v_add_u32 v82, 645120, v88\n" \
  "buffer_load_dwordx4 v82, s[12:15], 0 offen lds\n" \
  "s_waitcnt vmcnt(16)\n" \
  "ds_read_b64_tr_b16 a[96:97], v89 offset:0\n" \
  "ds_read_b64_tr_b16 a[98:99], v89 offset:2048\n" \
  "ds_read_b64_tr_b16 a[100:101], v89 offset:32\n" \
  "ds_read_b64_tr_b16 a[102:103], v89 offset:2080\n" \
  "ds_read_b64_tr_b16 a[104:105], v89 offset:64\n" \
  "ds_read_b64_tr_b16 a[106:107], v89 offset:2112\n" \
  "ds_read_b64_tr_b16 a[108:109], v89 offset:4096\n" \
  "ds_read_b64_tr_b16 a[110:111], v89 offset:6144\n" \
  "ds_read_b64_tr_b16 a[112:113], v89 offset:4128\n" \
  "ds_read_b64_tr_b16 a[114:115], v89 offset:6176\n" \
  "ds_read_b64_tr_b16 a[116:117], v89 offset:4160\n" \
  "ds_read_b64_tr_b16 a[118:119], v89 offset:6208\n" \
  "ds_read_b64_tr_b16 a[120:121], v89 offset:8192\n" \
  "ds_read_b64_tr_b16 a[122:123], v89 offset:10240\n" \
  "ds_read_b64_tr_b16 a[124:125], v89 offset:8224\n" \
  "ds_read_b64_tr_b16 a[126:127], v89 offset:10272\n" \
  "ds_read_b64_tr_b16 a[128:129], v89 offset:8256\n" \
  "ds_read_b64_tr_b16 a[130:131], v89 offset:10304\n" \
  "ds_read_b64_tr_b16 a[132:133], v89 offset:12288\n" \
  "ds_read_b64_tr_b16 a[134:135], v89 offset:14336\n" \
  "ds_read_b64_tr_b16 a[136:137], v89 offset:12320\n" \
  "ds_read_b64_tr_b16 a[138:139], v89 offset:14368\n" \
  "ds_read_b64_tr_b16 a[140:141], v89 offset:12352\n" \
  "ds_read_b64_tr_b16 a[142:143], v89 offset:14400\n" \
  "s_waitcnt lgkmcnt(0)\n" \
  "s_add_i32 m0, s59, 0\n" \
  "v_add_u32 v82, 655360, v88\n" \
  "buffer_load_dwordx4 v82, s[12:15], 0 offen lds\n" \
  "s_add_i32 m0, s59, 1024\n" \
  "v_add_u32 v82, 665600, v88\n" \
  "buffer_load_dwordx4 v82, s[12:15], 0 offen lds\n" \
  "s_add_i32 m0, s59, 2048\n" \
  "v_add_u32 v82, 675840, v88\n" \
  "buffer_load_dwordx4 v82, s[12:15], 0 offen lds\n" \
  "s_add_i32 m0, s59, 3072\n" \
  "v_add_u32 v82, 686080, v88\n" \
  "buffer_load_dwordx4 v82, s[12:15], 0 offen lds\n" \
  "s_add_i32 m0, s59, 4096\n" \
  "v_add_u32 v82, 696320, v88\n" \
  "buffer_load_dwordx4 v82, s[12:15], 0 offen lds\n" \
  "s_add_i32 m0, s59, 5120\n" \
  "v_add_u32 v82, 706560, v88\n" \
  "buffer_load_dwordx4 v82, s[12:15], 0 offen lds\n" \
  "s_add_i32 m0, s59, 6144\n" \
  "v_add_u32 v82, 716800, v88\n" \
  "buffer_load_dwordx4 v82, s[12:15], 0 offen lds\n" \
  "s_add_i32 m0, s59, 7168\n" \
  "v_add_u32 v82, 727040, v88\n" \
  "buffer_load_dwordx4 v82, s[12:15], 0 offen lds\n" \
  "s_add_i32 m0, s59, 8192\n" \
  "v_add_u32 v82, 737280, v88\n" \
  "buffer_load_dwordx4 v82, s[12:15], 0 offen lds\n" \
  "s_add_i32 m0, s59, 9216\n" \
  "v_add_u32 v82, 747520, v88\n" \
  "buffer_load_dwordx4 v82, s[12:15], 0 offen lds\n" \
  "s_add_i32 m0, s59, 10240\n" \
  "v_add_u32 v82, 757760, v88\n" \
  "buffer_load_dwordx4 v82, s[12:15], 0 offen lds\n" \
  "s_add_i32 m0, s59, 11264\n" \
  "v_add_u32 v82, 768000, v88\n" \
  "buffer_load_dwordx4 v82, s[12:15], 0 offen lds\n" \
  "s_add_i32 m0, s59, 12288\n" \
  "v_add_u32 v82, 778240, v88\n" \
  "buffer_load_dwordx4 v82, s[12:15], 0 offen lds\n" \
  "s_add_i32 m0, s59, 13312\n" \
  "v_add_u32 v82, 788480, v88\n" \
  "buffer_load_dwordx4 v82, s[12:15], 0 offen lds\n" \
  "s_add_i32 m0, s59, 14336\n" \
  "v_add_u32 v82, 798720, v88\n" \
  "buffer_load_dwordx4 v82, s[12:15], 0 offen lds\n" \
  "s_add_i32 m0, s59, 15360\n" \
  "v_add_u32 v82, 808960, v88\n" \
  "buffer_load_dwordx4 v82, s[12:15], 0 offen lds\n" \
  "s_waitcnt vmcnt(16)\n" \
  "ds_read_b64_tr_b16 a[144:145], v89 offset:16384\n" \
  "ds_read_b64_tr_b16 a[146:147], v89 offset:18432\n" \
  "ds_read_b64_tr_b16 a[148:149], v89 offset:16416\n" \
  "ds_read_b64_tr_b16 a[150:151], v89 offset:18464\n" \
  "ds_read_b64_tr_b16 a[152:153], v89 offset:16448\n" \
  "ds_read_b64_tr_b16 a[154:155], v89 offset:18496\n" \
  "ds_read_b64_tr_b16 a[156:157], v89 offset:20480\n" \
  "ds_read_b64_tr_b16 a[158:159], v89 offset:22528\n" \
  "ds_read_b64_tr_b16 a[160:161], v89 offset:20512\n" \
  "ds_read_b64_tr_b16 a[162:163], v89 offset:22560\n" \
  "ds_read_b64_tr_b16 a[164:165], v89 offset:20544\n" \
  "ds_read_b64_tr_b16 a[166:167], v89 offset:22592\n" \
  "ds_read_b64_tr_b16 a[168:169], v89 offset:24576\n" \
  "ds_read_b64_tr_b16 a[170:171], v89 offset:26624\n" \
  "ds_read_b64_tr_b16 a[172:173], v89 offset:24608\n" \
  "ds_read_b64_tr_b16 a[174:175], v89 offset:26656\n" \
  "ds_read_b64_tr_b16 a[176:177], v89 offset:24640\n" \
  "ds_read_b64_tr_b16 a[178:179], v89 offset:26688\n" \
  "ds_read_b64_tr_b16 a[180:181], v89 offset:28672\n" \
  "ds_read_b64_tr_b16 a[182:183], v89 offset:30720\n" \
  "ds_read_b64_tr_b16 a[184:185], v89 offset:28704\n" \
  "ds_read_b64_tr_b16 a[186:187], v89 offset:30752\n" \
  "ds_read_b64_tr_b16 a[188:189], v89 offset:28736\n" \
  "ds_read_b64_tr_b16 a[190:191], v89 offset:30784\n" \
  "s_waitcnt lgkmcnt(0)\n" \
  "s_add_i32 m0, s59, 16384\n" \
  "v_add_u32 v82, 819200, v88\n" \
  "buffer_load_dwordx4 v82, s[12:15], 0 offen lds\n" \
  "s_add_i32 m0, s59, 17408\n" \
  "v_add_u32 v82, 829440, v88\n" \
  "buffer_load_dwordx4 v82, s[12:15], 0 offen lds\n" \
  "s_add_i32 m0, s59, 18432\n" \
  "v_add_u32 v82, 839680, v88\n" \
  "buffer_load_dwordx4 v82, s[12:15], 0 offen lds\n" \
  "s_add_i32 m0, s59, 19456\n" \
  "v_add_u32 v82, 849920, v88\n" \
  "buffer_load_dwordx4 v82, s[12:15], 0 offen lds\n" \
  "s_add_i32 m0, s59, 20480\n" \
  "v_add_u32 v82, 860160, v88\n" \
  "buffer_load_dwordx4 v82, s[12:15], 0 offen lds\n" \
  "s_add_i32 m0, s59, 21504\n" \
  "v_add_u32 v82, 870400, v88\n" \
  "buffer_load_dwordx4 v82, s[12:15], 0 offen lds\n" \
  "s_add_i32 m0, s59, 22528\n" \
  "v_add_u32 v82, 880640, v88\n" \
  "buffer_load_dwordx4 v82, s[12:15], 0 offen lds\n" \
  "s_add_i32 m0, s59, 23552\n" \
  "v_add_u32 v82, 890880, v88\n" \
  "buffer_load_dwordx4 v82, s[12:15], 0 offen lds\n" \
  "s_add_i32 m0, s59, 24576\n" \
  "v_add_u32 v82, 901120, v88\n" \
  "buffer_load_dwordx4 v82, s[12:15], 0 offen lds\n" \
  "s_add_i32 m0, s59, 25600\n" \
  "v_add_u32 v82, 911360, v88\n" \
  "buffer_load_dwordx4 v82, s[12:15], 0 offen lds\n" \
  "s_add_i32 m0, s59, 26624\n" \
  "v_add_u32 v82, 921600, v88\n" \
  "buffer_load_dwordx4 v82, s[12:15], 0 offen lds\n" \
  "s_add_i32 m0, s59, 27648\n" \
  "v_add_u32 v82, 931840, v88\n" \
  "buffer_load_dwordx4 v82, s[12:15], 0 offen lds\n" \
  "s_add_i32 m0, s59, 28672\n" \
  "v_add_u32 v82, 942080, v88\n" \
  "buffer_load_dwordx4 v82, s[12:15], 0 offen lds\n" \
  "s_add_i32 m0, s59, 29696\n" \
  "v_add_u32 v82, 952320, v88\n" \
  "buffer_load_dwordx4 v82, s[12:15], 0 offen lds\n" \
  "s_add_i32 m0, s59, 30720\n" \
  "v_add_u32 v82, 962560, v88\n" \
  "buffer_load_dwordx4 v82, s[12:15], 0 offen lds\n" \
  "s_add_i32 m0, s59, 31744\n" \
  "v_add_u32 v82, 972800, v88\n" \
  "buffer_load_dwordx4 v82, s[12:15], 0 offen lds\n" \
  "s_waitcnt vmcnt(16)\n" \
  "ds_read_b64_tr_b16 a[192:193], v89 offset:0\n" \
  "ds_read_b64_tr_b16 a[194:195], v89 offset:2048\n" \
  "ds_read_b64_tr_b16 a[196:197], v89 offset:32\n" \
  "ds_read_b64_tr_b16 a[198:199], v89 offset:2080\n" \
  "ds_read_b64_tr_b16 a[200:201], v89 offset:64\n" \
  "ds_read_b64_tr_b16 a[202:203], v89 offset:2112\n" \
  "ds_read_b64_tr_b16 a[204:205], v89 offset:4096\n" \
  "ds_read_b64_tr_b16 a[206:207], v89 offset:6144\n" \
  "ds_read_b64_tr_b16 a[208:209], v89 offset:4128\n" \
  "ds_read_b64_tr_b16 a[210:211], v89 offset:6176\n" \
  "ds_read_b64_tr_b16 a[212:213], v89 offset:4160\n" \
  "ds_read_b64_tr_b16 a[214:215], v89 offset:6208\n" \
  "ds_read_b64_tr_b16 a[216:217], v89 offset:8192\n" \
  "ds_read_b64_tr_b16 a[218:219], v89 offset:10240\n" \
  "ds_read_b64_tr_b16 a[220:221], v89 offset:8224\n" \
  "ds_read_b64_tr_b16 a[222:223], v89 offset:10272\n" \
  "ds_read_b64_tr_b16 a[224:225], v89 offset:8256\n" \
  "ds_read_b64_tr_b16 a[226:227], v89 offset:10304\n" \
  "ds_read_b64_tr_b16 a[228:229], v89 offset:12288\n" \
  "ds_read_b64_tr_b16 a[230:231], v89 offset:14336\n" \
  "ds_read_b64_tr_b16 a[232:233], v89 offset:12320\n" \
  "ds_read_b64_tr_b16 a[234:235], v89 offset:14368\n" \
  "ds_read_b64_tr_b16 a[236:237], v89 offset:12352\n" \
  "ds_read_b64_tr_b16 a[238:239], v89 offset:14400\n" \
  "s_waitcnt lgkmcnt(0)\n" \
  "s_add_i32 m0, s59, 0\n" \
  "v_add_u32 v82, 983040, v88\n" \
  "buffer_load_dwordx4 v82, s[12:15], 0 offen lds\n" \
  "s_add_i32 m0, s59, 1024\n" \
  "v_add_u32 v82, 993280, v88\n" \
  "buffer_load_dwordx4 v82, s[12:15], 0 offen lds\n" \
  "s_add_i32 m0, s59, 2048\n" \
  "v_add_u32 v82, 1003520, v88\n" \
  "buffer_load_dwordx4 v82, s[12:15], 0 offen lds\n" \
  "s_add_i32 m0, s59, 3072\n" \
  "v_add_u32 v82, 1013760, v88\n" \
  "buffer_load_dwordx4 v82, s[12:15], 0 offen lds\n" \
  "s_add_i32 m0, s59, 4096\n" \
  "v_add_u32 v82, 1024000, v88\n" \
  "buffer_load_dwordx4 v82, s[12:15], 0 offen lds\n" \
  "s_add_i32 m0, s59, 5120\n" \
  "v_add_u32 v82, 1034240, v88\n" \
  "buffer_load_dwordx4 v82, s[12:15], 0 offen lds\n" \
  "s_add_i32 m0, s59, 6144\n" \
  "v_add_u32 v82, 1044480, v88\n" \
  "buffer_load_dwordx4 v82, s[12:15], 0 offen lds\n" \
  "s_add_i32 m0, s59, 7168\n" \
  "v_add_u32 v82, 1054720, v88\n" \
  "buffer_load_dwordx4 v82, s[12:15], 0 offen lds\n" \
  "s_add_i32 m0, s59, 8192\n" \
  "v_add_u32 v82, 1064960, v88\n" \
  "buffer_load_dwordx4 v82, s[12:15], 0 offen lds\n" \
  "s_add_i32 m0, s59, 9216\n" \
  "v_add_u32 v82, 1075200, v88\n" \
  "buffer_load_dwordx4 v82, s[12:15], 0 offen lds\n" \
  "s_add_i32 m0, s59, 10240\n" \
  "v_add_u32 v82, 1085440, v88\n" \
  "buffer_load_dwordx4 v82, s[12:15], 0 offen lds\n" \
  "s_add_i32 m0, s59, 11264\n" \
  "v_add_u32 v82, 1095680, v88\n" \
  "buffer_load_dwordx4 v82, s[12:15], 0 offen lds\n" \
  "s_add_i32 m0, s59, 12288\n" \
  "v_add_u32 v82, 1105920, v88\n" \
  "buffer_load_dwordx4 v82, s[12:15], 0 offen lds\n" \
  "s_add_i32 m0, s59, 13312\n" \
  "v_add_u32 v82, 1116160, v88\n" \
  "buffer_load_dwordx4 v82, s[12:15], 0 offen lds\n" \
  "s_add_i32 m0, s59, 14336\n" \
  "v_add_u32 v82, 1126400, v88\n" \
  "buffer_load_dwordx4 v82, s[12:15], 0 offen lds\n" \
  "s_add_i32 m0, s59, 15360\n" \
  "v_add_u32 v82, 1136640, v88\n" \
  "buffer_load_dwordx4 v82, s[12:15], 0 offen lds\n" \
  "s_waitcnt vmcnt(16)\n" \
  "ds_read_b64_tr_b16 a[240:241], v89 offset:16384\n" \
  "ds_read_b64_tr_b16 a[242:243], v89 offset:18432\n" \
  "ds_read_b64_tr_b16 a[244:245], v89 offset:16416\n" \
  "ds_read_b64_tr_b16 a[246:247], v89 offset:18464\n" \
  "ds_read_b64_tr_b16 a[248:249], v89 offset:16448\n" \
  "ds_read_b64_tr_b16 a[250:251], v89 offset:18496\n" \
  "ds_read_b64_tr_b16 a[252:253], v89 offset:20480\n" \
  "ds_read_b64_tr_b16 a[254:255], v89 offset:22528\n" \
  "ds_read_b64_tr_b16 v[128:129], v89 offset:20512\n" \
  "ds_read_b64_tr_b16 v[130:131], v89 offset:22560\n" \
  "ds_read_b64_tr_b16 v[132:133], v89 offset:20544\n" \
  "ds_read_b64_tr_b16 v[134:135], v89 offset:22592\n" \
  "ds_read_b64_tr_b16 v[136:137], v89 offset:24576\n" \
  "ds_read_b64_tr_b16 v[138:139], v89 offset:26624\n" \
  "ds_read_b64_tr_b16 v[140:141], v89 offset:24608\n" \
  "ds_read_b64_tr_b16 v[142:143], v89 offset:26656\n" \
  "ds_read_b64_tr_b16 v[144:145], v89 offset:24640\n" \
  "ds_read_b64_tr_b16 v[146:147], v89 offset:26688\n" \
  "ds_read_b64_tr_b16 v[148:149], v89 offset:28672\n" \
  "ds_read_b64_tr_b16 v[150:151], v89 offset:30720\n" \
  "ds_read_b64_tr_b16 v[152:153], v89 offset:28704\n" \
  "ds_read_b64_tr_b16 v[154:155], v89 offset:30752\n" \
  "ds_read_b64_tr_b16 v[156:157], v89 offset:28736\n" \
  "ds_read_b64_tr_b16 v[158:159], v89 offset:30784\n" \
  "s_waitcnt lgkmcnt(0)\n" \
  "s_add_i32 m0, s59, 16384\n" \
  "v_add_u32 v82, 1146880, v88\n" \
  "buffer_load_dwordx4 v82, s[12:15], 0 offen lds\n" \
  "s_add_i32 m0, s59, 17408\n" \
  "v_add_u32 v82, 1157120, v88\n" \
  "buffer_load_dwordx4 v82, s[12:15], 0 offen lds\n" \
  "s_add_i32 m0, s59, 18432\n" \
  "v_add_u32 v82, 1167360, v88\n" \
  "buffer_load_dwordx4 v82, s[12:15], 0 offen lds\n" \
  "s_add_i32 m0, s59, 19456\n" \
  "v_add_u32 v82, 1177600, v88\n" \
  "buffer_load_dwordx4 v82, s[12:15], 0 offen lds\n" \
  "s_add_i32 m0, s59, 20480\n" \
  "v_add_u32 v82, 1187840, v88\n" \
  "buffer_load_dwordx4 v82, s[12:15], 0 offen lds\n" \
  "s_add_i32 m0, s59, 21504\n" \
  "v_add_u32 v82, 1198080, v88\n" \
  "buffer_load_dwordx4 v82, s[12:15], 0 offen lds\n" \
  "s_add_i32 m0, s59, 22528\n" \
  "v_add_u32 v82, 1208320, v88\n" \
  "buffer_load_dwordx4 v82, s[12:15], 0 offen lds\n" \
  "s_add_i32 m0, s59, 23552\n" \
  "v_add_u32 v82, 1218560, v88\n" \
  "buffer_load_dwordx4 v82, s[12:15], 0 offen lds\n" \
  "s_add_i32 m0, s59, 24576\n" \
  "v_add_u32 v82, 1228800, v88\n" \
  "buffer_load_dwordx4 v82, s[12:15], 0 offen lds\n" \
  "s_add_i32 m0, s59, 25600\n" \
  "v_add_u32 v82, 1239040, v88\n" \
  "buffer_load_dwordx4 v82, s[12:15], 0 offen lds\n" \
  "s_add_i32 m0, s59, 26624\n" \
  "v_add_u32 v82, 1249280, v88\n" \
  "buffer_load_dwordx4 v82, s[12:15], 0 offen lds\n" \
  "s_add_i32 m0, s59, 27648\n" \
  "v_add_u32 v82, 1259520, v88\n" \
  "buffer_load_dwordx4 v82, s[12:15], 0 offen lds\n" \
  "s_add_i32 m0, s59, 28672\n" \
  "v_add_u32 v82, 1269760, v88\n" \
  "buffer_load_dwordx4 v82, s[12:15], 0 offen lds\n" \
  "s_add_i32 m0, s59, 29696\n" \
  "v_add_u32 v82, 1280000, v88\n" \
  "buffer_load_dwordx4 v82, s[12:15], 0 offen lds\n" \
  "s_add_i32 m0, s59, 30720\n" \
  "v_add_u32 v82, 1290240, v88\n" \
  "buffer_load_dwordx4 v82, s[12:15], 0 offen lds\n" \
  "s_add_i32 m0, s59, 31744\n" \
  "v_add_u32 v82, 1300480, v88\n" \
  "buffer_load_dwordx4 v82, s[12:15], 0 offen lds\n" \
  "s_waitcnt vmcnt(16)\n" \
  "ds_read_b64_tr_b16 v[160:161], v89 offset:0\n" \
  "ds_read_b64_tr_b16 v[162:163], v89 offset:2048\n" \
  "ds_read_b64_tr_b16 v[164:165], v89 offset:32\n" \
  "ds_read_b64_tr_b16 v[166:167], v89 offset:2080\n" \
  "ds_read_b64_tr_b16 v[168:169], v89 offset:64\n" \
  "ds_read_b64_tr_b16 v[170:171], v89 offset:2112\n" \
  "ds_read_b64_tr_b16 v[172:173], v89 offset:4096\n" \
  "ds_read_b64_tr_b16 v[174:175], v89 offset:6144\n" \
  "ds_read_b64_tr_b16 v[176:177], v89 offset:4128\n" \
  "ds_read_b64_tr_b16 v[178:179], v89 offset:6176\n" \
  "ds_read_b64_tr_b16 v[180:181], v89 offset:4160\n" \
  "ds_read_b64_tr_b16 v[182:183], v89 offset:6208\n" \
  "ds_read_b64_tr_b16 v[184:185], v89 offset:8192\n" \
  "ds_read_b64_tr_b16 v[186:187], v89 offset:10240\n" \
  "ds_read_b64_tr_b16 v[188:189], v89 offset:8224\n" \
  "ds_read_b64_tr_b16 v[190:191], v89 offset:10272\n" \
  "ds_read_b64_tr_b16 v[192:193], v89 offset:8256\n" \
  "ds_read_b64_tr_b16 v[194:195], v89 offset:10304\n" \
  "ds_read_b64_tr_b16 v[196:197], v89 offset:12288\n" \
  "ds_read_b64_tr_b16 v[198:199], v89 offset:14336\n" \
  "ds_read_b64_tr_b16 v[200:201], v89 offset:12320\n" \
  "ds_read_b64_tr_b16 v[202:203], v89 offset:14368\n" \
  "ds_read_b64_tr_b16 v[204:205], v89 offset:12352\n" \
  "ds_read_b64_tr_b16 v[206:207], v89 offset:14400\n" \
  "s_waitcnt lgkmcnt(0)\n" \
  "s_waitcnt vmcnt(0)\n" \
  "ds_read_b64_tr_b16 v[208:209], v89 offset:16384\n" \
  "ds_read_b64_tr_b16 v[210:211], v89 offset:18432\n" \
  "ds_read_b64_tr_b16 v[212:213], v89 offset:16416\n" \
  "ds_read_b64_tr_b16 v[214:215], v89 offset:18464\n" \
  "ds_read_b64_tr_b16 v[216:217], v89 offset:16448\n" \
  "ds_read_b64_tr_b16 v[218:219], v89 offset:18496\n" \
  "ds_read_b64_tr_b16 v[220:221], v89 offset:20480\n" \
  "ds_read_b64_tr_b16 v[222:223], v89 offset:22528\n" \
  "ds_read_b64_tr_b16 v[224:225], v89 offset:20512\n" \
  "ds_read_b64_tr_b16 v[226:227], v89 offset:22560\n" \
  "ds_read_b64_tr_b16 v[228:229], v89 offset:20544\n" \
  "ds_read_b64_tr_b16 v[230:231], v89 offset:22592\n" \
  "ds_read_b64_tr_b16 v[232:233], v89 offset:24576\n" \
  "ds_read_b64_tr_b16 v[234:235], v89 offset:26624\n" \
  "ds_read_b64_tr_b16 v[236:237], v89 offset:24608\n" \
  "ds_read_b64_tr_b16 v[238:239], v89 offset:26656\n" \
  "ds_read_b64_tr_b16 v[240:241], v89 offset:24640\n" \
  "ds_read_b64_tr_b16 v[242:243], v89 offset:26688\n" \
  "ds_read_b64_tr_b16 v[244:245], v89 offset:28672\n" \
  "ds_read_b64_tr_b16 v[246:247], v89 offset:30720\n" \
  "ds_read_b64_tr_b16 v[248:249], v89 offset:28704\n" \
  "ds_read_b64_tr_b16 v[250:251], v89 offset:30752\n" \
  "ds_read_b64_tr_b16 v[252:253], v89 offset:28736\n" \
  "ds_read_b64_tr_b16 v[254:255], v89 offset:30784\n" \
  "s_waitcnt lgkmcnt(0)\n" \
  "s_mov_b32 s57, 1048576\n" \
  "s_mov_b32 s56, 0\n" \
  "s_mov_b32 s61, 0\n" \
  "s_mov_b32 s62, 0\n" \
  "s_barrier\n" \
  "LSEG_%=:\n" \
  "s_cmp_lg_u32 s61, 0\n" \
  "s_cbranch_scc1 LPRE_%=\n" \
  "s_waitcnt vmcnt(0)\n" \
  "s_min_u32 s58, s56, 64\n" \
  "v_mov_b32 v93, s58\n" \
  "buffer_store_dword v93, v92, s[4:7], 0 offen sc1\n" \
  "s_add_i32 s60, s56, 1\n" \
  "LSo_%=:\n" \
  "buffer_load_dword v80, v85, s[4:7], 0 offen sc1\n" \
  "s_waitcnt vmcnt(0)\n" \
  "v_cmp_gt_u32 vcc, s60, v80\n" \
  "s_cbranch_vccz LSox_%=\n" \
  "s_cmp_eq_u32 s57, 0\n" \
  "s_cbranch_scc1 LSox_%=\n" \
  "s_sub_u32 s57, s57, 1\n" \
  "s_sleep 2\n" \
  "s_branch LSo_%=\n" \
  "LSox_%=:\n" \
  "s_and_b32 s64, s56, 63\n" \
  "s_lshl_b32 s64, s64, 18\n" \
  "buffer_load_dwordx4 v[0:3], v84, s[8:11], s64 offen offset:0 sc1\n" \
  "buffer_load_dwordx4 v[4:7], v84, s[8:11], s64 offen offset:1024 sc1\n" \
  "buffer_load_dwordx4 v[8:11], v84, s[8:11], s64 offen offset:2048 sc1\n" \
  "buffer_load_dwordx4 v[12:15], v84, s[8:11], s64 offen offset:3072 sc1\n" \
  "s_add_i32 s64, s64, 4096\n" \
  "buffer_load_dwordx4 v[16:19], v84, s[8:11], s64 offen offset:0 sc1\n" \
  "buffer_load_dwordx4 v[20:23], v84, s[8:11], s64 offen offset:1024 sc1\n" \
  "buffer_load_dwordx4 v[24:27], v84, s[8:11], s64 offen offset:2048 sc1\n" \
  "buffer_load_dwordx4 v[28:31], v84, s[8:11], s64 offen offset:3072 sc1\n" \
  "s_add_i32 s64, s64, 4096\n" \
  "buffer_load_dwordx4 v[32:35], v84, s[8:11], s64 offen offset:0 sc1\n" \
  "buffer_load_dwordx4 v[36:39], v84, s[8:11], s64 offen offset:1024 sc1\n" \
  "buffer_load_dwordx4 v[40:43], v84, s[8:11], s64 offen offset:2048 sc1\n" \
  "buffer_load_dwordx4 v[44:47], v84, s[8:11], s64 offen offset:3072 sc1\n" \
  "s_add_i32 s64, s64, 4096\n" \
  "buffer_load_dwordx4 v[48:51], v84, s[8:11], s64 offen offset:0 sc1\n" \
  "buffer_load_dwordx4 v[52:55], v84, s[8:11], s64 offen offset:1024 sc1\n" \
  "buffer_load_dwordx4 v[56:59], v84, s[8:11], s64 offen offset:2048 sc1\n" \
  "buffer_load_dwordx4 v[60:63], v84, s[8:11], s64 offen offset:3072 sc1\n" \
  "LPRE_%=:\n" \
  "s_mov_b32 s65, s61\n" \
  "s_mov_b32 s63, s64\n" \
  "s_lshr_b32 s59, s56, 6\n" \
  "s_mul_i32 s59, s59, s26\n" \
  "s_and_b32 s60, s56, 63\n" \
  "s_mul_i32 s60, s60, 40960\n" \
  "s_add_i32 s59, s59, s60\n" \
  "s_add_i32 s59, s59, s22\n" \
  "buffer_load_dword v80, v85, s[4:7], 0 offen sc1\n" \
  "s_waitcnt vmcnt(16)\n" \
  "v_mfma_f32_16x16x32_bf16 v[64:67], a[0:3], v[0:3], 0\n" \
  "v_mfma_f32_16x16x32_bf16 v[68:71], a[4:7], v[0:3], 0\n" \
  "v_mfma_f32_16x16x32_bf16 v[72:75], a[8:11], v[0:3], 0\n" \
  "s_add_i32 s63, s63, 4096\n" \
  "buffer_load_dwordx4 v[0:3], v84, s[8:11], s63 offen offset:0 sc1\n" \
  "s_waitcnt vmcnt(16)\n" \
  "v_mfma_f32_16x16x32_bf16 v[64:67], a[12:15], v[4:7], v[64:67]\n" \
  "v_mfma_f32_16x16x32_bf16 v[68:71], a[16:19], v[4:7], v[68:71]\n" \
  "v_mfma_f32_16x16x32_bf16 v[72:75], a[20:23], v[4:7], v[72:75]\n" \
  "buffer_load_dwordx4 v[4:7], v84, s[8:11], s63 offen offset:1024 sc1\n" \
  "s_waitcnt vmcnt(16)\n" \
  "v_mfma_f32_16x16x32_bf16 v[64:67], a[24:27], v[8:11], v[64:67]\n" \
  "v_mfma_f32_16x16x32_bf16 v[68:71], a[28:31], v[8:11], v[68:71]\n" \
  "v_mfma_f32_16x16x32_bf16 v[72:75], a[32:35], v[8:11], v[72:75]\n" \
  "buffer_load_dwordx4 v[8:11], v84, s[8:11], s63 offen offset:2048 sc1\n" \
  "s_waitcnt vmcnt(16)\n" \
  "v_mfma_f32_16x16x32_bf16 v[64:67], a[36:39], v[12:15], v[64:67]\n" \
  "v_mfma_f32_16x16x32_bf16 v[68:71], a[40:43], v[12:15], v[68:71]\n" \
  "v_mfma_f32_16x16x32_bf16 v[72:75], a[44:47], v[12:15], v[72:75]\n" \
  "buffer_load_dwordx4 v[12:15], v84, s[8:11], s63 offen offset:3072 sc1\n" \
  "s_cmp_lg_u32 s65, 0\n" \
  "s_cbranch_scc1 LDF_%=\n" \
  "buffer_load_dwordx4 v[76:79], v86, s[0:3], s59 offen sc1\n" \
  "s_branch LNXd_%=\n" \
  "LDF_%=:\n" \
  "s_waitcnt lgkmcnt(0)\n" \
  "s_barrier\n" \
  "s_cmp_gt_u32 s21, 1\n" \
  "s_cbranch_scc1 LSTd_%=\n" \
  "ds_read_b128 v[96:99], v83 offset:0\n" \
  "ds_read_b128 v[100:103], v83 offset:16\n" \
  "ds_read_b128 v[104:107], v83 offset:3328\n" \
  "ds_read_b128 v[108:111], v83 offset:3344\n" \
  "ds_read_b128 v[112:115], v83 offset:6656\n" \
  "ds_read_b128 v[116:119], v83 offset:6672\n" \
  "ds_read_b128 v[120:123], v83 offset:9984\n" \
  "ds_read_b128 v[124:127], v83 offset:10000\n" \
  "s_waitcnt lgkmcnt(4)\n" \
  "v_add_f32 v96, v96, v104\n" \
  "v_add_f32 v97, v97, v105\n" \
  "v_add_f32 v98, v98, v106\n" \
  "v_add_f32 v99, v99, v107\n" \
  "v_add_f32 v100, v100, v108\n" \
  "v_add_f32 v101, v101, v109\n" \
  "v_add_f32 v102, v102, v110\n" \
  "v_add_f32 v103, v103, v111\n" \
  "v_lshlrev_b32 v104, 16, v76\n" \
  "v_and_b32 v105, 0xffff0000, v76\n" \
  "v_lshlrev_b32 v106, 16, v77\n" \
  "v_and_b32 v107, 0xffff0000, v77\n" \
  "v_lshlrev_b32 v108, 16, v78\n" \
  "v_and_b32 v109, 0xffff0000, v78\n" \
  "v_lshlrev_b32 v110, 16, v79\n" \
  "v_and_b32 v111, 0xffff0000, v79\n" \
  "s_waitcnt lgkmcnt(0)\n" \
  "v_pk_add_f32 v[112:113], v[112:113], v[120:121]\n" \
  "v_pk_add_f32 v[114:115], v[114:115], v[122:123]\n" \
  "v_pk_add_f32 v[116:117], v[116:117], v[124:125]\n" \
  "v_pk_add_f32 v[118:119], v[118:119], v[126:127]\n" \
  "v_pk_add_f32 v[96:97], v[96:97], v[112:113]\n" \
  "v_pk_add_f32 v[98:99], v[98:99], v[114:115]\n" \
  "v_pk_add_f32 v[100:101], v[100:101], v[116:117]\n" \
  "v_pk_add_f32 v[102:103], v[102:103], v[118:119]\n" \
  "v_pk_add_f32 v[96:97], v[96:97], v[104:105]\n" \
  "v_pk_add_f32 v[98:99], v[98:99], v[106:107]\n" \
  "v_pk_add_f32 v[100:101], v[100:101], v[108:109]\n" \
  "v_pk_add_f32 v[102:103], v[102:103], v[110:111]\n" \
  "v_cvt_pk_bf16_f32 v96, v96, v97\n" \
  "v_cvt_pk_bf16_f32 v97, v98, v99\n" \
  "v_cvt_pk_bf16_f32 v98, v100, v101\n" \
  "v_cvt_pk_bf16_f32 v99, v102, v103\n" \
  "LSTd_%=:\n" \
  "s_cmp_ge_u32 s67, 64\n" \
  "s_cbranch_scc1 LFOd_%=\n" \
  "s_add_i32 s66, s66, s23\n" \
  "buffer_store_dwordx4 v[96:99], v87, s[0:3], s66 offen sc1\n" \
  "s_branch LNXd_%=\n" \
  "LFOd_%=:\n" \
  "buffer_store_dwordx4 v[96:99], v87, s[16:19], s66 offen\n" \
  "LNXd_%=:\n" \
  "buffer_load_dwordx4 v[76:79], v86, s[0:3], s59 offen sc1\n" \
  "s_waitcnt vmcnt(18)\n" \
  "v_mfma_f32_16x16x32_bf16 v[64:67], a[48:51], v[16:19], v[64:67]\n" \
  "v_mfma_f32_16x16x32_bf16 v[68:71], a[52:55], v[16:19], v[68:71]\n" \
  "v_mfma_f32_16x16x32_bf16 v[72:75], a[56:59], v[16:19], v[72:75]\n" \
  "s_add_i32 s63, s63, 4096\n" \
  "buffer_load_dwordx4 v[16:19], v84, s[8:11], s63 offen offset:0 sc1\n" \
  "s_waitcnt vmcnt(18)\n" \
  "v_mfma_f32_16x16x32_bf16 v[64:67], a[60:63], v[20:23], v[64:67]\n" \
  "v_mfma_f32_16x16x32_bf16 v[68:71], a[64:67], v[20:23], v[68:71]\n" \
  "v_mfma_f32_16x16x32_bf16 v[72:75], a[68:71], v[20:23], v[72:75]\n" \
  "buffer_load_dwordx4 v[20:23], v84, s[8:11], s63 offen offset:1024 sc1\n" \
  "s_waitcnt vmcnt(18)\n" \
  "v_mfma_f32_16x16x32_bf16 v[64:67], a[72:75], v[24:27], v[64:67]\n" \
  "v_mfma_f32_16x16x32_bf16 v[68:71], a[76:79], v[24:27], v[68:71]\n" \
  "v_mfma_f32_16x16x32_bf16 v[72:75], a[80:83], v[24:27], v[72:75]\n" \
  "buffer_load_dwordx4 v[24:27], v84, s[8:11], s63 offen offset:2048 sc1\n" \
  "s_waitcnt vmcnt(18)\n" \
  "v_mfma_f32_16x16x32_bf16 v[64:67], a[84:87], v[28:31], v[64:67]\n" \
  "v_mfma_f32_16x16x32_bf16 v[68:71], a[88:91], v[28:31], v[68:71]\n" \
  "v_mfma_f32_16x16x32_bf16 v[72:75], a[92:95], v[28:31], v[72:75]\n" \
  "buffer_load_dwordx4 v[28:31], v84, s[8:11], s63 offen offset:3072 sc1\n" \
  "s_waitcnt vmcnt(18)\n" \
  "v_mfma_f32_16x16x32_bf16 v[64:67], a[96:99], v[32:35], v[64:67]\n" \
  "v_mfma_f32_16x16x32_bf16 v[68:71], a[100:103], v[32:35], v[68:71]\n" \
  "v_mfma_f32_16x16x32_bf16 v[72:75], a[104:107], v[32:35], v[72:75]\n" \
  "s_add_i32 s63, s63, 4096\n" \
  "buffer_load_dwordx4 v[32:35], v84, s[8:11], s63 offen offset:0 sc1\n" \
  "s_waitcnt vmcnt(18)\n" \
  "v_mfma_f32_16x16x32_bf16 v[64:67], a[108:111], v[36:39], v[64:67]\n" \
  "v_mfma_f32_16x16x32_bf16 v[68:71], a[112:115], v[36:39], v[68:71]\n" \
  "v_mfma_f32_16x16x32_bf16 v[72:75], a[116:119], v[36:39], v[72:75]\n" \
  "buffer_load_dwordx4 v[36:39], v84, s[8:11], s63 offen offset:1024 sc1\n" \
  "s_waitcnt vmcnt(18)\n" \
  "v_mfma_f32_16x16x32_bf16 v[64:67], a[120:123], v[40:43], v[64:67]\n" \
  "v_mfma_f32_16x16x32_bf16 v[68:71], a[124:127], v[40:43], v[68:71]\n" \
  "v_mfma_f32_16x16x32_bf16 v[72:75], a[128:131], v[40:43], v[72:75]\n" \
  "buffer_load_dwordx4 v[40:43], v84, s[8:11], s63 offen offset:2048 sc1\n" \
  "s_waitcnt vmcnt(18)\n" \
  "v_mfma_f32_16x16x32_bf16 v[64:67], a[132:135], v[44:47], v[64:67]\n" \
  "v_mfma_f32_16x16x32_bf16 v[68:71], a[136:139], v[44:47], v[68:71]\n" \
  "v_mfma_f32_16x16x32_bf16 v[72:75], a[140:143], v[44:47], v[72:75]\n" \
  "buffer_load_dwordx4 v[44:47], v84, s[8:11], s63 offen offset:3072 sc1\n" \
  "s_waitcnt vmcnt(18)\n" \
  "v_mfma_f32_16x16x32_bf16 v[64:67], a[144:147], v[48:51], v[64:67]\n" \
  "v_mfma_f32_16x16x32_bf16 v[68:71], a[148:151], v[48:51], v[68:71]\n" \
  "v_mfma_f32_16x16x32_bf16 v[72:75], a[152:155], v[48:51], v[72:75]\n" \
  "s_add_i32 s63, s63, 4096\n" \
  "buffer_load_dwordx4 v[48:51], v84, s[8:11], s63 offen offset:0 sc1\n" \
  "s_waitcnt vmcnt(18)\n" \
  "v_mfma_f32_16x16x32_bf16 v[64:67], a[156:159], v[52:55], v[64:67]\n" \
  "v_mfma_f32_16x16x32_bf16 v[68:71], a[160:163], v[52:55], v[68:71]\n" \
  "v_mfma_f32_16x16x32_bf16 v[72:75], a[164:167], v[52:55], v[72:75]\n" \
  "buffer_load_dwordx4 v[52:55], v84, s[8:11], s63 offen offset:1024 sc1\n" \
  "s_waitcnt vmcnt(18)\n" \
  "v_mfma_f32_16x16x32_bf16 v[64:67], a[168:171], v[56:59], v[64:67]\n" \
  "v_mfma_f32_16x16x32_bf16 v[68:71], a[172:175], v[56:59], v[68:71]\n" \
  "v_mfma_f32_16x16x32_bf16 v[72:75], a[176:179], v[56:59], v[72:75]\n" \
  "buffer_load_dwordx4 v[56:59], v84, s[8:11], s63 offen offset:2048 sc1\n" \
  "s_waitcnt vmcnt(18)\n" \
  "v_mfma_f32_16x16x32_bf16 v[64:67], a[180:183], v[60:63], v[64:67]\n" \
  "v_mfma_f32_16x16x32_bf16 v[68:71], a[184:187], v[60:63], v[68:71]\n" \
  "v_mfma_f32_16x16x32_bf16 v[72:75], a[188:191], v[60:63], v[72:75]\n" \
  "buffer_load_dwordx4 v[60:63], v84, s[8:11], s63 offen offset:3072 sc1\n" \
  "s_waitcnt vmcnt(17)\n" \
  "v_mfma_f32_16x16x32_bf16 v[64:67], a[192:195], v[0:3], v[64:67]\n" \
  "v_mfma_f32_16x16x32_bf16 v[68:71], a[196:199], v[0:3], v[68:71]\n" \
  "v_mfma_f32_16x16x32_bf16 v[72:75], a[200:203], v[0:3], v[72:75]\n" \
  "s_add_i32 s58, s56, 2\n" \
  "v_cmp_gt_u32 vcc, s58, v80\n" \
  "s_add_i32 s64, s56, 1\n" \
  "s_and_b32 s64, s64, 63\n" \
  "s_lshl_b32 s64, s64, 18\n" \
  "s_cmp_eq_u64 vcc, 0\n" \
  "s_cselect_b32 s61, 1, 0\n" \
  "s_cselect_b32 s58, 0, 0x7fff0000\n" \
  "v_or_b32 v81, s58, v84\n" \
  "buffer_load_dwordx4 v[0:3], v81, s[8:11], s64 offen offset:0 sc1\n" \
  "s_waitcnt vmcnt(17)\n" \
  "v_mfma_f32_16x16x32_bf16 v[64:67], a[204:207], v[4:7], v[64:67]\n" \
  "v_mfma_f32_16x16x32_bf16 v[68:71], a[208:211], v[4:7], v[68:71]\n" \
  "v_mfma_f32_16x16x32_bf16 v[72:75], a[212:215], v[4:7], v[72:75]\n" \
  "buffer_load_dwordx4 v[4:7], v81, s[8:11], s64 offen offset:1024 sc1\n" \
  "s_waitcnt vmcnt(17)\n" \
  "v_mfma_f32_16x16x32_bf16 v[64:67], a[216:219], v[8:11], v[64:67]\n" \
  "v_mfma_f32_16x16x32_bf16 v[68:71], a[220:223], v[8:11], v[68:71]\n" \
  "v_mfma_f32_16x16x32_bf16 v[72:75], a[224:227], v[8:11], v[72:75]\n" \
  "buffer_load_dwordx4 v[8:11], v81, s[8:11], s64 offen offset:2048 sc1\n" \
  "s_waitcnt vmcnt(17)\n" \
  "v_mfma_f32_16x16x32_bf16 v[64:67], a[228:231], v[12:15], v[64:67]\n" \
  "v_mfma_f32_16x16x32_bf16 v[68:71], a[232:235], v[12:15], v[68:71]\n" \
  "v_mfma_f32_16x16x32_bf16 v[72:75], a[236:239], v[12:15], v[72:75]\n" \
  "buffer_load_dwordx4 v[12:15], v81, s[8:11], s64 offen offset:3072 sc1\n" \
  "s_waitcnt vmcnt(15)\n" \
  "v_mfma_f32_16x16x32_bf16 v[64:67], a[240:243], v[16:19], v[64:67]\n" \
  "v_mfma_f32_16x16x32_bf16 v[68:71], a[244:247], v[16:19], v[68:71]\n" \
  "v_mfma_f32_16x16x32_bf16 v[72:75], a[248:251], v[16:19], v[72:75]\n" \
  "s_min_u32 s58, s56, 64\n" \
  "v_mov_b32 v93, s58\n" \
  "buffer_store_dword v93, v92, s[4:7], 0 offen sc1\n" \
  "s_add_i32 s64, s64, 4096\n" \
  "buffer_load_dwordx4 v[16:19], v81, s[8:11], s64 offen offset:0 sc1\n" \
  "s_waitcnt vmcnt(16)\n" \
  "v_mfma_f32_16x16x32_bf16 v[64:67], a[252:255], v[20:23], v[64:67]\n" \
  "v_mfma_f32_16x16x32_bf16 v[68:71], v[128:131], v[20:23], v[68:71]\n" \
  "v_mfma_f32_16x16x32_bf16 v[72:75], v[132:135], v[20:23], v[72:75]\n" \
  "buffer_load_dwordx4 v[20:23], v81, s[8:11], s64 offen offset:1024 sc1\n" \
  "s_waitcnt vmcnt(16)\n" \
  "v_mfma_f32_16x16x32_bf16 v[64:67], v[136:139], v[24:27], v[64:67]\n" \
  "v_mfma_f32_16x16x32_bf16 v[68:71], v[140:143], v[24:27], v[68:71]\n" \
  "v_mfma_f32_16x16x32_bf16 v[72:75], v[144:147], v[24:27], v[72:75]\n" \
  "buffer_load_dwordx4 v[24:27], v81, s[8:11], s64 offen offset:2048 sc1\n" \
  "s_waitcnt vmcnt(16)\n" \
  "v_mfma_f32_16x16x32_bf16 v[64:67], v[148:151], v[28:31], v[64:67]\n" \
  "v_mfma_f32_16x16x32_bf16 v[68:71], v[152:155], v[28:31], v[68:71]\n" \
  "v_mfma_f32_16x16x32_bf16 v[72:75], v[156:159], v[28:31], v[72:75]\n" \
  "buffer_load_dwordx4 v[28:31], v81, s[8:11], s64 offen offset:3072 sc1\n" \
  "s_waitcnt vmcnt(16)\n" \
  "v_mfma_f32_16x16x32_bf16 v[64:67], v[160:163], v[32:35], v[64:67]\n" \
  "v_mfma_f32_16x16x32_bf16 v[68:71], v[164:167], v[32:35], v[68:71]\n" \
  "v_mfma_f32_16x16x32_bf16 v[72:75], v[168:171], v[32:35], v[72:75]\n" \
  "s_add_i32 s64, s64, 4096\n" \
  "buffer_load_dwordx4 v[32:35], v81, s[8:11], s64 offen offset:0 sc1\n" \
  "s_waitcnt vmcnt(16)\n" \
  "v_mfma_f32_16x16x32_bf16 v[64:67], v[172:175], v[36:39], v[64:67]\n" \
  "v_mfma_f32_16x16x32_bf16 v[68:71], v[176:179], v[36:39], v[68:71]\n" \
  "v_mfma_f32_16x16x32_bf16 v[72:75], v[180:183], v[36:39], v[72:75]\n" \
  "buffer_load_dwordx4 v[36:39], v81, s[8:11], s64 offen offset:1024 sc1\n" \
  "s_waitcnt vmcnt(16)\n" \
  "v_mfma_f32_16x16x32_bf16 v[64:67], v[184:187], v[40:43], v[64:67]\n" \
  "v_mfma_f32_16x16x32_bf16 v[68:71], v[188:191], v[40:43], v[68:71]\n" \
  "v_mfma_f32_16x16x32_bf16 v[72:75], v[192:195], v[40:43], v[72:75]\n" \
  "buffer_load_dwordx4 v[40:43], v81, s[8:11], s64 offen offset:2048 sc1\n" \
  "s_waitcnt vmcnt(16)\n" \
  "v_mfma_f32_16x16x32_bf16 v[64:67], v[196:199], v[44:47], v[64:67]\n" \
  "v_mfma_f32_16x16x32_bf16 v[68:71], v[200:203], v[44:47], v[68:71]\n" \
  "v_mfma_f32_16x16x32_bf16 v[72:75], v[204:207], v[44:47], v[72:75]\n" \
  "buffer_load_dwordx4 v[44:47], v81, s[8:11], s64 offen offset:3072 sc1\n" \
  "s_waitcnt vmcnt(16)\n" \
  "v_mfma_f32_16x16x32_bf16 v[64:67], v[208:211], v[48:51], v[64:67]\n" \
  "v_mfma_f32_16x16x32_bf16 v[68:71], v[212:215], v[48:51], v[68:71]\n" \
  "v_mfma_f32_16x16x32_bf16 v[72:75], v[216:219], v[48:51], v[72:75]\n" \
  "s_add_i32 s64, s64, 4096\n" \
  "buffer_load_dwordx4 v[48:51], v81, s[8:11], s64 offen offset:0 sc1\n" \
  "s_waitcnt vmcnt(16)\n" \
  "v_mfma_f32_16x16x32_bf16 v[64:67], v[220:223], v[52:55], v[64:67]\n" \
  "v_mfma_f32_16x16x32_bf16 v[68:71], v[224:227], v[52:55], v[68:71]\n" \
  "v_mfma_f32_16x16x32_bf16 v[72:75], v[228:231], v[52:55], v[72:75]\n" \
  "buffer_load_dwordx4 v[52:55], v81, s[8:11], s64 offen offset:1024 sc1\n" \
  "s_waitcnt vmcnt(16)\n" \
  "v_mfma_f32_16x16x32_bf16 v[64:67], v[232:235], v[56:59], v[64:67]\n" \
  "v_mfma_f32_16x16x32_bf16 v[68:71], v[236:239], v[56:59], v[68:71]\n" \
  "v_mfma_f32_16x16x32_bf16 v[72:75], v[240:243], v[56:59], v[72:75]\n" \
  "buffer_load_dwordx4 v[56:59], v81, s[8:11], s64 offen offset:2048 sc1\n" \
  "s_waitcnt vmcnt(16)\n" \
  "v_mfma_f32_16x16x32_bf16 v[64:67], v[244:247], v[60:63], v[64:67]\n" \
  "v_mfma_f32_16x16x32_bf16 v[68:71], v[248:251], v[60:63], v[68:71]\n" \
  "v_mfma_f32_16x16x32_bf16 v[72:75], v[252:255], v[60:63], v[72:75]\n" \
  "buffer_load_dwordx4 v[60:63], v81, s[8:11], s64 offen offset:3072 sc1\n" \
  "v_add_u32 v82, s62, v90\n" \
  "v_add_u32 v83, s62, v91\n" \
  "s_xor_b32 s62, s62, 16384\n" \
  "ds_write_b128 v82, v[64:67] offset:0\n" \
  "ds_write_b128 v82, v[68:71] offset:64\n" \
  "s_nop 1\n" \
  "ds_write_b128 v82, v[72:75] offset:128\n" \
  "s_mov_b32 s66, s60\n" \
  "s_mov_b32 s67, s56\n" \
  "s_add_i32 s56, s56, 1\n" \
  "s_cmp_lg_u32 s61, 0\n" \
  "s_cbranch_scc1 LPRE_%=\n" \
  "s_waitcnt lgkmcnt(0)\n" \
  "s_barrier\n" \
  "s_cmp_gt_u32 s21, 1\n" \
  "s_cbranch_scc1 LSTi_%=\n" \
  "ds_read_b128 v[96:99], v83 offset:0\n" \
  "ds_read_b128 v[100:103], v83 offset:16\n" \
  "ds_read_b128 v[104:107], v83 offset:3328\n" \
  "ds_read_b128 v[108:111], v83 offset:3344\n" \
  "ds_read_b128 v[112:115], v83 offset:6656\n" \
  "ds_read_b128 v[116:119], v83 offset:6672\n" \
  "ds_read_b128 v[120:123], v83 offset:9984\n" \
  "ds_read_b128 v[124:127], v83 offset:10000\n" \
  "s_waitcnt lgkmcnt(4)\n" \
  "v_add_f32 v96, v96, v104\n" \
  "v_add_f32 v97, v97, v105\n" \
  "v_add_f32 v98, v98, v106\n" \
  "v_add_f32 v99, v99, v107\n" \
  "v_add_f32 v100, v100, v108\n" \
  "v_add_f32 v101, v101, v109\n" \
  "v_add_f32 v102, v102, v110\n" \
  "v_add_f32 v103, v103, v111\n" \
  "v_lshlrev_b32 v104, 16, v76\n" \
  "v_and_b32 v105, 0xffff0000, v76\n" \
  "v_lshlrev_b32 v106, 16, v77\n" \
  "v_and_b32 v107, 0xffff0000, v77\n" \
  "v_lshlrev_b32 v108, 16, v78\n" \
  "v_and_b32 v109, 0xffff0000, v78\n" \
  "v_lshlrev_b32 v110, 16, v79\n" \
  "v_and_b32 v111, 0xffff0000, v79\n" \
  "s_waitcnt lgkmcnt(0)\n" \
  "v_pk_add_f32 v[112:113], v[112:113], v[120:121]\n" \
  "v_pk_add_f32 v[114:115], v[114:115], v[122:123]\n" \
  "v_pk_add_f32 v[116:117], v[116:117], v[124:125]\n" \
  "v_pk_add_f32 v[118:119], v[118:119], v[126:127]\n" \
  "v_pk_add_f32 v[96:97], v[96:97], v[112:113]\n" \
  "v_pk_add_f32 v[98:99], v[98:99], v[114:115]\n" \
  "v_pk_add_f32 v[100:101], v[100:101], v[116:117]\n" \
  "v_pk_add_f32 v[102:103], v[102:103], v[118:119]\n" \
  "v_pk_add_f32 v[96:97], v[96:97], v[104:105]\n" \
  "v_pk_add_f32 v[98:99], v[98:99], v[106:107]\n" \
  "v_pk_add_f32 v[100:101], v[100:101], v[108:109]\n" \
  "v_pk_add_f32 v[102:103], v[102:103], v[110:111]\n" \
  "v_cvt_pk_bf16_f32 v96, v96, v97\n" \
  "v_cvt_pk_bf16_f32 v97, v98, v99\n" \
  "v_cvt_pk_bf16_f32 v98, v100, v101\n" \
  "v_cvt_pk_bf16_f32 v99, v102, v103\n" \
  "LSTi_%=:\n" \
  "s_cmp_ge_u32 s67, 64\n" \
  "s_cbranch_scc1 LFOi_%=\n" \
  "s_add_i32 s66, s66, s23\n" \
  "buffer_store_dwordx4 v[96:99], v87, s[0:3], s66 offen sc1\n" \
  "s_branch LNXi_%=\n" \
  "LFOi_%=:\n" \
  "buffer_store_dwordx4 v[96:99], v87, s[16:19], s66 offen\n" \
  "LNXi_%=:\n" \
  "s_cmp_lt_u32 s56, s25\n" \
  "s_cbranch_scc1 LSEG_%=\n" \
  "s_waitcnt vmcnt(0)\n" \
  "s_min_u32 s58, s56, 64\n" \
  "v_mov_b32 v93, s58\n" \
  "buffer_store_dword v93, v92, s[4:7], 0 offen sc1\n" \
  "s_endpgm\n"

constexpr u32 S_LAYER = 12u << 20, S_X2 = 0, S_XB = 0x280000, S_QA = 0x500000, S_OA = 0x780000, S_KV = 0xA00000, S_ST = 0xA40000, S_SIZE = 24u << 20;
constexpr float SC_A = 0.22360679774997896f * 1.4426950408889634f;
constexpr u32 XBYTES = 2048 * 1280;

DI bool wait_words(rsrc_t rf, u32 addr, u32 need, u32& budget) {
#pragma nounroll
  for (;;) {
    asm volatile("" ::: "memory");
    if (!anyb(ld32(rf, addr) < need) || budget == 0) break;
    --budget;
    __builtin_amdgcn_s_sleep(2);
  }
  return true;
}
DI void set_word(rsrc_t rf, u32 addr, u32 val) { WAIT_VM0(); st32(val, rf, (threadIdx.x & 63) == 0 ? addr : OOB); }
constexpr int NG2 = 35;
constexpr u32 RL_SEEN = 143360;
DI void peek(u4 d, u32 off, int w) { asm volatile("s_mov_b32 m0, %2\n\ts_nop 0\n\tbuffer_load_dword %0, %1, 0 offen sc1 lds" ::"v"(off), "s"(d), "s"(RL_SEEN + w * 256) : "memory"); }
DI u32 peeked() { WAIT_VM0(); return lds_ld<u32>(RL_SEEN + threadIdx.x * 4); }
DI void put_word(rsrc_t rf, u32 addr, u32 val) { st32(val, rf, (threadIdx.x & 63) == 0 ? addr : OOB); }
DI float dot2(u32 a, u32 b, float c) { return __builtin_amdgcn_fdot2_f32_bf16(__builtin_bit_cast(bf2, a), __builtin_bit_cast(bf2, b), c, false); }
DI float xor32add(float v) { auto r = __builtin_amdgcn_permlane32_swap(__float_as_uint(v), __float_as_uint(v), false, false); return __uint_as_float(r[0]) + __uint_as_float(r[1]); }
DI float xor16add(float v) { auto r = __builtin_amdgcn_permlane16_swap(__float_as_uint(v), __float_as_uint(v), false, false); return __uint_as_float(r[0]) + __uint_as_float(r[1]); }
template <int CTRL> DI float dppf(float v) { return __uint_as_float((u32)__builtin_amdgcn_update_dpp(0, (int)__float_as_uint(v), CTRL, 0xf, 0xf, true)); }
DI f8 unpack8(u4 v) { f8 r;
#pragma unroll
  for (int i = 0; i < 4; ++i) { r[2 * i] = __uint_as_float(v[i] << 16); r[2 * i + 1] = __uint_as_float(v[i] & 0xffff0000u); } return r; }
DI u4 pack8(f8 v) { return u4{pk(v[0], v[1]), pk(v[2], v[3]), pk(v[4], v[5]), pk(v[6], v[7])}; }

DI u32 row_piece(int i, int w, int lane) { const int p = i * 64 + lane; return (p / 20) * 1280 + ((p % 20) ^ (-((p / 20) >> 2) & 3)) * 16 + w * 320; }
template <int NH> DI void stage_rows(u4 desc, u32 soff, int w, int lane, u32 slab) {
#pragma unroll
  for (int i = 0; i < 5 * NH; ++i) dma16(desc, row_piece(i, w, lane), soff, slab + i * 1024);
}
template <class ROW> DI void load_w128(u4 (&W)[8][5], const u16* wm, int col0, int w, int c, int g, ROW row) {
  rsrc_t rw = mkrs(wm, 640 * 1280);
#pragma unroll
  for (int ks = 0; ks < 5; ++ks) {
    u4 r[8];
#pragma unroll
    for (int j = 0; j < 8; ++j) r[j] = __builtin_amdgcn_raw_buffer_load_b128(rw, row(w * 160 + ks * 32 + g * 8 + j) * 1280 + (col0 + 8 * c) * 2, 0, 0);
#pragma unroll
    for (int u = 0; u < 8; ++u)
#pragma unroll
      for (int m = 0; m < 4; ++m) W[u][ks][m] = __builtin_amdgcn_perm(r[2 * m + 1][u >> 1], r[2 * m][u >> 1], (u & 1) ? 0x07060302 : 0x05040100);
  }
}
constexpr u32 RL_RED = 40960, RL_TS = 528, RL_RES = 81920;
template <int NH> DI void red_write(const f4 (&acc)[NH][8], int w, int c, int g) {
#pragma unroll
  for (int h = 0; h < NH; ++h)
#pragma unroll
    for (int j = 0; j < 4; ++j) {
      const u32 a = RL_RED + (w * NH * 16 + h * 16 + c) * RL_TS + (4 * g + j) * 32;
      lds_st<f4>(a, f4{acc[h][0][j], acc[h][1][j], acc[h][2][j], acc[h][3][j]});
      lds_st<f4>(a + 16, f4{acc[h][4][j], acc[h][5][j], acc[h][6][j], acc[h][7][j]});
    }
}
template <int NH> DI f8 red_read(int tok, int ch) {
  f4 lo = {0, 0, 0, 0}, hi = lo;
#pragma unroll
  for (int k = 0; k < 4; ++k) { const u32 a = RL_RED + (k * NH * 16 + tok) * RL_TS + ch * 32; lo += lds_ld<f4>(a); hi += lds_ld<f4>(a + 16); }
  return __builtin_shufflevector(lo, hi, 0, 1, 2, 3, 4, 5, 6, 7);
}

DI void role_g3(int cg, int rh, const u16* wo, const u16* x0, char* sm, char* flg, int n1) {
  const int tid = threadIdx.x, lane = tid & 63, w = __builtin_amdgcn_readfirstlane(tid >> 6), c = lane & 15, g = lane >> 4;
  u4 W[8][5];
  load_w128(W, wo, cg * 128, w, c, g, [](int k) { return k < 512 ? ((k >> 3) & 31) * 20 + (k >> 8) * 10 + (k & 7) : (((k - 512) >> 1) & 31) * 20 + ((k - 512) >> 6) * 10 + 8 + (k & 1); });
  const rsrc_t rf = mkrs(flg, F_LIM), rs = mkrs(sm, S_SIZE);
  const u4 ds = mkdesc(sm, S_SIZE);
  const int tok = 4 * w + (lane & 3), ch = lane >> 2;
  const u32 myf = F_XB + (cg * 2 + rh) * 128 + w * 4;
  const u4 df = mkdesc(flg, F_LIM), dx = mkdesc(x0, XBYTES);
  auto faddr = [&](int n) { return F_OA + (((n & 63) * 32 + rh * 16 + c) % NG2) * 128; };
  auto fneed = [&](int n) { return (u32)((n & 64) + ((n & 63) * 32 + rh * 16 + c) / NG2 + 1); };
  auto fetch = [&](int n) {
    const int L = n >> 6, s = n & 63;
    const u32 slab = ((n & 1) * 4 + w) * 5120;
    stage_rows<1>(ds, L * S_LAYER + S_OA + (s * 32 + rh * 16) * 1280, w, lane, slab);
    dma16(L ? ds : dx, (rh * 16 + tok) * 1280 + (cg * 16 + ch) * 16, S_X2 + s * 40960, RL_RES + ((n & 1) * 4 + w) * 1024);
  };
  u32 budget = 1u << 20, seen = 0;
  bool pre = false;
  for (int n = 0; n < n1; ++n) {
    const int L = n >> 6, s = n & 63;
    if (!pre) {
      set_word(rf, myf, n);
      wait_words(rf, faddr(n), fneed(n), budget);
      fetch(n);
      peek(df, faddr(n + 1), w);
      seen = peeked();
    }
    pre = n + 1 < n1 && !anyb(seen < fneed(n + 1));
    if (pre) { fetch(n + 1); peek(df, faddr(n + 2), w); }
    const u4 res = lds_ld<u4>(RL_RES + ((n & 1) * 4 + w) * 1024 + lane * 16);
    f4 acc[1][8] = {};
#pragma unroll
    for (int ks = 0; ks < 5; ++ks) {
      const u4 b = lds_ld<u4>(((n & 1) * 4 + w) * 5120 + c * 320 + ks * 64 + (g ^ (-(c >> 2) & 3)) * 16);
#pragma unroll
      for (int u = 0; u < 8; ++u) acc[0][u] = mf16x32(W[u][ks], b, acc[0][u]);
    }
    BAR();
    red_write<1>(acc, w, c, g);
    WAIT_LGKM(0); BAR();
    const u4 o = pack8(red_read<1>(tok, ch) + unpack8(res));
    if (pre) { seen = peeked(); put_word(rf, myf, n); }
    st128(o, rs, L * S_LAYER + S_XB + s * 40960 + (cg * 16 + ch) * 512 + (rh * 16 + tok) * 16);
    float s1 = 0, s2 = 0;
#pragma unroll
    for (int i = 0; i < 4; ++i) { s1 = dot2(o[i], 0x3F803F80u, s1); s2 = dot2(o[i], o[i], s2); }
    s1 += dppf<0x124>(s1); s2 += dppf<0x124>(s2); s1 += dppf<0x128>(s1); s2 += dppf<0x128>(s2);
    s1 = xor32add(xor16add(s1)); s2 = xor32add(xor16add(s2));
    st64(u2{__float_as_uint(s1), __float_as_uint(s2)}, rs, lane < 4 ? L * S_LAYER + S_ST + s * 1280 + cg * 256 + (rh * 16 + tok) * 8 : OOB);
  }
  set_word(rf, myf, n1);
}

DI f2 ln_coef(int tok, u32 ts, u32 off) {
  f2 t = {0, 0};
#pragma unroll
  for (int k = 0; k < 4; ++k) t += lds_ld<f2>(RL_RED + (k * 32 + tok) * ts + off);
  const float mu = t[0] * (1.f / 640);
  return f2{mu, __builtin_amdgcn_rsqf(fmaxf(t[1] * (1.f / 640) - mu * mu, 0.f) + 1e-5f)};
}

DI void role_g1q(int cg, const u16* wq, const u16* x0, char* sm, char* flg, int n0, int n1, bool flags) {
  const int tid = threadIdx.x, lane = tid & 63, w = __builtin_amdgcn_readfirstlane(tid >> 6), c = lane & 15, g = lane >> 4;
  const rsrc_t rf = mkrs(flg, F_LIM), rs = mkrs(sm, S_SIZE);
  const u4 ds = mkdesc(sm, S_SIZE), dx = mkdesc(x0, XBYTES);
  if (n0 < 64) stage_rows<2>(dx, n0 * 40960, w, lane, w * 10240);
  u4 W[8][5];
  load_w128(W, wq, cg * 128, w, c, g, [](int k) { return k; });
  f8 cs;
  {
    f4 acc[1][8] = {};
#pragma unroll
    for (int ks = 0; ks < 5; ++ks)
#pragma unroll
      for (int u = 0; u < 8; ++u) acc[0][u] = mf16x32(W[u][ks], u4{0x3F803F80u, 0x3F803F80u, 0x3F803F80u, 0x3F803F80u}, acc[0][u]);
    red_write<1>(acc, w, c, g);
    WAIT_LGKM(0); BAR();
    cs = red_read<1>(0, c);
  }
  const u32 myf = F_QA + cg * 128 + w * 4, fa = F_X2 + (min(lane, 55) >> 1) * 128 + (lane & 1) * 4;
  const u4 df = mkdesc(flg, F_LIM);
  u32 budget = 1u << 20, seen = 0;
  bool pre = n0 < 64;
  WAIT_VM0();
  for (int n = n0; n < n1; ++n) {
    const int L = n >> 6, s = n & 63;
    if (!pre) {
      set_word(rf, myf, n);
      wait_words(rf, fa, s + 1, budget);
      stage_rows<2>(ds, S_X2 + s * 40960, w, lane, w * 10240);
      peek(df, fa, w);
      seen = peeked();
    }
    f4 acc[2][8] = {};
    f2 st[2];
    u4 b[2][5];
#pragma unroll
    for (int h = 0; h < 2; ++h)
#pragma unroll
      for (int ks = 0; ks < 5; ++ks) b[h][ks] = lds_ld<u4>(w * 10240 + (h * 16 + c) * 320 + ks * 64 + (g ^ (-(c >> 2) & 3)) * 16);
    WAIT_LGKM(0);
    pre = n + 1 < n1 && (n + 1 < 64 || !anyb(seen < (u32)(n - 62)));
    const u4 dn = n + 1 < 64 ? dx : ds;
    const u32 so = S_X2 + ((n + 1) & 63) * 40960;
    SB();
#pragma unroll
    for (int h = 0; h < 2; ++h) {
      float s1 = 0, s2 = 0;
#pragma unroll
      for (int ks = 0; ks < 5; ++ks)
#pragma unroll
        for (int u = 0; u < 8; ++u) {
          acc[h][u] = mf16x32(W[u][ks], b[h][ks], acc[h][u]);
          if (u & 1) s2 = dot2(b[h][ks][u >> 1], b[h][ks][u >> 1], s2); else s1 = dot2(b[h][ks][u >> 1], 0x3F803F80u, s1);
          if (u == 7 && pre) dma16(dn, row_piece(h * 5 + ks, w, lane), so, w * 10240 + (h * 5 + ks) * 1024);
          SB();
        }
      st[h] = f2{xor32add(xor16add(s1)), xor32add(xor16add(s2))};
    }
    if (pre) peek(df, fa, w);
    WAIT_LGKM(0); BAR();
    red_write<2>(acc, w, c, g);
    if (g < 2) lds_st<f2>(RL_RED + (w * 32 + g * 16 + c) * RL_TS + 512, g ? st[1] : st[0]);
    WAIT_LGKM(0); BAR();
    if (pre) { seen = peeked(); put_word(rf, myf, n); }
#pragma unroll
    for (int h = 0; h < 2; ++h) {
      const int tok = 8 * w + 4 * h + g;
      const f2 k = ln_coef(tok, RL_TS, 512);
      st128(pack8((red_read<2>(tok, c) - k[0] * cs) * (k[1] * SC_A)), rs, L * S_LAYER + S_QA + (s * 32 + tok) * 1280 + (cg * 16 + c) * 16);
    }
  }
  if (flags) set_word(rf, myf, n1);
}

constexpr u32 KV_W = 5248, KV_V = 2304, KV_S = 4864;
DI void role_g1kv(int rh, const u16* wk, const u16* wv, const u16* x0, char* sm, char* flg, int n0, int n1, bool flags) {
  const int tid = threadIdx.x, lane = tid & 63, w = __builtin_amdgcn_readfirstlane(tid >> 6), c = lane & 15, g = lane >> 4;
  const rsrc_t rf = mkrs(flg, F_LIM), rs = mkrs(sm, S_SIZE);
  const u4 ds = mkdesc(sm, S_SIZE), dx = mkdesc(x0, XBYTES), df = mkdesc(flg, F_LIM);
  if (n0 < 64) stage_rows<1>(dx, n0 * 40960 + rh * 20480, w, lane, w * 5120);
  u4 W[4][5];
  {
    const u4 dk = mkdesc(wk, 25600), dv = mkdesc(wv, 25600);
    const u32 slab = 83968 + w * 14336;
#pragma unroll
    for (int i = 0; i < 7; ++i) { dma16(dk, w * 6400 + i * 1024 + lane * 16, 0, slab + i * 1024); dma16(dv, w * 6400 + i * 1024 + lane * 16, 0, slab + 7168 + i * 1024); }
    WAIT_VM0();
#pragma unroll
    for (int u = 0; u < 4; ++u) {
      const u32 a = slab + (u >> 1) * 7168 + ((u & 1) ? 16 + (c & 3) : c) * 2 + g * 320;
#pragma unroll
      for (int ks = 0; ks < 5; ++ks)
#pragma unroll
        for (int m = 0; m < 4; ++m) W[u][ks][m] = (u32)lds_ld<u16>(a + ks * 1280 + m * 80) | ((u32)lds_ld<u16>(a + ks * 1280 + m * 80 + 40) << 16);
    }
  }
  auto put = [&](u32 rb, const f4 (&acc)[4]) {
    lds_st<f4>(rb + w * KV_W + c * 144 + g * 16, acc[0]); lds_st<f4>(rb + w * KV_W + c * 144 + 64 + g * 16, acc[1]);
    lds_st<f4>(rb + w * KV_W + KV_V + c * 80 + g * 16, acc[2]); lds_st<f4>(rb + w * KV_W + KV_V + (16 + c) * 80 + g * 16, acc[3]);
  };
  const int d = min(lane >> 1, 19), t4 = 8 * (w & 1) + 4 * (lane & 1), key = w == 0 ? (lane >> 1) & 15 : c;
  const u32 aR = w == 0 ? key * 144 + ((lane >> 5) * 2 + (lane & 1)) * 16 : w == 1 ? key * 144 + 64 : KV_V + d * 80 + t4 * 4;
  const int di = d % 10;
  const u32 oo = w == 0 ? (lane >> 5) * 512 + rh * 256 + (lane & 31) * 8 : w == 1 ? (lane < 16 ? 1024 + rh * 128 + lane * 8 : OOB)
                        : lane < 40 ? 1536 + (2 * rh + (w & 1)) * 384 + ((di & 3) + 8 * (di >> 2) + 4 * (d / 10)) * 16 + (lane & 1) * 8 : OOB;
  auto get = [&](u32 a) { return (lds_ld<f4>(a) + lds_ld<f4>(a + KV_W)) + (lds_ld<f4>(a + 2 * KV_W) + lds_ld<f4>(a + 3 * KV_W)); };
  const u4 ones = {0x3F803F80u, 0x3F803F80u, 0x3F803F80u, 0x3F803F80u};
  f4 cs;
  {
    f4 acc[4] = {};
#pragma unroll
    for (int ks = 0; ks < 5; ++ks)
#pragma unroll
      for (int u = 0; u < 4; ++u) acc[u] = u < 2 ? mf16x32(W[u][ks], ones, acc[u]) : mf16x32(ones, W[u][ks], acc[u]);
    const u32 rb = RL_RED + ((n0 + 1) & 1) * 4 * KV_W;
    put(rb, acc);
    WAIT_LGKM(0); BAR();
    cs = get(rb + aR);
  }
  const u32 myf = F_KV + rh * 128 + w * 4, fa = F_X2 + (rh * 14 + min(lane >> 2, 13)) * 128 + (lane & 1) * 4;
  u32 budget = 1u << 20, seen = 0;
  bool pre = n0 < 64;
  for (int n = n0; n < n1; ++n) {
    const int L = n >> 6, s = n & 63;
    if (!pre) {
      set_word(rf, myf, n);
      wait_words(rf, fa, s + 1, budget);
      stage_rows<1>(ds, S_X2 + s * 40960 + rh * 20480, w, lane, w * 5120);
      peek(df, fa, w);
      seen = peeked();
    }
    u4 b[5];
#pragma unroll
    for (int ks = 0; ks < 5; ++ks) b[ks] = lds_ld<u4>(w * 5120 + c * 320 + ks * 64 + (g ^ (-(c >> 2) & 3)) * 16);
    WAIT_LGKM(0);
    pre = n + 1 < n1 && !anyb(seen < (u32)(s + 2));
    if (pre) { stage_rows<1>(ds, S_X2 + (s + 1) * 40960 + rh * 20480, w, lane, w * 5120); peek(df, fa, w); }
    f4 acc[4] = {};
    float s1 = 0, s2 = 0;
#pragma unroll
    for (int ks = 0; ks < 5; ++ks) {
#pragma unroll
      for (int u = 0; u < 4; ++u) acc[u] = u < 2 ? mf16x32(W[u][ks], b[ks], acc[u]) : mf16x32(b[ks], W[u][ks], acc[u]);
#pragma unroll
      for (int i = 0; i < 4; ++i) { s1 = dot2(b[ks][i], 0x3F803F80u, s1); s2 = dot2(b[ks][i], b[ks][i], s2); }
    }
    s1 = xor32add(xor16add(s1)); s2 = xor32add(xor16add(s2));
    const u32 rb = RL_RED + (n & 1) * 4 * KV_W;
    put(rb, acc);
    lds_st<float>(rb + w * KV_W + KV_S + (lane & 31) * 4, (g & 1) ? s2 : s1);
    WAIT_LGKM(0); BAR();
    f4 m1, m2;
    if (w < 2) {
      float a = 0, q = 0;
#pragma unroll
      for (int k = 0; k < 4; ++k) { a += lds_ld<float>(rb + k * KV_W + KV_S + key * 4); q += lds_ld<float>(rb + k * KV_W + KV_S + 64 + key * 4); }
      m1 = f4{a, a, a, a}; m2 = f4{q, q, q, q};
    } else { m1 = get(rb + KV_S + t4 * 4); m2 = get(rb + KV_S + 64 + t4 * 4); }
    f4 x = get(rb + aR);
#pragma unroll
    for (int i = 0; i < 4; ++i) {
      const float mu = m1[i] * (1.f / 640);
      x[i] = (x[i] - mu * cs[i]) * __builtin_amdgcn_rsqf(fmaxf(m2[i] * (1.f / 640) - mu * mu, 0.f) + 1e-5f);
    }
    if (pre) { seen = peeked(); put_word(rf, myf, n); }
    st64(u2{pk(x[0], x[1]), pk(x[2], x[3])}, rs, oo + L * S_LAYER + S_KV + s * 3072);
  }
  if (flags) set_word(rf, myf, max(n1, 64));
}

DI void role_g5(int cg, int rh, const u16* Wo, char* sm, char* oT, char* out, char* flg, int n1) {
  const int tid = threadIdx.x, lane = tid & 63, w = __builtin_amdgcn_readfirstlane(tid >> 6), c = lane & 15, g = lane >> 4;
  const int tk = 8 * (w & 1) + min(lane / 6, 7), ch = lane % 6;
  const bool fin = w < 2 && lane < 48 && cg * 6 + ch < 80;
  const u32 tb[9] = {(u32)(rh * 131072 + w * 32768 + lane * 16), F_OT + (w * 16 + (lane >> 2)) * 128, fin ? (u32)((cg * 6 + ch) * 512 + (rh * 16 + tk) * 16) : OOB,
                     fin ? (u32)((rh * 16 + tk) * 1280 + (cg * 6 + ch) * 16) : OOB, (u32)((w * 1024 + (lane >> 3)) * 1280 + cg * 96 + (lane & 7) * 16),
                     (u32)(w * 32768 + (4 * g + ((lane >> 2) & 3)) * 128 + (lane & 3) * 8), (u32)(w * 3328 + c * 208 + g * 16), (u32)(tk * 208 + ch * 32),
                     w < 2 && lane == 0 ? F_X2 + (rh * 14 + cg) * 128 + w * 4 : OOB};
#pragma unroll
  for (int i = 0; i < 9; ++i) lds_st<u32>(131072 + i * 1024 + tid * 4, tb[i]);
  put_scalars(w, mkdesc(sm, S_SIZE), mkdesc(flg, F_LIM), mkdesc(oT, 16u << 20), mkdesc(Wo, 4096 * 1280), mkdesc(out, XBYTES), w * 256, w, S_XB, S_X2, F_X2 + ((cg * 2 + rh) * 2 + (w & 1)) * 128 + (w >> 1) * 64, n1, S_LAYER);
  RUN_ASM(G5_ASM);
}

DI void role_g2(int t, char* sm, char* flg, int n1) {
  const int tid = threadIdx.x, lane = tid & 63, w = __builtin_amdgcn_readfirstlane(tid >> 6), r = lane & 31, h = lane >> 5;
  const u32 tb[11] = {(u32)(r * 40 + h * 16), h ? OOB : (u32)(r * 40 + 32), (u32)(lane * 16), (u32)(1024 + lane * 8),
                      r < 24 ? (u32)(1536 + h * 384 + r * 16) : OOB, F_QA + (min(lane, 19) >> 2) * 128 + (lane & 3) * 4, F_KV + ((lane >> 2) & 1) * 128 + (lane & 3) * 4,
                      (u32)(lane * 16), (u32)(1024 + lane * 4), (u32)(lane * 16), 0xDDDDu};
#pragma unroll
  for (int i = 0; i < 11; ++i) lds_st<u32>(32768 + i * 1024 + tid * 4, tb[i]);
  WAIT_LGKM(0);
  const u4 dS = mkdesc(sm, S_SIZE), dF = mkdesc(flg, F_LIM);
  put_scalars(w, dS, dF, w * 256, w, S_QA, S_OA, S_KV + w * 3072, F_OA + t * 128, n1, S_LAYER, t);
  RUN_ASM(G2_ASM);
}

DI void role_g4(int ty, int hp, const u16* Wm, char* sm, char* qkv, char* flg, int n1) {
  const int tid = threadIdx.x, lane = tid & 63, w = __builtin_amdgcn_readfirstlane(tid >> 6), r = lane & 31, h = lane >> 5;
  const int g16 = lane >> 4, q = (lane >> 2) & 3, p = lane & 3, head = 2 * hp + (w >> 1);
  const int nf = ty == 2 ? 16 * (g16 & 1) + 4 * p : 8 * (2 * (g16 & 1) + (p & 1)) + 4 * (p >> 1);
  const u32 tb[8] = {(u32)(tid * 16), (u32)(lane * 16), (u32)(h * 768 + r * 8), F_XB + (min(lane, 39) >> 2) * 128 + (lane & 3) * 4, ty == 2 ? (u32)(h * 1024 + r * 16) : (u32)(lane * 16),
                     (u32)((lane >> 3) * 8192 + (hp * 256 + w * 64) * 2 + (lane & 7) * 16), (u32)(w * 32768 + (8 * h + q) * 128 + nf * 2), (u32)(0x14000 + w * 256 + h * 16)};
#pragma unroll
  for (int i = 0; i < 8; ++i) lds_st<u32>(131072 + i * 1024 + tid * 4, tb[i]);
  WAIT_LGKM(0);
  const u4 dS = mkdesc(sm, S_SIZE), dF = mkdesc(flg, F_LIM), dQ = mkdesc(qkv, 48u << 20), dW = mkdesc(Wm, 640 * 8192);
  const u32 sc = ty == 1 ? __float_as_uint(SC_B) : 0x3F800000u;
  put_scalars(w, (dS), (dF), (dQ), (dW), (w * 256), (S_XB), (S_ST), (head * 24576 + ty * 8192 + (w & 1) * 4096),
      (F_QKV + ty * F_TY + head * 256 + (w & 1) * 4), (n1), (S_LAYER), (sc));
  if (ty == 2) RUN_ASM(G4V_ASM);
  else RUN_ASM(G4QK_ASM);
}

struct Args { const u16 *x0, *wq, *wk, *wv, *wo, *mem0, *Wq, *Wk, *Wv, *Wo; char *ring, *sm, *qkv, *oT, *out; Tm tm; int nl, mask; };
constexpr int NQ0 = 19;
constexpr int LDS_BYTES = 144 * 1024;

__global__ void __launch_bounds__(256, 1) k_pre(Args a) {
  const int b = blockIdx.x, tid = threadIdx.x;
  a.ring += -(uintptr_t)a.ring & ((2u << 20) - 1);
  const rsrc_t rr = mkrs(a.ring, 2u << 20);
  if (b < 128) { role_g1kv(b & 1, a.wk, a.wv, a.x0, a.sm, a.ring, b >> 1, (b >> 1) + 1, false); return; }
  if (b < 128 + 5 * NQ0) { const int sg = (b - 128) / 5; role_g1q((b - 128) % 5, a.wq, a.x0, a.sm, a.ring, sg, sg + 1, false); return; }
  if (b == 223) {
    const rsrc_t rs = mkrs(a.sm, S_SIZE);
    for (int i = tid; i < 128 * 32; i += 256) {
      const int t = i >> 5, p = i & 31, r = (p & 1) + 18 + 4 * ((p >> 1) & 1);
      const u32 one = 0x3F803F80u, base = (t >> 6) * S_LAYER + S_KV + (t & 63) * 3072;
      if (p < 16) st128(u4{one, 0x3F80u, one, 0x3F80u}, rs, base + 1280 + p * 16);
      else st128((p & 1) ? u4{0, 0, 0, 0} : u4{one, one, one, one}, rs, base + 1536 + ((p >> 2) & 3) * 384 + r * 16);
    }
    return;
  }
  if (b == 224) {
    if (tid == 0) {
      u32 best[2] = {~0u, ~0u};
#pragma unroll
      for (int r = 0; r < 3; ++r)
#pragma unroll
        for (int h = 0; h < 2; ++h) {
          u32* p = (u32*)(a.ring + R_CTL + 1024 + h * 8192); u32 v, z = 0;
          unsigned long long c0 = ct();
          asm volatile("global_atomic_add %0, %1, %2, off sc0 sc1\n\ts_waitcnt vmcnt(0)" : "=v"(v) : "v"(p), "v"(z) : "memory");
          best[h] = min(best[h], (u32)(ct() - c0));
        }
      u32* ctl = (u32*)(a.ring + R_CTL);
      ctl[0] = best[1] < best[0]; ctl[1] = best[0]; ctl[2] = best[1];
    }
    return;
  }
  if (b == 225) {
    for (u32 p = tid * 128; p < F_SIZE; p += 256 * 128) {
      const u32 o = p & ~8192u;
      u32 v = o >= F_KV ? 64u : (o >= F_QA && o < F_X2) ? (u32)NQ0 : 0u;
      st128(u4{v, v, v, v}, rr, R_PRG + p);
    }
    return;
  }
  if (b < 228) {
    for (int q = tid; q < 2592; q += 256) {
      const int j = q / 96, r = (q % 96) / 12, d = 24 * j + 2 * (q % 12);
      u32 v = d < 640 ? *(const u32*)(a.mem0 + r * 640 + d) : 0u;
      if (v == SENT) v = 0xFFFFFFFFu;
#pragma unroll
      for (int k = 0; k < 4; ++k) st32(v, rr, rmap(R_MEM0 + k * R_MCOPY + q * 4, b & 1));
    }
    return;
  }
  for (u32 o = ((b - 228) * 256 + tid) * 16; o < 4 * R_SLOT + 2 * (R_END - R_FLG); o += 28 * 4096)
    st128(u4{SENT, SENT, SENT, SENT}, rr, o < 4 * R_SLOT ? o : o - 4 * R_SLOT + 2 * R_FLG);
}

__global__ void __launch_bounds__(256, 1) k_main(Args a) {
  const int x = blockIdx.x & 7, r = blockIdx.x >> 3, n1 = a.nl * 64;
  a.ring += -(uintptr_t)a.ring & ((2u << 20) - 1);
  const u32 pol = __builtin_amdgcn_readfirstlane(*(const u32*)(a.ring + R_CTL));
  char* flg = a.ring + R_PRG + (pol ^ 1) * 8192;
  if (x < 4) {
    if (r < 16) { role_x(x * 16 + r, x, a.Wq, a.Wk, a.Wv, a.ring, pol, a.qkv, a.oT, flg, 0, a.nl, a.tm); return; }
    const int j = (r - 16) * 2 + (x & 1);
    if (j < 27) { role_y(x >> 1, j, x, x & 1, a.Wo, a.ring, pol, flg, 0, a.nl, a.tm); return; }
    return;
  }
  const int y = x - 4;
  if (r < 12) { const int i = y * 12 + r, ty = i % 3; role_g4(ty, i / 3, ty == 0 ? a.Wq : ty == 1 ? a.Wk : a.Wv, a.sm, a.qkv, flg, n1); return; }
  if (r < 19) { role_g5((y & 1) * 7 + r - 12, y >> 1, a.Wo, a.sm, a.oT, a.out, flg, n1); return; }
  const int i = (r - 19) * 4 + y;
  if (i < NG2) { role_g2(i, a.sm, flg, n1); }
  else if (i < NG2 + 10) { role_g3((i - NG2) >> 1, i & 1, a.wo, a.x0, a.sm, flg, n1); }
  else if (i < NG2 + 15) { role_g1q(i - NG2 - 10, a.wq, a.x0, a.sm, flg, NQ0, n1, true); }
  else { role_g1kv(i & 1, a.wk, a.wv, a.x0, a.sm, flg, 64, n1, true); }
}

extern "C" void kernel_launch(void* const* d_in, const int* in_sizes, int n_in, void* d_out, int out_size, void* d_ws, size_t ws_size, hipStream_t st) {
  Args a;
  a.x0 = (const u16*)d_in[0]; a.wq = (const u16*)d_in[1]; a.wk = (const u16*)d_in[2]; a.wv = (const u16*)d_in[3]; a.wo = (const u16*)d_in[4];
  a.mem0 = (const u16*)d_in[6]; a.Wq = (const u16*)d_in[7]; a.Wk = (const u16*)d_in[8]; a.Wv = (const u16*)d_in[9]; a.Wo = (const u16*)d_in[10];
  char* w = (char*)d_ws;
  a.ring = w;
  a.sm = w + (8u << 20); a.qkv = w + (32u << 20); a.oT = w + (80u << 20); a.out = (char*)d_out;
  a.tm = Tm{2400, 4300, 500, 0}; a.nl = 2; a.mask = 127;
  (void)hipFuncSetAttribute((const void*)k_pre, hipFuncAttributeMaxDynamicSharedMemorySize, LDS_BYTES);
  (void)hipFuncSetAttribute((const void*)k_main, hipFuncAttributeMaxDynamicSharedMemorySize, LDS_BYTES);
  k_pre<<<256, 256, LDS_BYTES, st>>>(a);
  k_main<<<256, 256, LDS_BYTES, st>>>(a);
}
```
